```python
import torch

def _softmax(x, axis=-1):
    return torch.softmax(x, dim=axis)
def _gelu(x):
    return torch.nn.functional.gelu(x, approximate="tanh")
def _silu(x):
    return torch.nn.functional.silu(x)
def _sigmoid(x):
    return torch.sigmoid(x)
def _layernorm(x, axis=-1, eps=1e-5):
    m = x.mean(dim=axis, keepdim=True)
    v = x.var(dim=axis, keepdim=True, unbiased=False)
    return (x - m) / torch.sqrt(v + eps)
def _scatter_add(tgt, idx, src, axis=0):
    return tgt.clone().index_add_(axis, idx, src)
def _segment_sum(src, seg, n):
    out = torch.zeros((n,) + src.shape[1:], dtype=src.dtype)
    return out.index_add_(0, seg, src)
def _pad(x, pw):
    flat = tuple(v for pair in reversed(pw) for v in pair)
    return torch.nn.functional.pad(x, flat)

kernel_name = 'sequential_preLN__softmax_full_mha__glu_gated_swiglu__prompt'

def setup_inputs():

    _w = (lambda a: (lambda t: t.to(torch.bfloat16) if t.is_floating_point() else t)(torch.as_tensor(a)))
    def _A_setup():
        B, S, D = 1, 2048, 512
        return {
            "x": _w((lambda *s: torch.randn(*s, dtype=torch.bfloat16) * 0.05)(B, S, D)),
            "wqkv": _w((lambda *s: torch.randn(*s, dtype=torch.bfloat16) * 0.05)(D, 3 * D)),
            "wo": _w((lambda *s: torch.randn(*s, dtype=torch.bfloat16) * 0.05)(D, D)),
        }
    _A_in = _A_setup()

    def _B_setup():
        B, S, D, F = 1, 2048, 512, 1376
        return {
            "x": _w((lambda *s: torch.randn(*s, dtype=torch.bfloat16) * 0.05)(B, S, D)),
            "w1": _w((lambda *s: torch.randn(*s, dtype=torch.bfloat16) * 0.4)(D, F) * (2.0 / D) ** 0.5),
            "w3": _w((lambda *s: torch.randn(*s, dtype=torch.bfloat16) * 0.4)(D, F) * (2.0 / D) ** 0.5),
            "w2": _w((lambda *s: torch.randn(*s, dtype=torch.bfloat16) * 0.4)(F, D) * (2.0 / F) ** 0.5),
        }
    _B_in = _B_setup()

    return {
        "A_x": _A_in["x"],
        "A_wqkv": _A_in["wqkv"],
        "A_wo": _A_in["wo"],
        "B_x": _B_in["x"],
        "B_w1": _B_in["w1"],
        "B_w3": _B_in["w3"],
        "B_w2": _B_in["w2"],
    }

def reference(A_x, A_wqkv, A_wo, B_x, B_w1, B_w3, B_w2):
    def _A_ref(x, wqkv, wo):
        B, S, D = x.shape
        H = 32; Dh = D // H
        qkv = (lambda x, *d: x.permute(*d))((x @ wqkv).reshape(B, S, 3, H, Dh), 2, 0, 3, 1, 4)
        q, k, v = qkv[0], qkv[1], qkv[2]
        scale = Dh ** -0.5
        scores = torch.einsum("bhqd,bhkd->bhqk", q, k) * scale
        p = _softmax(scores, axis=-1)
        o = torch.einsum("bhqk,bhkd->bhqd", p, v)
        return (lambda x, *d: x.permute(*d))(o, 0, 2, 1, 3).reshape(B, S, D) @ wo

    def _B_ref(x, w1, w3, w2):
        gate = torch.einsum("bsd,df->bsf", x, w1)
        up = torch.einsum("bsd,df->bsf", x, w3)
        h = _silu(gate) * up
        out = torch.einsum("bsf,fd->bsd", h, w2)
        return out

    x = A_x
    h = x + _A_ref(_layernorm(x, axis=-1), A_wqkv, A_wo)
    h2 = h + _B_ref(_layernorm(h, axis=-1), B_w1, B_w3, B_w2)
    return h2
```

```cpp
#include <hip/hip_runtime.h>
#include <hip/hip_bf16.h>
typedef unsigned short u16;
#define NDUMMY 0
__device__ __forceinline__ float b2f(u16 v) { return __uint_as_float((unsigned)v << 16); }
__device__ __forceinline__ u16 f2b(float f) { unsigned u = __float_as_uint(f); u += 0x7fff + ((u >> 16) & 1); return u >> 16; }
constexpr int S = 2048, D = 512, F = 1376;
__global__ void n_ln(const u16* xb, const float* xf, float* y) {
  __shared__ float r1[256], r2[256];
  int row = blockIdx.x, t = threadIdx.x;
  float a = xb ? b2f(xb[row * D + t]) : xf[row * D + t], b = xb ? b2f(xb[row * D + t + 256]) : xf[row * D + t + 256];
  r1[t] = a + b; __syncthreads();
  for (int s = 128; s; s >>= 1) { if (t < s) r1[t] += r1[t + s]; __syncthreads(); }
  float m = r1[0] / D; r2[t] = (a - m) * (a - m) + (b - m) * (b - m); __syncthreads();
  for (int s = 128; s; s >>= 1) { if (t < s) r2[t] += r2[t + s]; __syncthreads(); }
  float rs = 1.f / sqrtf(r2[0] / D + 1e-5f);
  y[row * D + t] = (a - m) * rs; y[row * D + t + 256] = (b - m) * rs;
}
__global__ void n_gemm(const float* A, const u16* W, float* C, int N, int K) {
  __shared__ float As[16][17], Ws[16][17];
  int tx = threadIdx.x & 15, ty = threadIdx.x >> 4, m = blockIdx.y * 16 + ty, n = blockIdx.x * 16 + tx; float acc = 0;
  for (int k0 = 0; k0 < K; k0 += 16) { As[ty][tx] = A[m * K + k0 + tx]; Ws[ty][tx] = b2f(W[(k0 + ty) * N + n]); __syncthreads();
    for (int k = 0; k < 16; ++k) acc += As[ty][k] * Ws[k][tx]; __syncthreads(); }
  C[m * N + n] = acc;
}
__global__ void n_attn(const float* qkv, float* o) {
  int h = blockIdx.y, q = blockIdx.x * 64 + threadIdx.x; float qv[16], acc[16], m = -1e30f, l = 0;
  for (int d = 0; d < 16; ++d) { qv[d] = qkv[q * 1536 + h * 16 + d] * 0.25f; acc[d] = 0; }
  for (int k = 0; k < S; ++k) { float s = 0; for (int d = 0; d < 16; ++d) s += qv[d] * qkv[k * 1536 + 512 + h * 16 + d];
    float mn = fmaxf(m, s), a = __expf(m - mn), p = __expf(s - mn); l = l * a + p; for (int d = 0; d < 16; ++d) acc[d] = acc[d] * a + p * qkv[k * 1536 + 1024 + h * 16 + d]; m = mn; }
  for (int d = 0; d < 16; ++d) o[q * D + h * 16 + d] = acc[d] / l;
}
__global__ void n_addx(const u16* x, float* h) { int i = blockIdx.x * 256 + threadIdx.x; h[i] += b2f(x[i]); }
__global__ void n_swiglu(float* g, const float* u) { int i = blockIdx.x * 256 + threadIdx.x; float a = g[i]; g[i] = a / (1.f + expf(-a)) * u[i]; }
__global__ void n_out(const float* h, const float* y, u16* o) { int i = blockIdx.x * 256 + threadIdx.x; o[i] = f2b(h[i] + y[i]); }
__global__ void n_dummy(const uint4* x, uint4* out) { int i = blockIdx.x * 512 + threadIdx.x; out[i] = x[i]; }
extern "C" void kernel_launch(void* const* d_in, const int* in_sizes, int n_in, void* d_out, int out_size, void* d_ws, size_t ws_size, hipStream_t st) {
  const u16 *x = (const u16*)d_in[0], *wqkv = (const u16*)d_in[1], *wo = (const u16*)d_in[2], *w1 = (const u16*)d_in[4], *w3 = (const u16*)d_in[5], *w2 = (const u16*)d_in[6];
  float* w = (float*)d_ws; float *xn = w, *qkv = xn + S * D, *o = qkv + S * 1536, *h = o + S * D, *hn = h + S * D, *g = hn + S * D, *u = g + S * F, *y = u + S * F; uint4* dm = (uint4*)(y + S * D);
  n_ln<<<S, 256, 0, st>>>(x, nullptr, xn);
  n_gemm<<<dim3(1536 / 16, S / 16), 256, 0, st>>>(xn, wqkv, qkv, 1536, D);
  n_attn<<<dim3(S / 64, 32), 64, 0, st>>>(qkv, o);
  n_gemm<<<dim3(D / 16, S / 16), 256, 0, st>>>(o, wo, h, D, D);
  n_addx<<<S * D / 256, 256, 0, st>>>(x, h);
  n_ln<<<S, 256, 0, st>>>(nullptr, h, hn);
  n_gemm<<<dim3(F / 16, S / 16), 256, 0, st>>>(hn, w1, g, F, D);
  n_gemm<<<dim3(F / 16, S / 16), 256, 0, st>>>(hn, w3, u, F, D);
  n_swiglu<<<S * F / 256, 256, 0, st>>>(g, u);
  n_gemm<<<dim3(D / 16, S / 16), 256, 0, st>>>(g, w2, y, D, F);
  n_out<<<S * D / 256, 256, 0, st>>>(h, y, (u16*)d_out);
  for (int i = 0; i < NDUMMY; ++i) n_dummy<<<256, 512, 0, st>>>(i ? dm + (i - 1) * 131072 : (const uint4*)x, dm + i * 131072);
}
```

```cpp
#include <hip/hip_runtime.h>

typedef unsigned u32;
typedef unsigned long long u64;
typedef u32 u32x2 __attribute__((ext_vector_type(2)));
typedef u32 u32x4 __attribute__((ext_vector_type(4)));
typedef u32 u32x8 __attribute__((ext_vector_type(8)));
typedef u32 u32x16 __attribute__((ext_vector_type(16)));
typedef float f32x2 __attribute__((ext_vector_type(2)));
typedef float f32x4 __attribute__((ext_vector_type(4)));
typedef float f32x16 __attribute__((ext_vector_type(16)));
typedef short s16x4 __attribute__((ext_vector_type(4)));
typedef __bf16 bf16x2 __attribute__((ext_vector_type(2)));
typedef __bf16 bf16x8 __attribute__((ext_vector_type(8)));
typedef __bf16 bf16x16 __attribute__((ext_vector_type(16)));
typedef __amdgpu_buffer_rsrc_t rsrc_t;
#define LAS __attribute__((address_space(3)))
#define GAS __attribute__((address_space(1)))
#define DI __device__ __forceinline__
#define RLX_AGENT __ATOMIC_RELAXED, __HIP_MEMORY_SCOPE_AGENT
#define SC1 16

constexpr u32 WS_EPOCH = 0;
constexpr u32 WS_FA = 4096, WS_FB = WS_FA + 32768, WS_FC = WS_FB + 32768, WS_FD = WS_FC + 32768;
constexpr u32 WS_FR = WS_FD + 32768;
constexpr u32 WS_STAMP = 262144;
constexpr u32 WS_KV = 1u << 20;
constexpr u32 WS_O = WS_KV + (4u << 20);
constexpr u32 WS_H = WS_O + (2u << 20);
constexpr u32 WS_G = WS_H + (2u << 20);
constexpr u32 WS_WOT = WS_G + 128u * 43u * 1024u;
constexpr u32 WS_W13T = WS_WOT + 512u * 1024u;
constexpr u32 WS_W2T = WS_W13T + 86u * 32u * 1024u;
constexpr u32 WS_ST = WS_W2T + 8u * 43u * 4096u;
constexpr u32 WS_END = WS_ST + 128u * 1024u;
constexpr u32 OOB = 0x7f000000u;

constexpr u32 LA_STAT = 0, LA_CNT = 1024, LA_K = 4096, LA_Q = 8192, LA_X = 16384, LA_W = 81920;
constexpr u32 LQ = 98304;
constexpr u32 NB = 6, VSTRIDE = 1056, RING = NB * VSTRIDE;
constexpr u32 LC_X = LQ + 32768, LC_ST = 38 * 4096, LD_CS = 39 * 4096;

#define STAMP(k) do {} while (0)
#define STV_ARG
#define STV_PASS
#define XSTAMP_A(j) do {} while (0)

DI rsrc_t mk(const void* p, u32 n) { return __builtin_amdgcn_make_buffer_rsrc((void*)p, (short)0, (int)n, 0x00020000); }
DI void dma(rsrc_t rs, u32 voff, u32 soff, u32 dst) {
  asm volatile("s_mov_b32 m0, %3\n\ts_nop 0\n\tbuffer_load_dwordx4 %0, %1, %2 offen lds" :: "v"(voff), "s"(rs), "s"(soff), "s"(dst) : "memory"); }
DI void dma_sc1(rsrc_t rs, u32 voff, u32 soff, u32 dst) {
  asm volatile("s_mov_b32 m0, %3\n\ts_nop 0\n\tbuffer_load_dwordx4 %0, %1, %2 offen sc1 lds" :: "v"(voff), "s"(rs), "s"(soff), "s"(dst) : "memory"); }
#define WAITVM(n) asm volatile("s_waitcnt vmcnt(" #n ")" ::: "memory")
#define WAITVM_BAR(n) asm volatile("s_waitcnt vmcnt(" #n ") lgkmcnt(0)\n\ts_barrier" ::: "memory")
#define BAR() asm volatile("s_waitcnt lgkmcnt(0)\n\ts_barrier" ::: "memory")
#define SCHED() __builtin_amdgcn_sched_barrier(0)
DI u32x4 ld4(rsrc_t rs, u32 voff, u32 soff) { return __builtin_amdgcn_raw_buffer_load_b128(rs, voff, soff, SC1); }
DI void st4(rsrc_t rs, u32x4 v, u32 voff, u32 soff) { __builtin_amdgcn_raw_buffer_store_b128(v, rs, voff, soff, SC1);
  asm volatile("s_nop 1" :: "v"(v) : "memory"); }
DI u32x4 lds4(u32 a) { return *(LAS const u32x4*)a; }
DI void lds4w(u32 a, u32x4 v) { *(LAS u32x4*)a = v; }
DI f32x4 lds4f(u32 a) { return *(LAS const f32x4*)a; }
DI void lds4fw(u32 a, f32x4 v) { *(LAS f32x4*)a = v; }
DI u32x2 ldtr(u32 a) { return __builtin_bit_cast(u32x2, __builtin_amdgcn_ds_read_tr16_b64_v4i16((LAS s16x4*)a)); }
DI u32x4 ldtr2(u32 a, u32 b) { u32x2 lo = ldtr(a), hi = ldtr(b); return u32x4{lo[0], lo[1], hi[0], hi[1]}; }
DI f32x16 mf32(u32x4 a, u32x4 b, f32x16 c) { return __builtin_amdgcn_mfma_f32_32x32x16_bf16(__builtin_bit_cast(bf16x8, a), __builtin_bit_cast(bf16x8, b), c, 0, 0, 0); }
DI f32x4 mf16(u32x4 a, u32x4 b, f32x4 c) { return __builtin_amdgcn_mfma_f32_16x16x32_bf16(__builtin_bit_cast(bf16x8, a), __builtin_bit_cast(bf16x8, b), c, 0, 0, 0); }
DI f32x16 mfr1(float a, float b, f32x16 c) { asm volatile("s_nop 9" : "+v"(a), "+v"(b), "+v"(c)); return __builtin_amdgcn_mfma_f32_32x32x2f32(a, b, c, 0, 0, 0); }
DI f32x4 sum2(u32x4 sel, u32 idx, u32x4 a, u32x4 b, f32x4 c) { const u32x8 t = __builtin_shufflevector(a, b, 0, 1, 2, 3, 4, 5, 6, 7);
  return __builtin_amdgcn_smfmac_f32_16x16x64_bf16(__builtin_bit_cast(bf16x8, sel), __builtin_bit_cast(bf16x16, t), c, idx, 0, 0); }
DI void stat1(u32 v, float& s1, float& s2, float& s3, float& s4) { float a, b;
  asm("v_lshlrev_b32 %0, 16, %6\n\tv_and_b32 %1, 0xffff0000, %6\n\tv_add_f32 %2, %2, %0\n\tv_fmac_f32 %3, %0, %0\n\tv_add_f32 %4, %4, %1\n\tv_fmac_f32 %5, %1, %1"
      : "=&v"(a), "=&v"(b), "+v"(s1), "+v"(s2), "+v"(s3), "+v"(s4) : "v"(v)); }
DI float dot2(u32 a, u32 b, float c) { return __builtin_amdgcn_fdot2_f32_bf16(__builtin_bit_cast(bf16x2, a), __builtin_bit_cast(bf16x2, b), c, false); }
DI float dsum(u32x4 v, float c) { for (int i = 0; i < 4; ++i) c = dot2(v[i], 0x3f803f80u, c); return c; }
DI float dsq(u32x4 v, float c) { for (int i = 0; i < 4; ++i) c = dot2(v[i], v[i], c); return c; }
DI u32 pk2(float a, float b) { return __builtin_bit_cast(u32, __builtin_convertvector((f32x2{a, b}), bf16x2)); }
DI float blo(u32 v) { return __uint_as_float(v << 16); }
DI float bhi(u32 v) { return __uint_as_float(v & 0xffff0000u); }
DI float swap32_add(float v) { auto r = __builtin_amdgcn_permlane32_swap(__float_as_uint(v), __float_as_uint(v), false, false); return __uint_as_float(r[0]) + __uint_as_float(r[1]); }
DI float swap16_add(float v) { auto r = __builtin_amdgcn_permlane16_swap(__float_as_uint(v), __float_as_uint(v), false, false); return __uint_as_float(r[0]) + __uint_as_float(r[1]); }
DI float swap32_max(float v) { auto r = __builtin_amdgcn_permlane32_swap(__float_as_uint(v), __float_as_uint(v), false, false); return fmaxf(__uint_as_float(r[0]), __uint_as_float(r[1])); }
DI float max16(const f32x16& s) { float m = fmaxf(s[0], s[1]); for (int i = 2; i < 16; i += 2) m = fmaxf(fmaxf(m, s[i]), s[i + 1]); return m; }
DI f32x16 splat16(float v) { f32x16 r; for (int i = 0; i < 16; ++i) r[i] = v; return r; }
DI void set_flag(char* ws, u32 off, u64 tag) { __hip_atomic_store((GAS u64*)(ws + off), tag, RLX_AGENT); }
DI bool arrive(u32 a, u32 n, int lane) { u32 o = 0; if (lane == 0) o = __hip_atomic_fetch_add((LAS u32*)a, 1u, __ATOMIC_RELAXED, __HIP_MEMORY_SCOPE_WORKGROUP); return lane == 0 && o == n - 1; }
DI void poll(const char* ws, u32 off, u64 tag) {
  const GAS u64* p = (const GAS u64*)(ws + off);
#pragma unroll 1
  for (u32 n = 0; n < (1u << 16); ++n) { if (__all(__hip_atomic_load(p, RLX_AGENT) == tag)) break; __builtin_amdgcn_s_sleep(1); }
  asm volatile("" ::: "memory");
}

constexpr float EPS = 1e-5f, LOG2E = 1.4426950408889634f;

#define OUTAUX 0
#define DWIN 5
#define EWIN 7
DI float fma0(float a, float c) { asm("v_fmac_f32 %0, 0, %1" : "+v"(c) : "v"(a)); return c; }
DI u64 mktag(u64 e) { return ((u64)(u32)__builtin_amdgcn_readfirstlane((u32)(e >> 32)) << 32 | (u32)__builtin_amdgcn_readfirstlane((u32)e)) * 6364136223846793005ull + 1442695040888963407ull; }
DI float swiglu(float ag, float au, float cg, float cu, float nm, float ne, float var) { float g, u, e;
  asm("v_fma_f32 %0, %5, %7, %3\n\tv_fma_f32 %1, %6, %7, %4\n\tv_mul_f32 %2, %0, %8\n\tv_exp_f32 %2, %2\n\tv_mul_f32 %0, %0, %1\n\tv_fma_f32 %2, %2, %9, %9\n\tv_rcp_f32 %2, %2\n\ts_nop 0\n\tv_mul_f32 %0, %0, %2"
      : "=&v"(g), "=&v"(u), "=&v"(e) : "v"(ag), "v"(au), "v"(cg), "v"(cu), "v"(nm), "v"(ne), "v"(var));
  return g; }

struct Job { u32x4 d[4]; };
DI void job_load(int J, int lane, const char* wo, const char* w1, const char* w3, const char* w2, Job& jb_) {
  const int t13 = J < 1376, two = !t13 && J < 1632, tw2 = J >= 1632 && J < 1976;
  const int ja = t13 ? J / 43 : (two ? (J - 1376) >> 3 : (J - 1632) >> 3), jb = t13 ? J - ja * 43 : (J & 7);
  const rsrc_t r0 = mk(t13 ? (ja >= 16 ? w3 : w1) : (two ? wo : w2), two ? 512 * 1024 : 1376 * 1024);
  const u32 jv = t13 ? (lane >> 2) * 2752 + (lane & 3) * 16 : (J < 1976 ? (lane >> 3) * 1024 + (lane & 7) * 16 : OOB), jv2 = tw2 ? jv : OOB;
  const u32 js = t13 ? (ja & 15) * 32 * 2752 + jb * 64 : (two ? ja * 16384 + jb * 128 : (tw2 ? ja * 32768 + jb * 128 : 0));
  jb_.d[0] = __builtin_amdgcn_raw_buffer_load_b128(r0, jv, js, 0);
  jb_.d[1] = __builtin_amdgcn_raw_buffer_load_b128(r0, jv, js + (t13 ? 16 * 2752 : 8192), 0);
  jb_.d[2] = __builtin_amdgcn_raw_buffer_load_b128(r0, jv2, js + 16384, 0);
  jb_.d[3] = __builtin_amdgcn_raw_buffer_load_b128(r0, jv2, js + 24576, 0);
}
DI void job_store(int J, int lane, rsrc_t rS, u32 sc, const Job& jb_) {
  const int t13 = J < 1376, two = !t13 && J < 1632, tw2 = J >= 1632 && J < 1976;
  const int ja = t13 ? J / 43 : (two ? (J - 1376) >> 3 : (J - 1632) >> 3), jb = t13 ? J - ja * 43 : (J & 7);
  const int h = lane >> 5, blk = (lane >> 4) & 1, tq = (lane & 15) >> 2, tp = lane & 3; const u32 l16 = lane * 16;
#pragma unroll
  for (int p = 0; p < 4; ++p) lds4w(sc + p * 1024 + l16, jb_.d[p]);
  asm volatile("s_waitcnt lgkmcnt(0)" ::: "memory");
  const u32 ta = sc + tp * 8 + (t13 ? (16 * h + 4 * blk + tq) * 64 : (two ? (4 * h + tq) * 128 + blk * 32 : (16 * h + 8 * blk + tq) * 128));
  const u32 ts = two ? 1024 : 512, es = two ? 64 : 32;
  const u32 d0 = t13 ? WS_W13T + (jb * 64 + ja) * 1024 : (two ? WS_WOT + (jb * 64 + ja) * 1024 : (tw2 ? WS_W2T + (jb * 43 + ja) * 4096 : 0)), dsr = tw2 ? 1024 : 32768;
#pragma unroll
  for (int e = 0; e < 4; ++e) st4(rS, ldtr2(ta + e * es, ta + e * es + ts), (e < 2 ? J < 1976 : tw2) ? l16 : OOB, d0 + e * dsr);
}

__global__ __launch_bounds__(512, 2) void block_fwd(const char* __restrict__ x, const char* __restrict__ wqkv, const char* __restrict__ wo, const char* __restrict__ w1,
                                                    const char* __restrict__ w3, const char* __restrict__ w2, char* __restrict__ out, char* ws) {
  __shared__ __attribute__((aligned(4096))) char lds[163840];
  const u32 L = (u32)(size_t)(LAS char*)lds;
  const int w = __builtin_amdgcn_readfirstlane(threadIdx.x >> 6);
  const int b = blockIdx.x, g = b & 7, r = b >> 3;
  int lane = threadIdx.x & 63;
#define LANEVALS() asm volatile("" : "+v"(lane)); \
  const int r32 = lane & 31, h = (lane >> 5) & 1, blk = (lane >> 4) & 1, tq = (lane & 15) >> 2, tp = lane & 3, g16 = (lane >> 4) & 3, t15 = lane & 15; const u32 l16 = lane * 16; \
  (void)r32; (void)h; (void)blk; (void)tq; (void)tp; (void)g16; (void)t15; (void)l16
  const rsrc_t rS = mk(ws, WS_END);
  STAMP(0);

  const int quad = 2 * (g & 3) + (r & 1), rb = 16 * (g >> 2) + (r >> 1);
  u64 tag;
  {
    LANEVALS();
    const int tb = w & 1, hp2 = (w >> 1) & 1, row = 32 * tb + r32, sz = (row >> 1) & 7;
    u32 xa[4];
#pragma unroll
    for (int ks = 0; ks < 4; ++ks) xa[ks] = L + LA_X + row * 128 + (((2 * ks + h) ^ sz) << 4);
    const u32 wa = L + LA_W + (8 * h + tq) * 128 + ((hp2 ^ (tq >> 1)) << 6) + blk * 32 + tp * 8;
    const u32 so = (h == (tp >> 1) && blk == (tq >> 1)) ? 0x3f803f80u : 0u, sidx = (tp & 1) ? 0xEEEEu : 0x4444u;
    const u32x4 sel = (tq & 1) ? u32x4{0u, 0u, so, so} : u32x4{so, so, 0u, 0u};
    u64 epoch;
    if (w >= 4) {
      const int lw = w - 4, row8 = lane >> 3, p8 = lane & 7;
      const rsrc_t rX = mk(x, 2048 * 1024), rW = mk(wqkv, 512 * 3072);
      const u32 vx0 = row8 * 1024 + ((p8 ^ (row8 >> 1)) << 4), vx1 = vx0 ^ 64, sx = (64 * rb + 16 * lw) * 1024;
      const u32 vw = row8 * 3072 + ((p8 ^ ((row8 & 2) << 1)) << 4), sw = 16 * lw * 3072 + quad * 128;
      auto lx = [&](int j, int e) { dma(rX, e ? vx1 : vx0, sx + e * 8192 + j * 128, L + LA_X + j * 8192 + (2 * lw + e) * 1024); };
      auto lwu = [&](int u, int e) { const int sl = u < 16 ? u >> 1 : u - 16, m = u < 16 ? 1 + (u & 1) : 0;
        dma(rW, vw, sw + (sl * 64 + e * 8) * 3072 + m * 1024, L + LA_W + (u % 10) * 8192 + (2 * lw + e) * 1024); };
#pragma unroll
      for (int j = 0; j < 4; ++j) { lx(j, 0); lx(j, 1); lwu(2 * j, 0); lwu(2 * j, 1); lwu(2 * j + 1, 0); lwu(2 * j + 1, 1); }
      const u32 xs = L + LA_X + (16 * lw + t15) * 128 + ((g16 ^ (t15 >> 1)) << 4);
      float s1 = 0.f, s2 = 0.f, s3 = 0.f, s4 = 0.f;
#pragma unroll
      for (int j = 0; j < 8; ++j) {
        if (j < 3 || j == 4) WAITVM_BAR(18); else if (j == 3) WAITVM_BAR(20); else if (j == 5) WAITVM_BAR(16); else WAITVM_BAR(12);
        XSTAMP_A(j);
        const int G = j + 4;
        u32x4 a, c;
        if (j < 7) { a = lds4(xs + j * 8192); c = lds4((xs ^ 64) + j * 8192); }
        if (G <= 6) { lx(G, 0); lx(G, 1); }
        if (j < 7) { stat1(a[0], s1, s2, s3, s4); stat1(a[1], s1, s2, s3, s4); SCHED(); }
        if (G == 6) { lx(7, 0); lx(7, 1); }
        lwu(2 * G, 0);
        if (j < 7) { stat1(a[2], s1, s2, s3, s4); stat1(a[3], s1, s2, s3, s4); SCHED(); }
        lwu(2 * G, 1);
        if (j < 7) { stat1(c[0], s1, s2, s3, s4); stat1(c[1], s1, s2, s3, s4); SCHED(); }
        lwu(2 * G + 1, 0);
        if (j < 7) { stat1(c[2], s1, s2, s3, s4); stat1(c[3], s1, s2, s3, s4); SCHED(); }
        lwu(2 * G + 1, 1);
        if (j == 6) {
          a = lds4(xs + 7 * 8192); c = lds4((xs ^ 64) + 7 * 8192);
#pragma unroll
          for (int i = 0; i < 4; ++i) { stat1(a[i], s1, s2, s3, s4); stat1(c[i], s1, s2, s3, s4); }
          const float mean = swap32_add(swap16_add(s1 + s3)) * (1.f / 512.f);
          const float rstd = __builtin_amdgcn_rsqf(fmaxf(swap32_add(swap16_add(s2 + s4)) * (1.f / 512.f) - mean * mean, 0.f) + EPS);
          if (lane < 16) *(LAS f32x2*)(L + LA_STAT + (16 * lw + lane) * 8) = f32x2{mean, rstd};
        }
      }
      epoch = __hip_atomic_load((const GAS u64*)(ws + WS_EPOCH), RLX_AGENT);
      WAITVM_BAR(1);
      STAMP(3);
      const f32x2 st = *(LAS const f32x2*)(L + LA_STAT + row * 8);
      f32x16 acc = splat16(0.f); f32x4 cs = {0.f, 0.f, 0.f, 0.f};
      u32x4 xf[2][2], wf[2][2];
#pragma unroll
      for (int s = 0; s <= 16; ++s) {
        if (s < 16) {
#pragma unroll
          for (int e = 0; e < 2; ++e) { const int k = 2 * s + e; const u32 o = ((16 + (k >> 2)) % 10) * 8192 + (k & 3) * 2048;
            xf[s & 1][e] = lds4(xa[k & 3] + (k >> 2) * 8192); wf[s & 1][e] = ldtr2(wa + o, wa + o + 512); } }
        SCHED();
        if (s > 0) { const int p = (s - 1) & 1; acc = mf32(wf[p][0], xf[p][0], acc); cs = sum2(sel, sidx, wf[p][0], wf[p][1], cs); acc = mf32(wf[p][1], xf[p][1], acc); }
        SCHED();
      }
      acc = mfr1((cs[0] + cs[1]) + (cs[2] + cs[3]), -st[0], acc);
      const float qs = st[1] * (0.25f * LOG2E);
#pragma unroll
      for (int e = 0; e < 2; ++e) { u32x4 v; for (int i = 0; i < 4; ++i) v[i] = pk2(acc[8 * e + 2 * i] * qs, acc[8 * e + 2 * i + 1] * qs);
        lds4w(L + LA_Q + ((2 * hp2 + e) * 2 + tb) * 1024 + l16, v); }
      asm volatile("" : "+v"(epoch));
      tag = mktag(epoch);
    } else {
      epoch = __hip_atomic_load((const GAS u64*)(ws + WS_EPOCH), RLX_AGENT);
      if (w == 0 && lane == 0) *(LAS u32x2*)(L + LA_CNT) = u32x2{0u, 0u};
      f32x16 ak = splat16(0.f), av = splat16(0.f), rs; f32x4 cs = {0.f, 0.f, 0.f, 0.f}; f32x2 st;
      u32x4 xf[3], wk[3], wv[3];
#pragma unroll
      for (int s = 0; s < 34; ++s) {
        const int c = s % 3, p = (s + 1) % 3, j = s >> 2; const u32 o = ((2 * j) % 10) * 8192 + (s & 3) * 2048;
        if (s < 32 && (s & 3) == 0) { BAR(); XSTAMP_A(j); }
        if (s == 28) st = *(LAS const f32x2*)(L + LA_STAT + row * 8);
        if (s < 32) wk[c] = ldtr2(wa + o, wa + o + 512);
        SCHED();
        if (s >= 2) ak = mf32(wk[p], xf[p], ak);
        SCHED();
        if (s < 32) wv[c] = ldtr2(wa + o + 8192, wa + o + 8192 + 512);
        SCHED();
        if (s >= 2) av = mf32(xf[p], wv[p], av);
        SCHED();
        if (s < 32) xf[c] = lds4(xa[s & 3] + j * 8192);
        SCHED();
        if (s >= 2) cs = sum2(sel, sidx, wk[p], wv[p], cs);
        if (s == 30) rs = __builtin_amdgcn_mfma_f32_32x32x2f32(h ? 0.f : st[1], 1.f, splat16(0.f), 0, 0, 0);
        SCHED();
      }
      STAMP(2);
      ak = __builtin_amdgcn_mfma_f32_32x32x2f32(cs[0] + cs[1], -st[0], ak, 0, 0, 0);
      av = __builtin_amdgcn_mfma_f32_32x32x2f32(-st[0], cs[2] + cs[3], av, 0, 0, 0);
      const u32 kvw = WS_KV + ((4 * quad + 2 * hp2) * 64 + 2 * rb + tb) * 2048;
      const u32 posk = ((r32 & ~12) | ((r32 & 4) << 1) | ((r32 & 8) >> 1)) * 16 + h * 512, posv = blk * 131072 + 1024 + h * 256 + t15 * 16;
#pragma unroll
      for (int e = 0; e < 2; ++e) { u32x4 v; for (int i = 0; i < 4; ++i) v[i] = pk2(ak[8 * e + 2 * i] * st[1], ak[8 * e + 2 * i + 1] * st[1]);
        st4(rS, v, posk, kvw + e * 131072); if (tb == 0) lds4w(L + LA_K + (2 * hp2 + e) * 1024 + l16, v); }
#pragma unroll
      for (int e = 0; e < 2; ++e) { u32x4 v; for (int i = 0; i < 4; ++i) v[i] = pk2(av[8 * e + 2 * i] * rs[8 * e + 2 * i], av[8 * e + 2 * i + 1] * rs[8 * e + 2 * i + 1]);
        st4(rS, v, posv, kvw + e * 512); }
      BAR();
      Job j0, j1;
      job_load(b * 8 + 2 * w, lane, wo, w1, w3, w2, j0); job_load(b * 8 + 2 * w + 1, lane, wo, w1, w3, w2, j1);
      SCHED();
      asm volatile("" : "+v"(epoch));
      tag = mktag(epoch);
      WAITVM(8);
      if (arrive(L + LA_CNT, 4, lane)) set_flag(ws, WS_FA + (quad * 32 + rb) * 128, tag);
      STAMP(3);
      const u32 sc = L + LA_W + 4 * 8192 + w * 4096;
      job_store(b * 8 + 2 * w, lane, rS, sc, j0); job_store(b * 8 + 2 * w + 1, lane, rS, sc, j1);
    }
    STAMP(4);
    BAR();
  }

  const int mt = g, ntr = r; const bool dact = r < 29;
  const int nt = dact ? ntr : 0, u0 = nt * 3 > 83 ? 83 : nt * 3;
  const int ctb = 8 * g + (r >> 2), cg = r & 3;
  const int erb = 4 * g + (r >> 3), ecb = r & 7;
  const int w3_ = w & 3, kh = w >> 2;
  u32x16 wo0, wo1, wo2, wo3; u32x2 xr0, xr1;

  {
    LANEVALS();
    const int head = w3_;
    const u32 ring = L + LQ + w * RING;
    { const rsrc_t rX = mk(x, 2048 * 1024); const u32 xo = (32 * ctb + r32) * 1024 + (128 * cg + 32 * w3_ + 16 * kh + 4 * h) * 2;
      xr0 = __builtin_amdgcn_raw_buffer_load_b64(rX, xo, 0, 0); xr1 = __builtin_amdgcn_raw_buffer_load_b64(rX, xo + 16, 0, 0); }
    if (lane < 2 * (int)NB) { const u32 one = (lane & 1) ? 0u : 0x3f803f80u; lds4w(ring + (lane >> 1) * VSTRIDE + 1024 + (lane & 1) * 16, u32x4{one, one, one, one}); }
    const u32x4 q0 = lds4(L + LA_Q + head * 2048 + kh * 1024 + l16), q1 = lds4(L + LA_Q + head * 2048 + (kh ^ 1) * 1024 + l16), kown = lds4(L + LA_K + head * 1024 + l16);
    float m0 = swap32_max(max16(mf32(kown, q0, splat16(0.f)))), m1 = swap32_max(max16(mf32(kown, q1, splat16(0.f))));
    const u32 vdat = ring + h * 256 + t15 * 16, vcon = ring + 1024 + ((lane & 11) ? 16 : 0);
    const u32 va0 = blk ? vcon : vdat, va1 = blk ? vcon : vdat + 512;
    const u32 kv0 = WS_KV + ((4 * quad + head) * 64 + 32 * kh) * 2048, lwo = l16 ^ (kh * 256);
    u32 swo0 = WS_WOT + ((4 * cg + w3_) * 32 + 16 * kh) * 1024, s130 = WS_W13T + u0 * 32768 + 12 * w * 1024, spl = L + 12 * w * 1024;
    asm volatile("" : "+v"(m0), "+v"(m1), "+s"(swo0), "+s"(s130), "+s"(spl));
    WAITVM(0);
    if (arrive(L + LA_CNT + 4, 8, lane)) __hip_atomic_store((GAS u32*)(ws + WS_FR + b * 4), (u32)tag, RLX_AGENT);
    STAMP(5);
    poll(ws, WS_FA + (quad * 32 + 16 * kh + t15) * 128, tag);
    STAMP(6);
    f32x16 o0, o1;
#pragma unroll 1
    for (int attempt = 0; attempt < 2; ++attempt) {
      u32 kv = kv0, swo = swo0, s13 = s130, tmp, cnt; u64 t2;
      asm volatile(
    "s_cmp_lt_u32 %[wv], 4\n"
    "s_cselect_b32 %[tmp], 1, 0\n"
    "s_setprio 1\n"
    "s_cbranch_scc0 1f\n"
    "s_setprio 2\n"
    "1:\n"
    "buffer_load_dwordx4 v[56:59], %[l16], %[rs], %[kv] offen offset:0 sc1\n"
    "s_add_u32 m0, %[ring], 0xfffffc00\n"
    "s_nop 0\n"
    "buffer_load_dwordx4 %[l16], %[rs], %[kv] offen offset:1024 sc1 lds\n"
    "buffer_load_dwordx4 v[60:63], %[l16], %[rs], %[kv] offen offset:2048 sc1\n"
    "s_add_u32 m0, %[ring], 0xfffff820\n"
    "s_nop 0\n"
    "buffer_load_dwordx4 %[l16], %[rs], %[kv] offen offset:3072 sc1 lds\n"
    "s_add_u32 %[kv], %[kv], 0x1000\n"
    "buffer_load_dwordx4 v[64:67], %[l16], %[rs], %[kv] offen offset:0 sc1\n"
    "s_add_u32 m0, %[ring], 0x440\n"
    "s_nop 0\n"
    "buffer_load_dwordx4 %[l16], %[rs], %[kv] offen offset:1024 sc1 lds\n"
    "buffer_load_dwordx4 v[68:71], %[l16], %[rs], %[kv] offen offset:2048 sc1\n"
    "s_add_u32 m0, %[ring], 0x60\n"
    "s_nop 0\n"
    "buffer_load_dwordx4 %[l16], %[rs], %[kv] offen offset:3072 sc1 lds\n"
    "s_add_u32 %[kv], %[kv], 0x1000\n"
    "buffer_load_dwordx4 v[72:75], %[l16], %[rs], %[kv] offen offset:0 sc1\n"
    "s_add_u32 m0, %[ring], 0xc80\n"
    "s_nop 0\n"
    "buffer_load_dwordx4 %[l16], %[rs], %[kv] offen offset:1024 sc1 lds\n"
    "buffer_load_dwordx4 v[76:79], %[l16], %[rs], %[kv] offen offset:2048 sc1\n"
    "s_add_u32 m0, %[ring], 0x8a0\n"
    "s_nop 0\n"
    "buffer_load_dwordx4 %[l16], %[rs], %[kv] offen offset:3072 sc1 lds\n"
    "v_xor_b32 v160, 0x80000000, %[m0]\n"
    "v_xor_b32 v176, 0x80000000, %[m1]\n"
    "v_mov_b32 v161, v160\n"
    "v_mov_b32 v177, v176\n"
    "v_mov_b32 v162, v160\n"
    "v_mov_b32 v178, v176\n"
    "v_mov_b32 v163, v160\n"
    "v_mov_b32 v179, v176\n"
    "v_mov_b32 v164, v160\n"
    "v_mov_b32 v180, v176\n"
    "v_mov_b32 v165, v160\n"
    "v_mov_b32 v181, v176\n"
    "v_mov_b32 v166, v160\n"
    "v_mov_b32 v182, v176\n"
    "v_mov_b32 v167, v160\n"
    "v_mov_b32 v183, v176\n"
    "v_mov_b32 v168, v160\n"
    "v_mov_b32 v184, v176\n"
    "v_mov_b32 v169, v160\n"
    "v_mov_b32 v185, v176\n"
    "v_mov_b32 v170, v160\n"
    "v_mov_b32 v186, v176\n"
    "v_mov_b32 v171, v160\n"
    "v_mov_b32 v187, v176\n"
    "v_mov_b32 v172, v160\n"
    "v_mov_b32 v188, v176\n"
    "v_mov_b32 v173, v160\n"
    "v_mov_b32 v189, v176\n"
    "v_mov_b32 v174, v160\n"
    "v_mov_b32 v190, v176\n"
    "v_mov_b32 v175, v160\n"
    "v_mov_b32 v191, v176\n"
    "v_mov_b32 v128, 0\n"
    "v_mov_b32 v129, 0\n"
    "v_mov_b32 v130, 0\n"
    "v_mov_b32 v131, 0\n"
    "v_mov_b32 v132, 0\n"
    "v_mov_b32 v133, 0\n"
    "v_mov_b32 v134, 0\n"
    "v_mov_b32 v135, 0\n"
    "v_mov_b32 v136, 0\n"
    "v_mov_b32 v137, 0\n"
    "v_mov_b32 v138, 0\n"
    "v_mov_b32 v139, 0\n"
    "v_mov_b32 v140, 0\n"
    "v_mov_b32 v141, 0\n"
    "v_mov_b32 v142, 0\n"
    "v_mov_b32 v143, 0\n"
    "v_mov_b32 v144, 0\n"
    "v_mov_b32 v145, 0\n"
    "v_mov_b32 v146, 0\n"
    "v_mov_b32 v147, 0\n"
    "v_mov_b32 v148, 0\n"
    "v_mov_b32 v149, 0\n"
    "v_mov_b32 v150, 0\n"
    "v_mov_b32 v151, 0\n"
    "v_mov_b32 v152, 0\n"
    "v_mov_b32 v153, 0\n"
    "v_mov_b32 v154, 0\n"
    "v_mov_b32 v155, 0\n"
    "v_mov_b32 v156, 0\n"
    "v_mov_b32 v157, 0\n"
    "v_mov_b32 v158, 0\n"
    "v_mov_b32 v159, 0\n"
    "s_waitcnt vmcnt(10)\n"
    "ds_read_b128 v[48:51], %[va0] offset:0\n"
    "v_mfma_f32_32x32x16_bf16 v[96:111], v[56:59], v[88:91], v[160:175]\n"
    "s_nop 11\n"
    "v_exp_f32 v96, v96\n"
    "v_exp_f32 v97, v97\n"
    "ds_read_b128 v[52:55], %[va1] offset:0\n"
    "v_exp_f32 v98, v98\n"
    "v_exp_f32 v99, v99\n"
    "v_exp_f32 v100, v100\n"
    "v_mfma_f32_32x32x16_bf16 v[112:127], v[56:59], v[92:95], v[176:191]\n"
    "s_waitcnt lgkmcnt(1)\n"
    "v_exp_f32 v101, v101\n"
    "v_exp_f32 v102, v102\n"
    "v_exp_f32 v103, v103\n"
    "v_exp_f32 v104, v104\n"
    "v_exp_f32 v105, v105\n"
    "v_exp_f32 v106, v106\n"
    "v_smfmac_f32_32x32x32_bf16 v[128:143], v[48:51], v[96:103], %[idx]\n"
    "s_add_u32 %[kv], %[kv], 0x1000\n"
    "buffer_load_dwordx4 v[56:59], %[l16], %[rs], %[kv] offen offset:0 sc1\n"
    "v_exp_f32 v107, v107\n"
    "v_exp_f32 v108, v108\n"
    "v_exp_f32 v109, v109\n"
    "v_exp_f32 v110, v110\n"
    "v_exp_f32 v111, v111\n"
    "v_exp_f32 v112, v112\n"
    "v_exp_f32 v113, v113\n"
    "s_waitcnt vmcnt(9) lgkmcnt(0)\n"
    "v_smfmac_f32_32x32x32_bf16 v[128:143], v[52:55], v[104:111], %[idx]\n"
    "s_add_u32 m0, %[ring], 0xfffffc00\n"
    "v_exp_f32 v114, v114\n"
    "v_exp_f32 v115, v115\n"
    "v_exp_f32 v116, v116\n"
    "v_mfma_f32_32x32x16_bf16 v[96:111], v[60:63], v[88:91], v[160:175]\n"
    "buffer_load_dwordx4 %[l16], %[rs], %[kv] offen offset:1024 sc1 lds\n"
    "v_exp_f32 v117, v117\n"
    "v_exp_f32 v118, v118\n"
    "v_exp_f32 v119, v119\n"
    "v_exp_f32 v120, v120\n"
    "v_exp_f32 v121, v121\n"
    "v_exp_f32 v122, v122\n"
    "v_smfmac_f32_32x32x32_bf16 v[144:159], v[48:51], v[112:119], %[idx]\n"
    "ds_read_b128 v[48:51], %[va0] offset:1056\n"
    "v_exp_f32 v123, v123\n"
    "v_exp_f32 v124, v124\n"
    "v_exp_f32 v125, v125\n"
    "v_exp_f32 v126, v126\n"
    "v_exp_f32 v127, v127\n"
    "v_exp_f32 v96, v96\n"
    "v_exp_f32 v97, v97\n"
    "v_smfmac_f32_32x32x32_bf16 v[144:159], v[52:55], v[120:127], %[idx]\n"
    "ds_read_b128 v[52:55], %[va1] offset:1056\n"
    "v_exp_f32 v98, v98\n"
    "v_exp_f32 v99, v99\n"
    "v_exp_f32 v100, v100\n"
    "v_mfma_f32_32x32x16_bf16 v[112:127], v[60:63], v[92:95], v[176:191]\n"
    "s_waitcnt lgkmcnt(1)\n"
    "v_exp_f32 v101, v101\n"
    "v_exp_f32 v102, v102\n"
    "v_exp_f32 v103, v103\n"
    "v_exp_f32 v104, v104\n"
    "v_exp_f32 v105, v105\n"
    "v_exp_f32 v106, v106\n"
    "v_smfmac_f32_32x32x32_bf16 v[128:143], v[48:51], v[96:103], %[idx]\n"
    "buffer_load_dwordx4 v[60:63], %[l16], %[rs], %[kv] offen offset:2048 sc1\n"
    "v_exp_f32 v107, v107\n"
    "v_exp_f32 v108, v108\n"
    "v_exp_f32 v109, v109\n"
    "v_exp_f32 v110, v110\n"
    "v_exp_f32 v111, v111\n"
    "v_exp_f32 v112, v112\n"
    "v_exp_f32 v113, v113\n"
    "s_waitcnt vmcnt(9) lgkmcnt(0)\n"
    "v_smfmac_f32_32x32x32_bf16 v[128:143], v[52:55], v[104:111], %[idx]\n"
    "s_add_u32 m0, %[ring], 0xfffff820\n"
    "v_exp_f32 v114, v114\n"
    "v_exp_f32 v115, v115\n"
    "v_exp_f32 v116, v116\n"
    "v_mfma_f32_32x32x16_bf16 v[96:111], v[64:67], v[88:91], v[160:175]\n"
    "buffer_load_dwordx4 %[l16], %[rs], %[kv] offen offset:3072 sc1 lds\n"
    "v_exp_f32 v117, v117\n"
    "v_exp_f32 v118, v118\n"
    "v_exp_f32 v119, v119\n"
    "v_exp_f32 v120, v120\n"
    "v_exp_f32 v121, v121\n"
    "v_exp_f32 v122, v122\n"
    "v_smfmac_f32_32x32x32_bf16 v[144:159], v[48:51], v[112:119], %[idx]\n"
    "ds_read_b128 v[48:51], %[va0] offset:2112\n"
    "v_exp_f32 v123, v123\n"
    "v_exp_f32 v124, v124\n"
    "v_exp_f32 v125, v125\n"
    "v_exp_f32 v126, v126\n"
    "v_exp_f32 v127, v127\n"
    "v_exp_f32 v96, v96\n"
    "v_exp_f32 v97, v97\n"
    "v_smfmac_f32_32x32x32_bf16 v[144:159], v[52:55], v[120:127], %[idx]\n"
    "ds_read_b128 v[52:55], %[va1] offset:2112\n"
    "v_exp_f32 v98, v98\n"
    "v_exp_f32 v99, v99\n"
    "v_exp_f32 v100, v100\n"
    "v_mfma_f32_32x32x16_bf16 v[112:127], v[64:67], v[92:95], v[176:191]\n"
    "s_waitcnt lgkmcnt(1)\n"
    "v_exp_f32 v101, v101\n"
    "v_exp_f32 v102, v102\n"
    "v_exp_f32 v103, v103\n"
    "v_exp_f32 v104, v104\n"
    "v_exp_f32 v105, v105\n"
    "v_exp_f32 v106, v106\n"
    "v_smfmac_f32_32x32x32_bf16 v[128:143], v[48:51], v[96:103], %[idx]\n"
    "s_add_u32 %[kv], %[kv], 0x1000\n"
    "buffer_load_dwordx4 v[64:67], %[l16], %[rs], %[kv] offen offset:0 sc1\n"
    "v_exp_f32 v107, v107\n"
    "v_exp_f32 v108, v108\n"
    "v_exp_f32 v109, v109\n"
    "v_exp_f32 v110, v110\n"
    "v_exp_f32 v111, v111\n"
    "v_exp_f32 v112, v112\n"
    "v_exp_f32 v113, v113\n"
    "s_waitcnt vmcnt(9) lgkmcnt(0)\n"
    "v_smfmac_f32_32x32x32_bf16 v[128:143], v[52:55], v[104:111], %[idx]\n"
    "s_add_u32 m0, %[ring], 0x440\n"
    "v_exp_f32 v114, v114\n"
    "v_exp_f32 v115, v115\n"
    "v_exp_f32 v116, v116\n"
    "v_mfma_f32_32x32x16_bf16 v[96:111], v[68:71], v[88:91], v[160:175]\n"
    "buffer_load_dwordx4 %[l16], %[rs], %[kv] offen offset:1024 sc1 lds\n"
    "v_exp_f32 v117, v117\n"
    "v_exp_f32 v118, v118\n"
    "v_exp_f32 v119, v119\n"
    "v_exp_f32 v120, v120\n"
    "v_exp_f32 v121, v121\n"
    "v_exp_f32 v122, v122\n"
    "v_smfmac_f32_32x32x32_bf16 v[144:159], v[48:51], v[112:119], %[idx]\n"
    "ds_read_b128 v[48:51], %[va0] offset:3168\n"
    "v_exp_f32 v123, v123\n"
    "v_exp_f32 v124, v124\n"
    "v_exp_f32 v125, v125\n"
    "v_exp_f32 v126, v126\n"
    "v_exp_f32 v127, v127\n"
    "v_exp_f32 v96, v96\n"
    "v_exp_f32 v97, v97\n"
    "v_smfmac_f32_32x32x32_bf16 v[144:159], v[52:55], v[120:127], %[idx]\n"
    "ds_read_b128 v[52:55], %[va1] offset:3168\n"
    "v_exp_f32 v98, v98\n"
    "v_exp_f32 v99, v99\n"
    "v_exp_f32 v100, v100\n"
    "v_mfma_f32_32x32x16_bf16 v[112:127], v[68:71], v[92:95], v[176:191]\n"
    "buffer_load_dwordx4 v[192:195], %[l16], %[rs], %[fr] offen sc1\n"
    "s_waitcnt lgkmcnt(1)\n"
    "v_exp_f32 v101, v101\n"
    "v_exp_f32 v102, v102\n"
    "v_exp_f32 v103, v103\n"
    "v_exp_f32 v104, v104\n"
    "v_exp_f32 v105, v105\n"
    "v_exp_f32 v106, v106\n"
    "v_smfmac_f32_32x32x32_bf16 v[128:143], v[48:51], v[96:103], %[idx]\n"
    "buffer_load_dwordx4 v[68:71], %[l16], %[rs], %[kv] offen offset:2048 sc1\n"
    "v_exp_f32 v107, v107\n"
    "v_exp_f32 v108, v108\n"
    "v_exp_f32 v109, v109\n"
    "v_exp_f32 v110, v110\n"
    "v_exp_f32 v111, v111\n"
    "v_exp_f32 v112, v112\n"
    "v_exp_f32 v113, v113\n"
    "s_waitcnt vmcnt(10) lgkmcnt(0)\n"
    "v_smfmac_f32_32x32x32_bf16 v[128:143], v[52:55], v[104:111], %[idx]\n"
    "s_add_u32 m0, %[ring], 0x60\n"
    "v_exp_f32 v114, v114\n"
    "v_exp_f32 v115, v115\n"
    "v_exp_f32 v116, v116\n"
    "v_mfma_f32_32x32x16_bf16 v[96:111], v[72:75], v[88:91], v[160:175]\n"
    "buffer_load_dwordx4 %[l16], %[rs], %[kv] offen offset:3072 sc1 lds\n"
    "v_exp_f32 v117, v117\n"
    "v_exp_f32 v118, v118\n"
    "v_exp_f32 v119, v119\n"
    "v_exp_f32 v120, v120\n"
    "v_exp_f32 v121, v121\n"
    "v_exp_f32 v122, v122\n"
    "v_smfmac_f32_32x32x32_bf16 v[144:159], v[48:51], v[112:119], %[idx]\n"
    "ds_read_b128 v[48:51], %[va0] offset:4224\n"
    "v_exp_f32 v123, v123\n"
    "v_exp_f32 v124, v124\n"
    "v_exp_f32 v125, v125\n"
    "v_exp_f32 v126, v126\n"
    "v_exp_f32 v127, v127\n"
    "v_exp_f32 v96, v96\n"
    "v_exp_f32 v97, v97\n"
    "v_smfmac_f32_32x32x32_bf16 v[144:159], v[52:55], v[120:127], %[idx]\n"
    "ds_read_b128 v[52:55], %[va1] offset:4224\n"
    "v_exp_f32 v98, v98\n"
    "v_exp_f32 v99, v99\n"
    "v_exp_f32 v100, v100\n"
    "v_mfma_f32_32x32x16_bf16 v[112:127], v[72:75], v[92:95], v[176:191]\n"
    "s_waitcnt lgkmcnt(1)\n"
    "v_exp_f32 v101, v101\n"
    "v_exp_f32 v102, v102\n"
    "v_exp_f32 v103, v103\n"
    "v_exp_f32 v104, v104\n"
    "v_exp_f32 v105, v105\n"
    "v_exp_f32 v106, v106\n"
    "v_smfmac_f32_32x32x32_bf16 v[128:143], v[48:51], v[96:103], %[idx]\n"
    "s_add_u32 %[kv], %[kv], 0x1000\n"
    "buffer_load_dwordx4 v[72:75], %[l16], %[rs], %[kv] offen offset:0 sc1\n"
    "v_exp_f32 v107, v107\n"
    "v_exp_f32 v108, v108\n"
    "v_exp_f32 v109, v109\n"
    "v_exp_f32 v110, v110\n"
    "v_exp_f32 v111, v111\n"
    "v_exp_f32 v112, v112\n"
    "v_exp_f32 v113, v113\n"
    "s_waitcnt vmcnt(10) lgkmcnt(0)\n"
    "v_smfmac_f32_32x32x32_bf16 v[128:143], v[52:55], v[104:111], %[idx]\n"
    "s_add_u32 m0, %[ring], 0xc80\n"
    "v_exp_f32 v114, v114\n"
    "v_exp_f32 v115, v115\n"
    "v_exp_f32 v116, v116\n"
    "v_mfma_f32_32x32x16_bf16 v[96:111], v[76:79], v[88:91], v[160:175]\n"
    "buffer_load_dwordx4 %[l16], %[rs], %[kv] offen offset:1024 sc1 lds\n"
    "v_exp_f32 v117, v117\n"
    "v_exp_f32 v118, v118\n"
    "v_exp_f32 v119, v119\n"
    "v_exp_f32 v120, v120\n"
    "v_exp_f32 v121, v121\n"
    "v_exp_f32 v122, v122\n"
    "v_smfmac_f32_32x32x32_bf16 v[144:159], v[48:51], v[112:119], %[idx]\n"
    "ds_read_b128 v[48:51], %[va0] offset:5280\n"
    "v_exp_f32 v123, v123\n"
    "v_exp_f32 v124, v124\n"
    "v_exp_f32 v125, v125\n"
    "v_exp_f32 v126, v126\n"
    "v_exp_f32 v127, v127\n"
    "v_exp_f32 v96, v96\n"
    "v_exp_f32 v97, v97\n"
    "v_smfmac_f32_32x32x32_bf16 v[144:159], v[52:55], v[120:127], %[idx]\n"
    "ds_read_b128 v[52:55], %[va1] offset:5280\n"
    "v_exp_f32 v98, v98\n"
    "v_exp_f32 v99, v99\n"
    "v_exp_f32 v100, v100\n"
    "v_mfma_f32_32x32x16_bf16 v[112:127], v[76:79], v[92:95], v[176:191]\n"
    "s_waitcnt lgkmcnt(1)\n"
    "v_exp_f32 v101, v101\n"
    "v_exp_f32 v102, v102\n"
    "v_exp_f32 v103, v103\n"
    "v_exp_f32 v104, v104\n"
    "v_exp_f32 v105, v105\n"
    "v_exp_f32 v106, v106\n"
    "v_smfmac_f32_32x32x32_bf16 v[128:143], v[48:51], v[96:103], %[idx]\n"
    "buffer_load_dwordx4 v[76:79], %[l16], %[rs], %[kv] offen offset:2048 sc1\n"
    "v_exp_f32 v107, v107\n"
    "v_exp_f32 v108, v108\n"
    "v_exp_f32 v109, v109\n"
    "v_exp_f32 v110, v110\n"
    "v_exp_f32 v111, v111\n"
    "v_exp_f32 v112, v112\n"
    "v_exp_f32 v113, v113\n"
    "s_waitcnt vmcnt(10) lgkmcnt(0)\n"
    "v_smfmac_f32_32x32x32_bf16 v[128:143], v[52:55], v[104:111], %[idx]\n"
    "s_add_u32 m0, %[ring], 0x8a0\n"
    "v_exp_f32 v114, v114\n"
    "v_exp_f32 v115, v115\n"
    "v_exp_f32 v116, v116\n"
    "v_mfma_f32_32x32x16_bf16 v[96:111], v[56:59], v[88:91], v[160:175]\n"
    "buffer_load_dwordx4 %[l16], %[rs], %[kv] offen offset:3072 sc1 lds\n"
    "v_exp_f32 v117, v117\n"
    "v_exp_f32 v118, v118\n"
    "v_exp_f32 v119, v119\n"
    "v_exp_f32 v120, v120\n"
    "v_exp_f32 v121, v121\n"
    "v_exp_f32 v122, v122\n"
    "v_smfmac_f32_32x32x32_bf16 v[144:159], v[48:51], v[112:119], %[idx]\n"
    "ds_read_b128 v[48:51], %[va0] offset:0\n"
    "v_exp_f32 v123, v123\n"
    "v_exp_f32 v124, v124\n"
    "v_exp_f32 v125, v125\n"
    "v_exp_f32 v126, v126\n"
    "v_exp_f32 v127, v127\n"
    "v_exp_f32 v96, v96\n"
    "v_exp_f32 v97, v97\n"
    "v_smfmac_f32_32x32x32_bf16 v[144:159], v[52:55], v[120:127], %[idx]\n"
    "ds_read_b128 v[52:55], %[va1] offset:0\n"
    "v_exp_f32 v98, v98\n"
    "v_exp_f32 v99, v99\n"
    "v_exp_f32 v100, v100\n"
    "v_mfma_f32_32x32x16_bf16 v[112:127], v[56:59], v[92:95], v[176:191]\n"
    "s_waitcnt lgkmcnt(1)\n"
    "v_exp_f32 v101, v101\n"
    "v_exp_f32 v102, v102\n"
    "v_exp_f32 v103, v103\n"
    "v_exp_f32 v104, v104\n"
    "v_exp_f32 v105, v105\n"
    "v_exp_f32 v106, v106\n"
    "v_smfmac_f32_32x32x32_bf16 v[128:143], v[48:51], v[96:103], %[idx]\n"
    "s_add_u32 %[kv], %[kv], 0x1000\n"
    "buffer_load_dwordx4 v[56:59], %[l16], %[rs], %[kv] offen offset:0 sc1\n"
    "v_exp_f32 v107, v107\n"
    "v_exp_f32 v108, v108\n"
    "v_exp_f32 v109, v109\n"
    "v_exp_f32 v110, v110\n"
    "v_exp_f32 v111, v111\n"
    "v_exp_f32 v112, v112\n"
    "v_exp_f32 v113, v113\n"
    "s_waitcnt vmcnt(10) lgkmcnt(0)\n"
    "v_smfmac_f32_32x32x32_bf16 v[128:143], v[52:55], v[104:111], %[idx]\n"
    "s_add_u32 m0, %[ring], 0xfffffc00\n"
    "v_exp_f32 v114, v114\n"
    "v_exp_f32 v115, v115\n"
    "v_exp_f32 v116, v116\n"
    "v_mfma_f32_32x32x16_bf16 v[96:111], v[60:63], v[88:91], v[160:175]\n"
    "buffer_load_dwordx4 %[l16], %[rs], %[kv] offen offset:1024 sc1 lds\n"
    "v_exp_f32 v117, v117\n"
    "v_exp_f32 v118, v118\n"
    "v_exp_f32 v119, v119\n"
    "v_exp_f32 v120, v120\n"
    "v_exp_f32 v121, v121\n"
    "v_exp_f32 v122, v122\n"
    "v_smfmac_f32_32x32x32_bf16 v[144:159], v[48:51], v[112:119], %[idx]\n"
    "ds_read_b128 v[48:51], %[va0] offset:1056\n"
    "v_exp_f32 v123, v123\n"
    "v_exp_f32 v124, v124\n"
    "v_exp_f32 v125, v125\n"
    "v_exp_f32 v126, v126\n"
    "v_exp_f32 v127, v127\n"
    "v_exp_f32 v96, v96\n"
    "v_exp_f32 v97, v97\n"
    "v_smfmac_f32_32x32x32_bf16 v[144:159], v[52:55], v[120:127], %[idx]\n"
    "ds_read_b128 v[52:55], %[va1] offset:1056\n"
    "v_exp_f32 v98, v98\n"
    "v_exp_f32 v99, v99\n"
    "v_exp_f32 v100, v100\n"
    "v_mfma_f32_32x32x16_bf16 v[112:127], v[60:63], v[92:95], v[176:191]\n"
    "s_waitcnt lgkmcnt(1)\n"
    "v_exp_f32 v101, v101\n"
    "v_exp_f32 v102, v102\n"
    "v_exp_f32 v103, v103\n"
    "v_exp_f32 v104, v104\n"
    "v_exp_f32 v105, v105\n"
    "v_exp_f32 v106, v106\n"
    "v_smfmac_f32_32x32x32_bf16 v[128:143], v[48:51], v[96:103], %[idx]\n"
    "buffer_load_dwordx4 v[60:63], %[l16], %[rs], %[kv] offen offset:2048 sc1\n"
    "v_exp_f32 v107, v107\n"
    "v_exp_f32 v108, v108\n"
    "v_exp_f32 v109, v109\n"
    "v_exp_f32 v110, v110\n"
    "v_exp_f32 v111, v111\n"
    "v_exp_f32 v112, v112\n"
    "v_exp_f32 v113, v113\n"
    "s_waitcnt vmcnt(10) lgkmcnt(0)\n"
    "v_smfmac_f32_32x32x32_bf16 v[128:143], v[52:55], v[104:111], %[idx]\n"
    "s_add_u32 m0, %[ring], 0xfffff820\n"
    "v_exp_f32 v114, v114\n"
    "v_exp_f32 v115, v115\n"
    "v_exp_f32 v116, v116\n"
    "v_mfma_f32_32x32x16_bf16 v[96:111], v[64:67], v[88:91], v[160:175]\n"
    "buffer_load_dwordx4 %[l16], %[rs], %[kv] offen offset:3072 sc1 lds\n"
    "v_exp_f32 v117, v117\n"
    "v_exp_f32 v118, v118\n"
    "v_exp_f32 v119, v119\n"
    "v_exp_f32 v120, v120\n"
    "v_exp_f32 v121, v121\n"
    "v_exp_f32 v122, v122\n"
    "v_smfmac_f32_32x32x32_bf16 v[144:159], v[48:51], v[112:119], %[idx]\n"
    "ds_read_b128 v[48:51], %[va0] offset:2112\n"
    "v_exp_f32 v123, v123\n"
    "v_exp_f32 v124, v124\n"
    "v_exp_f32 v125, v125\n"
    "v_exp_f32 v126, v126\n"
    "v_exp_f32 v127, v127\n"
    "v_exp_f32 v96, v96\n"
    "v_exp_f32 v97, v97\n"
    "v_smfmac_f32_32x32x32_bf16 v[144:159], v[52:55], v[120:127], %[idx]\n"
    "ds_read_b128 v[52:55], %[va1] offset:2112\n"
    "v_exp_f32 v98, v98\n"
    "v_exp_f32 v99, v99\n"
    "v_exp_f32 v100, v100\n"
    "v_mfma_f32_32x32x16_bf16 v[112:127], v[64:67], v[92:95], v[176:191]\n"
    "s_waitcnt lgkmcnt(1)\n"
    "v_exp_f32 v101, v101\n"
    "v_exp_f32 v102, v102\n"
    "v_exp_f32 v103, v103\n"
    "v_exp_f32 v104, v104\n"
    "v_exp_f32 v105, v105\n"
    "v_exp_f32 v106, v106\n"
    "v_smfmac_f32_32x32x32_bf16 v[128:143], v[48:51], v[96:103], %[idx]\n"
    "s_add_u32 %[kv], %[kv], 0x1000\n"
    "buffer_load_dwordx4 v[64:67], %[l16], %[rs], %[kv] offen offset:0 sc1\n"
    "v_exp_f32 v107, v107\n"
    "v_exp_f32 v108, v108\n"
    "v_exp_f32 v109, v109\n"
    "v_exp_f32 v110, v110\n"
    "v_exp_f32 v111, v111\n"
    "v_exp_f32 v112, v112\n"
    "v_exp_f32 v113, v113\n"
    "s_waitcnt vmcnt(9) lgkmcnt(0)\n"
    "v_smfmac_f32_32x32x32_bf16 v[128:143], v[52:55], v[104:111], %[idx]\n"
    "s_add_u32 m0, %[ring], 0x440\n"
    "v_exp_f32 v114, v114\n"
    "v_exp_f32 v115, v115\n"
    "v_exp_f32 v116, v116\n"
    "v_mfma_f32_32x32x16_bf16 v[96:111], v[68:71], v[88:91], v[160:175]\n"
    "buffer_load_dwordx4 %[l16], %[rs], %[kv] offen offset:1024 sc1 lds\n"
    "v_exp_f32 v117, v117\n"
    "v_exp_f32 v118, v118\n"
    "v_exp_f32 v119, v119\n"
    "v_exp_f32 v120, v120\n"
    "v_exp_f32 v121, v121\n"
    "v_exp_f32 v122, v122\n"
    "v_smfmac_f32_32x32x32_bf16 v[144:159], v[48:51], v[112:119], %[idx]\n"
    "ds_read_b128 v[48:51], %[va0] offset:3168\n"
    "v_exp_f32 v123, v123\n"
    "v_exp_f32 v124, v124\n"
    "v_exp_f32 v125, v125\n"
    "v_exp_f32 v126, v126\n"
    "v_exp_f32 v127, v127\n"
    "s_mov_b32 %[cnt], 0x10000\n"
    "7:\n"
    "v_cmp_ne_u32 vcc, %[tag], v192\n"
    "v_cmp_ne_u32 %[t2], %[tag], v193\n"
    "s_or_b64 vcc, vcc, %[t2]\n"
    "v_cmp_ne_u32 %[t2], %[tag], v194\n"
    "s_or_b64 vcc, vcc, %[t2]\n"
    "v_cmp_ne_u32 %[t2], %[tag], v195\n"
    "s_or_b64 vcc, vcc, %[t2]\n"
    "s_cbranch_scc0 8f\n"
    "s_sub_u32 %[cnt], %[cnt], 1\n"
    "s_cbranch_scc1 8f\n"
    "s_sleep 1\n"
    "buffer_load_dwordx4 v[192:195], %[l16], %[rs], %[fr] offen sc1\n"
    "s_waitcnt vmcnt(0)\n"
    "s_branch 7b\n"
    "8:\n"
    "v_exp_f32 v96, v96\n"
    "v_exp_f32 v97, v97\n"
    "v_smfmac_f32_32x32x32_bf16 v[144:159], v[52:55], v[120:127], %[idx]\n"
    "s_add_u32 m0, %[pl], 0x0\n"
    "ds_read_b128 v[52:55], %[va1] offset:3168\n"
    "v_exp_f32 v98, v98\n"
    "v_exp_f32 v99, v99\n"
    "v_exp_f32 v100, v100\n"
    "v_mfma_f32_32x32x16_bf16 v[112:127], v[68:71], v[92:95], v[176:191]\n"
    "buffer_load_dwordx4 %[l16], %[rs], %[w13] offen offset:0 sc1 lds\n"
    "s_waitcnt lgkmcnt(1)\n"
    "v_exp_f32 v101, v101\n"
    "v_exp_f32 v102, v102\n"
    "v_exp_f32 v103, v103\n"
    "v_exp_f32 v104, v104\n"
    "v_exp_f32 v105, v105\n"
    "v_exp_f32 v106, v106\n"
    "v_smfmac_f32_32x32x32_bf16 v[128:143], v[48:51], v[96:103], %[idx]\n"
    "buffer_load_dwordx4 v[68:71], %[l16], %[rs], %[kv] offen offset:2048 sc1\n"
    "v_exp_f32 v107, v107\n"
    "v_exp_f32 v108, v108\n"
    "v_exp_f32 v109, v109\n"
    "v_exp_f32 v110, v110\n"
    "v_exp_f32 v111, v111\n"
    "v_exp_f32 v112, v112\n"
    "v_exp_f32 v113, v113\n"
    "s_waitcnt vmcnt(10) lgkmcnt(0)\n"
    "v_smfmac_f32_32x32x32_bf16 v[128:143], v[52:55], v[104:111], %[idx]\n"
    "s_add_u32 m0, %[ring], 0x60\n"
    "buffer_load_dwordx4 v[192:195], %[lwo], %[rs], %[wo] offen offset:0 sc1\n"
    "v_exp_f32 v114, v114\n"
    "v_exp_f32 v115, v115\n"
    "v_exp_f32 v116, v116\n"
    "v_mfma_f32_32x32x16_bf16 v[96:111], v[72:75], v[88:91], v[160:175]\n"
    "buffer_load_dwordx4 %[l16], %[rs], %[kv] offen offset:3072 sc1 lds\n"
    "v_exp_f32 v117, v117\n"
    "v_exp_f32 v118, v118\n"
    "v_exp_f32 v119, v119\n"
    "v_exp_f32 v120, v120\n"
    "v_exp_f32 v121, v121\n"
    "v_exp_f32 v122, v122\n"
    "v_smfmac_f32_32x32x32_bf16 v[144:159], v[48:51], v[112:119], %[idx]\n"
    "ds_read_b128 v[48:51], %[va0] offset:4224\n"
    "v_exp_f32 v123, v123\n"
    "v_exp_f32 v124, v124\n"
    "v_exp_f32 v125, v125\n"
    "v_exp_f32 v126, v126\n"
    "v_exp_f32 v127, v127\n"
    "v_exp_f32 v96, v96\n"
    "v_exp_f32 v97, v97\n"
    "v_smfmac_f32_32x32x32_bf16 v[144:159], v[52:55], v[120:127], %[idx]\n"
    "s_add_u32 m0, %[pl], 0x0\n"
    "ds_read_b128 v[52:55], %[va1] offset:4224\n"
    "v_exp_f32 v98, v98\n"
    "v_exp_f32 v99, v99\n"
    "v_exp_f32 v100, v100\n"
    "v_mfma_f32_32x32x16_bf16 v[112:127], v[72:75], v[92:95], v[176:191]\n"
    "buffer_load_dwordx4 %[l16], %[rs], %[w13] offen offset:1024 sc1 lds\n"
    "s_waitcnt lgkmcnt(1)\n"
    "v_exp_f32 v101, v101\n"
    "v_exp_f32 v102, v102\n"
    "v_exp_f32 v103, v103\n"
    "v_exp_f32 v104, v104\n"
    "v_exp_f32 v105, v105\n"
    "v_exp_f32 v106, v106\n"
    "v_smfmac_f32_32x32x32_bf16 v[128:143], v[48:51], v[96:103], %[idx]\n"
    "s_add_u32 %[kv], %[kv], 0x1000\n"
    "buffer_load_dwordx4 v[72:75], %[l16], %[rs], %[kv] offen offset:0 sc1\n"
    "v_exp_f32 v107, v107\n"
    "v_exp_f32 v108, v108\n"
    "v_exp_f32 v109, v109\n"
    "v_exp_f32 v110, v110\n"
    "v_exp_f32 v111, v111\n"
    "v_exp_f32 v112, v112\n"
    "v_exp_f32 v113, v113\n"
    "s_waitcnt vmcnt(12) lgkmcnt(0)\n"
    "v_smfmac_f32_32x32x32_bf16 v[128:143], v[52:55], v[104:111], %[idx]\n"
    "s_add_u32 m0, %[ring], 0xc80\n"
    "buffer_load_dwordx4 v[196:199], %[lwo], %[rs], %[wo] offen offset:1024 sc1\n"
    "v_exp_f32 v114, v114\n"
    "v_exp_f32 v115, v115\n"
    "v_exp_f32 v116, v116\n"
    "v_mfma_f32_32x32x16_bf16 v[96:111], v[76:79], v[88:91], v[160:175]\n"
    "buffer_load_dwordx4 %[l16], %[rs], %[kv] offen offset:1024 sc1 lds\n"
    "v_exp_f32 v117, v117\n"
    "v_exp_f32 v118, v118\n"
    "v_exp_f32 v119, v119\n"
    "v_exp_f32 v120, v120\n"
    "v_exp_f32 v121, v121\n"
    "v_exp_f32 v122, v122\n"
    "v_smfmac_f32_32x32x32_bf16 v[144:159], v[48:51], v[112:119], %[idx]\n"
    "ds_read_b128 v[48:51], %[va0] offset:5280\n"
    "v_exp_f32 v123, v123\n"
    "v_exp_f32 v124, v124\n"
    "v_exp_f32 v125, v125\n"
    "v_exp_f32 v126, v126\n"
    "v_exp_f32 v127, v127\n"
    "v_exp_f32 v96, v96\n"
    "v_exp_f32 v97, v97\n"
    "v_smfmac_f32_32x32x32_bf16 v[144:159], v[52:55], v[120:127], %[idx]\n"
    "s_add_u32 m0, %[pl], 0x0\n"
    "ds_read_b128 v[52:55], %[va1] offset:5280\n"
    "v_exp_f32 v98, v98\n"
    "v_exp_f32 v99, v99\n"
    "v_exp_f32 v100, v100\n"
    "v_mfma_f32_32x32x16_bf16 v[112:127], v[76:79], v[92:95], v[176:191]\n"
    "buffer_load_dwordx4 %[l16], %[rs], %[w13] offen offset:2048 sc1 lds\n"
    "s_waitcnt lgkmcnt(1)\n"
    "v_exp_f32 v101, v101\n"
    "v_exp_f32 v102, v102\n"
    "v_exp_f32 v103, v103\n"
    "v_exp_f32 v104, v104\n"
    "v_exp_f32 v105, v105\n"
    "v_exp_f32 v106, v106\n"
    "v_smfmac_f32_32x32x32_bf16 v[128:143], v[48:51], v[96:103], %[idx]\n"
    "buffer_load_dwordx4 v[76:79], %[l16], %[rs], %[kv] offen offset:2048 sc1\n"
    "v_exp_f32 v107, v107\n"
    "v_exp_f32 v108, v108\n"
    "v_exp_f32 v109, v109\n"
    "v_exp_f32 v110, v110\n"
    "v_exp_f32 v111, v111\n"
    "v_exp_f32 v112, v112\n"
    "v_exp_f32 v113, v113\n"
    "s_waitcnt vmcnt(14) lgkmcnt(0)\n"
    "v_smfmac_f32_32x32x32_bf16 v[128:143], v[52:55], v[104:111], %[idx]\n"
    "s_add_u32 m0, %[ring], 0x8a0\n"
    "buffer_load_dwordx4 v[200:203], %[lwo], %[rs], %[wo] offen offset:2048 sc1\n"
    "v_exp_f32 v114, v114\n"
    "v_exp_f32 v115, v115\n"
    "v_exp_f32 v116, v116\n"
    "v_mfma_f32_32x32x16_bf16 v[96:111], v[56:59], v[88:91], v[160:175]\n"
    "buffer_load_dwordx4 %[l16], %[rs], %[kv] offen offset:3072 sc1 lds\n"
    "v_exp_f32 v117, v117\n"
    "v_exp_f32 v118, v118\n"
    "v_exp_f32 v119, v119\n"
    "v_exp_f32 v120, v120\n"
    "v_exp_f32 v121, v121\n"
    "v_exp_f32 v122, v122\n"
    "v_smfmac_f32_32x32x32_bf16 v[144:159], v[48:51], v[112:119], %[idx]\n"
    "ds_read_b128 v[48:51], %[va0] offset:0\n"
    "v_exp_f32 v123, v123\n"
    "v_exp_f32 v124, v124\n"
    "v_exp_f32 v125, v125\n"
    "v_exp_f32 v126, v126\n"
    "v_exp_f32 v127, v127\n"
    "v_exp_f32 v96, v96\n"
    "v_exp_f32 v97, v97\n"
    "v_smfmac_f32_32x32x32_bf16 v[144:159], v[52:55], v[120:127], %[idx]\n"
    "ds_read_b128 v[52:55], %[va1] offset:0\n"
    "v_exp_f32 v98, v98\n"
    "v_exp_f32 v99, v99\n"
    "v_exp_f32 v100, v100\n"
    "v_mfma_f32_32x32x16_bf16 v[112:127], v[56:59], v[92:95], v[176:191]\n"
    "s_waitcnt lgkmcnt(1)\n"
    "v_exp_f32 v101, v101\n"
    "v_exp_f32 v102, v102\n"
    "v_exp_f32 v103, v103\n"
    "v_exp_f32 v104, v104\n"
    "v_exp_f32 v105, v105\n"
    "v_exp_f32 v106, v106\n"
    "v_smfmac_f32_32x32x32_bf16 v[128:143], v[48:51], v[96:103], %[idx]\n"
    "s_add_u32 %[kv], %[kv], 0x1000\n"
    "buffer_load_dwordx4 v[56:59], %[l16], %[rs], %[kv] offen offset:0 sc1\n"
    "v_exp_f32 v107, v107\n"
    "v_exp_f32 v108, v108\n"
    "v_exp_f32 v109, v109\n"
    "v_exp_f32 v110, v110\n"
    "v_exp_f32 v111, v111\n"
    "v_exp_f32 v112, v112\n"
    "v_exp_f32 v113, v113\n"
    "s_waitcnt vmcnt(15) lgkmcnt(0)\n"
    "v_smfmac_f32_32x32x32_bf16 v[128:143], v[52:55], v[104:111], %[idx]\n"
    "s_add_u32 m0, %[ring], 0xfffffc00\n"
    "buffer_load_dwordx4 v[204:207], %[lwo], %[rs], %[wo] offen offset:3072 sc1\n"
    "v_exp_f32 v114, v114\n"
    "v_exp_f32 v115, v115\n"
    "v_exp_f32 v116, v116\n"
    "v_mfma_f32_32x32x16_bf16 v[96:111], v[60:63], v[88:91], v[160:175]\n"
    "buffer_load_dwordx4 %[l16], %[rs], %[kv] offen offset:1024 sc1 lds\n"
    "v_exp_f32 v117, v117\n"
    "v_exp_f32 v118, v118\n"
    "v_exp_f32 v119, v119\n"
    "v_exp_f32 v120, v120\n"
    "v_exp_f32 v121, v121\n"
    "v_exp_f32 v122, v122\n"
    "v_smfmac_f32_32x32x32_bf16 v[144:159], v[48:51], v[112:119], %[idx]\n"
    "ds_read_b128 v[48:51], %[va0] offset:1056\n"
    "v_exp_f32 v123, v123\n"
    "v_exp_f32 v124, v124\n"
    "v_exp_f32 v125, v125\n"
    "v_exp_f32 v126, v126\n"
    "v_exp_f32 v127, v127\n"
    "v_exp_f32 v96, v96\n"
    "v_exp_f32 v97, v97\n"
    "v_smfmac_f32_32x32x32_bf16 v[144:159], v[52:55], v[120:127], %[idx]\n"
    "s_add_u32 m0, %[pl], 0x0\n"
    "ds_read_b128 v[52:55], %[va1] offset:1056\n"
    "v_exp_f32 v98, v98\n"
    "v_exp_f32 v99, v99\n"
    "v_exp_f32 v100, v100\n"
    "v_mfma_f32_32x32x16_bf16 v[112:127], v[60:63], v[92:95], v[176:191]\n"
    "buffer_load_dwordx4 %[l16], %[rs], %[w13] offen offset:3072 sc1 lds\n"
    "s_waitcnt lgkmcnt(1)\n"
    "v_exp_f32 v101, v101\n"
    "v_exp_f32 v102, v102\n"
    "v_exp_f32 v103, v103\n"
    "v_exp_f32 v104, v104\n"
    "v_exp_f32 v105, v105\n"
    "v_exp_f32 v106, v106\n"
    "v_smfmac_f32_32x32x32_bf16 v[128:143], v[48:51], v[96:103], %[idx]\n"
    "buffer_load_dwordx4 v[60:63], %[l16], %[rs], %[kv] offen offset:2048 sc1\n"
    "v_exp_f32 v107, v107\n"
    "v_exp_f32 v108, v108\n"
    "v_exp_f32 v109, v109\n"
    "v_exp_f32 v110, v110\n"
    "v_exp_f32 v111, v111\n"
    "v_exp_f32 v112, v112\n"
    "v_exp_f32 v113, v113\n"
    "s_waitcnt vmcnt(17) lgkmcnt(0)\n"
    "v_smfmac_f32_32x32x32_bf16 v[128:143], v[52:55], v[104:111], %[idx]\n"
    "s_add_u32 m0, %[ring], 0xfffff820\n"
    "s_add_u32 %[wo], %[wo], 0x1000\n"
    "buffer_load_dwordx4 v[208:211], %[lwo], %[rs], %[wo] offen offset:0 sc1\n"
    "v_exp_f32 v114, v114\n"
    "v_exp_f32 v115, v115\n"
    "v_exp_f32 v116, v116\n"
    "v_mfma_f32_32x32x16_bf16 v[96:111], v[64:67], v[88:91], v[160:175]\n"
    "buffer_load_dwordx4 %[l16], %[rs], %[kv] offen offset:3072 sc1 lds\n"
    "v_exp_f32 v117, v117\n"
    "v_exp_f32 v118, v118\n"
    "v_exp_f32 v119, v119\n"
    "v_exp_f32 v120, v120\n"
    "v_exp_f32 v121, v121\n"
    "v_exp_f32 v122, v122\n"
    "v_smfmac_f32_32x32x32_bf16 v[144:159], v[48:51], v[112:119], %[idx]\n"
    "ds_read_b128 v[48:51], %[va0] offset:2112\n"
    "v_exp_f32 v123, v123\n"
    "v_exp_f32 v124, v124\n"
    "v_exp_f32 v125, v125\n"
    "v_exp_f32 v126, v126\n"
    "v_exp_f32 v127, v127\n"
    "v_exp_f32 v96, v96\n"
    "v_exp_f32 v97, v97\n"
    "v_smfmac_f32_32x32x32_bf16 v[144:159], v[52:55], v[120:127], %[idx]\n"
    "s_add_u32 %[w13], %[w13], 0x1000\n"
    "s_add_u32 m0, %[pl], 0x1000\n"
    "ds_read_b128 v[52:55], %[va1] offset:2112\n"
    "v_exp_f32 v98, v98\n"
    "v_exp_f32 v99, v99\n"
    "v_exp_f32 v100, v100\n"
    "v_mfma_f32_32x32x16_bf16 v[112:127], v[64:67], v[92:95], v[176:191]\n"
    "buffer_load_dwordx4 %[l16], %[rs], %[w13] offen offset:0 sc1 lds\n"
    "s_waitcnt lgkmcnt(1)\n"
    "v_exp_f32 v101, v101\n"
    "v_exp_f32 v102, v102\n"
    "v_exp_f32 v103, v103\n"
    "v_exp_f32 v104, v104\n"
    "v_exp_f32 v105, v105\n"
    "v_exp_f32 v106, v106\n"
    "v_smfmac_f32_32x32x32_bf16 v[128:143], v[48:51], v[96:103], %[idx]\n"
    "s_add_u32 %[kv], %[kv], 0x1000\n"
    "buffer_load_dwordx4 v[64:67], %[l16], %[rs], %[kv] offen offset:0 sc1\n"
    "v_exp_f32 v107, v107\n"
    "v_exp_f32 v108, v108\n"
    "v_exp_f32 v109, v109\n"
    "v_exp_f32 v110, v110\n"
    "v_exp_f32 v111, v111\n"
    "v_exp_f32 v112, v112\n"
    "v_exp_f32 v113, v113\n"
    "s_waitcnt vmcnt(17) lgkmcnt(0)\n"
    "v_smfmac_f32_32x32x32_bf16 v[128:143], v[52:55], v[104:111], %[idx]\n"
    "s_add_u32 m0, %[ring], 0x440\n"
    "buffer_load_dwordx4 v[212:215], %[lwo], %[rs], %[wo] offen offset:1024 sc1\n"
    "v_exp_f32 v114, v114\n"
    "v_exp_f32 v115, v115\n"
    "v_exp_f32 v116, v116\n"
    "v_mfma_f32_32x32x16_bf16 v[96:111], v[68:71], v[88:91], v[160:175]\n"
    "buffer_load_dwordx4 %[l16], %[rs], %[kv] offen offset:1024 sc1 lds\n"
    "v_exp_f32 v117, v117\n"
    "v_exp_f32 v118, v118\n"
    "v_exp_f32 v119, v119\n"
    "v_exp_f32 v120, v120\n"
    "v_exp_f32 v121, v121\n"
    "v_exp_f32 v122, v122\n"
    "v_smfmac_f32_32x32x32_bf16 v[144:159], v[48:51], v[112:119], %[idx]\n"
    "ds_read_b128 v[48:51], %[va0] offset:3168\n"
    "v_exp_f32 v123, v123\n"
    "v_exp_f32 v124, v124\n"
    "v_exp_f32 v125, v125\n"
    "v_exp_f32 v126, v126\n"
    "v_exp_f32 v127, v127\n"
    "v_exp_f32 v96, v96\n"
    "v_exp_f32 v97, v97\n"
    "v_smfmac_f32_32x32x32_bf16 v[144:159], v[52:55], v[120:127], %[idx]\n"
    "s_add_u32 m0, %[pl], 0x1000\n"
    "ds_read_b128 v[52:55], %[va1] offset:3168\n"
    "v_exp_f32 v98, v98\n"
    "v_exp_f32 v99, v99\n"
    "v_exp_f32 v100, v100\n"
    "v_mfma_f32_32x32x16_bf16 v[112:127], v[68:71], v[92:95], v[176:191]\n"
    "buffer_load_dwordx4 %[l16], %[rs], %[w13] offen offset:1024 sc1 lds\n"
    "s_waitcnt lgkmcnt(1)\n"
    "v_exp_f32 v101, v101\n"
    "v_exp_f32 v102, v102\n"
    "v_exp_f32 v103, v103\n"
    "v_exp_f32 v104, v104\n"
    "v_exp_f32 v105, v105\n"
    "v_exp_f32 v106, v106\n"
    "v_smfmac_f32_32x32x32_bf16 v[128:143], v[48:51], v[96:103], %[idx]\n"
    "buffer_load_dwordx4 v[68:71], %[l16], %[rs], %[kv] offen offset:2048 sc1\n"
    "v_exp_f32 v107, v107\n"
    "v_exp_f32 v108, v108\n"
    "v_exp_f32 v109, v109\n"
    "v_exp_f32 v110, v110\n"
    "v_exp_f32 v111, v111\n"
    "v_exp_f32 v112, v112\n"
    "v_exp_f32 v113, v113\n"
    "s_waitcnt vmcnt(17) lgkmcnt(0)\n"
    "v_smfmac_f32_32x32x32_bf16 v[128:143], v[52:55], v[104:111], %[idx]\n"
    "s_add_u32 m0, %[ring], 0x60\n"
    "buffer_load_dwordx4 v[216:219], %[lwo], %[rs], %[wo] offen offset:2048 sc1\n"
    "v_exp_f32 v114, v114\n"
    "v_exp_f32 v115, v115\n"
    "v_exp_f32 v116, v116\n"
    "v_mfma_f32_32x32x16_bf16 v[96:111], v[72:75], v[88:91], v[160:175]\n"
    "buffer_load_dwordx4 %[l16], %[rs], %[kv] offen offset:3072 sc1 lds\n"
    "v_exp_f32 v117, v117\n"
    "v_exp_f32 v118, v118\n"
    "v_exp_f32 v119, v119\n"
    "v_exp_f32 v120, v120\n"
    "v_exp_f32 v121, v121\n"
    "v_exp_f32 v122, v122\n"
    "v_smfmac_f32_32x32x32_bf16 v[144:159], v[48:51], v[112:119], %[idx]\n"
    "ds_read_b128 v[48:51], %[va0] offset:4224\n"
    "v_exp_f32 v123, v123\n"
    "v_exp_f32 v124, v124\n"
    "v_exp_f32 v125, v125\n"
    "v_exp_f32 v126, v126\n"
    "v_exp_f32 v127, v127\n"
    "v_exp_f32 v96, v96\n"
    "v_exp_f32 v97, v97\n"
    "v_smfmac_f32_32x32x32_bf16 v[144:159], v[52:55], v[120:127], %[idx]\n"
    "ds_read_b128 v[52:55], %[va1] offset:4224\n"
    "v_exp_f32 v98, v98\n"
    "v_exp_f32 v99, v99\n"
    "v_exp_f32 v100, v100\n"
    "v_mfma_f32_32x32x16_bf16 v[112:127], v[72:75], v[92:95], v[176:191]\n"
    "s_waitcnt lgkmcnt(1)\n"
    "v_exp_f32 v101, v101\n"
    "v_exp_f32 v102, v102\n"
    "v_exp_f32 v103, v103\n"
    "v_exp_f32 v104, v104\n"
    "v_exp_f32 v105, v105\n"
    "v_exp_f32 v106, v106\n"
    "v_smfmac_f32_32x32x32_bf16 v[128:143], v[48:51], v[96:103], %[idx]\n"
    "s_add_u32 %[kv], %[kv], 0x1000\n"
    "buffer_load_dwordx4 v[72:75], %[l16], %[rs], %[kv] offen offset:0 sc1\n"
    "v_exp_f32 v107, v107\n"
    "v_exp_f32 v108, v108\n"
    "v_exp_f32 v109, v109\n"
    "v_exp_f32 v110, v110\n"
    "v_exp_f32 v111, v111\n"
    "v_exp_f32 v112, v112\n"
    "v_exp_f32 v113, v113\n"
    "s_waitcnt vmcnt(16) lgkmcnt(0)\n"
    "v_smfmac_f32_32x32x32_bf16 v[128:143], v[52:55], v[104:111], %[idx]\n"
    "s_add_u32 m0, %[ring], 0xc80\n"
    "buffer_load_dwordx4 v[220:223], %[lwo], %[rs], %[wo] offen offset:3072 sc1\n"
    "v_exp_f32 v114, v114\n"
    "v_exp_f32 v115, v115\n"
    "v_exp_f32 v116, v116\n"
    "v_mfma_f32_32x32x16_bf16 v[96:111], v[76:79], v[88:91], v[160:175]\n"
    "buffer_load_dwordx4 %[l16], %[rs], %[kv] offen offset:1024 sc1 lds\n"
    "v_exp_f32 v117, v117\n"
    "v_exp_f32 v118, v118\n"
    "v_exp_f32 v119, v119\n"
    "v_exp_f32 v120, v120\n"
    "v_exp_f32 v121, v121\n"
    "v_exp_f32 v122, v122\n"
    "v_smfmac_f32_32x32x32_bf16 v[144:159], v[48:51], v[112:119], %[idx]\n"
    "ds_read_b128 v[48:51], %[va0] offset:5280\n"
    "v_exp_f32 v123, v123\n"
    "v_exp_f32 v124, v124\n"
    "v_exp_f32 v125, v125\n"
    "v_exp_f32 v126, v126\n"
    "v_exp_f32 v127, v127\n"
    "v_exp_f32 v96, v96\n"
    "v_exp_f32 v97, v97\n"
    "v_smfmac_f32_32x32x32_bf16 v[144:159], v[52:55], v[120:127], %[idx]\n"
    "s_add_u32 m0, %[pl], 0x1000\n"
    "ds_read_b128 v[52:55], %[va1] offset:5280\n"
    "v_exp_f32 v98, v98\n"
    "v_exp_f32 v99, v99\n"
    "v_exp_f32 v100, v100\n"
    "v_mfma_f32_32x32x16_bf16 v[112:127], v[76:79], v[92:95], v[176:191]\n"
    "buffer_load_dwordx4 %[l16], %[rs], %[w13] offen offset:2048 sc1 lds\n"
    "s_waitcnt lgkmcnt(1)\n"
    "v_exp_f32 v101, v101\n"
    "v_exp_f32 v102, v102\n"
    "v_exp_f32 v103, v103\n"
    "v_exp_f32 v104, v104\n"
    "v_exp_f32 v105, v105\n"
    "v_exp_f32 v106, v106\n"
    "v_smfmac_f32_32x32x32_bf16 v[128:143], v[48:51], v[96:103], %[idx]\n"
    "buffer_load_dwordx4 v[76:79], %[l16], %[rs], %[kv] offen offset:2048 sc1\n"
    "v_exp_f32 v107, v107\n"
    "v_exp_f32 v108, v108\n"
    "v_exp_f32 v109, v109\n"
    "v_exp_f32 v110, v110\n"
    "v_exp_f32 v111, v111\n"
    "v_exp_f32 v112, v112\n"
    "v_exp_f32 v113, v113\n"
    "s_waitcnt vmcnt(17) lgkmcnt(0)\n"
    "v_smfmac_f32_32x32x32_bf16 v[128:143], v[52:55], v[104:111], %[idx]\n"
    "s_add_u32 m0, %[ring], 0x8a0\n"
    "s_add_u32 %[wo], %[wo], 0x1000\n"
    "buffer_load_dwordx4 v[224:227], %[lwo], %[rs], %[wo] offen offset:0 sc1\n"
    "v_exp_f32 v114, v114\n"
    "v_exp_f32 v115, v115\n"
    "v_exp_f32 v116, v116\n"
    "v_mfma_f32_32x32x16_bf16 v[96:111], v[56:59], v[88:91], v[160:175]\n"
    "buffer_load_dwordx4 %[l16], %[rs], %[kv] offen offset:3072 sc1 lds\n"
    "v_exp_f32 v117, v117\n"
    "v_exp_f32 v118, v118\n"
    "v_exp_f32 v119, v119\n"
    "v_exp_f32 v120, v120\n"
    "v_exp_f32 v121, v121\n"
    "v_exp_f32 v122, v122\n"
    "v_smfmac_f32_32x32x32_bf16 v[144:159], v[48:51], v[112:119], %[idx]\n"
    "ds_read_b128 v[48:51], %[va0] offset:0\n"
    "v_exp_f32 v123, v123\n"
    "v_exp_f32 v124, v124\n"
    "v_exp_f32 v125, v125\n"
    "v_exp_f32 v126, v126\n"
    "v_exp_f32 v127, v127\n"
    "v_exp_f32 v96, v96\n"
    "v_exp_f32 v97, v97\n"
    "v_smfmac_f32_32x32x32_bf16 v[144:159], v[52:55], v[120:127], %[idx]\n"
    "s_add_u32 m0, %[pl], 0x1000\n"
    "ds_read_b128 v[52:55], %[va1] offset:0\n"
    "v_exp_f32 v98, v98\n"
    "v_exp_f32 v99, v99\n"
    "v_exp_f32 v100, v100\n"
    "v_mfma_f32_32x32x16_bf16 v[112:127], v[56:59], v[92:95], v[176:191]\n"
    "buffer_load_dwordx4 %[l16], %[rs], %[w13] offen offset:3072 sc1 lds\n"
    "s_waitcnt lgkmcnt(1)\n"
    "v_exp_f32 v101, v101\n"
    "v_exp_f32 v102, v102\n"
    "v_exp_f32 v103, v103\n"
    "v_exp_f32 v104, v104\n"
    "v_exp_f32 v105, v105\n"
    "v_exp_f32 v106, v106\n"
    "v_smfmac_f32_32x32x32_bf16 v[128:143], v[48:51], v[96:103], %[idx]\n"
    "s_add_u32 %[kv], %[kv], 0x1000\n"
    "buffer_load_dwordx4 v[56:59], %[l16], %[rs], %[kv] offen offset:0 sc1\n"
    "v_exp_f32 v107, v107\n"
    "v_exp_f32 v108, v108\n"
    "v_exp_f32 v109, v109\n"
    "v_exp_f32 v110, v110\n"
    "v_exp_f32 v111, v111\n"
    "v_exp_f32 v112, v112\n"
    "v_exp_f32 v113, v113\n"
    "s_waitcnt vmcnt(17) lgkmcnt(0)\n"
    "v_smfmac_f32_32x32x32_bf16 v[128:143], v[52:55], v[104:111], %[idx]\n"
    "s_add_u32 m0, %[ring], 0xfffffc00\n"
    "buffer_load_dwordx4 v[228:231], %[lwo], %[rs], %[wo] offen offset:1024 sc1\n"
    "v_exp_f32 v114, v114\n"
    "v_exp_f32 v115, v115\n"
    "v_exp_f32 v116, v116\n"
    "v_mfma_f32_32x32x16_bf16 v[96:111], v[60:63], v[88:91], v[160:175]\n"
    "buffer_load_dwordx4 %[l16], %[rs], %[kv] offen offset:1024 sc1 lds\n"
    "v_exp_f32 v117, v117\n"
    "v_exp_f32 v118, v118\n"
    "v_exp_f32 v119, v119\n"
    "v_exp_f32 v120, v120\n"
    "v_exp_f32 v121, v121\n"
    "v_exp_f32 v122, v122\n"
    "v_smfmac_f32_32x32x32_bf16 v[144:159], v[48:51], v[112:119], %[idx]\n"
    "ds_read_b128 v[48:51], %[va0] offset:1056\n"
    "v_exp_f32 v123, v123\n"
    "v_exp_f32 v124, v124\n"
    "v_exp_f32 v125, v125\n"
    "v_exp_f32 v126, v126\n"
    "v_exp_f32 v127, v127\n"
    "v_exp_f32 v96, v96\n"
    "v_exp_f32 v97, v97\n"
    "v_smfmac_f32_32x32x32_bf16 v[144:159], v[52:55], v[120:127], %[idx]\n"
    "s_add_u32 %[w13], %[w13], 0x1000\n"
    "s_add_u32 m0, %[pl], 0x2000\n"
    "ds_read_b128 v[52:55], %[va1] offset:1056\n"
    "v_exp_f32 v98, v98\n"
    "v_exp_f32 v99, v99\n"
    "v_exp_f32 v100, v100\n"
    "v_mfma_f32_32x32x16_bf16 v[112:127], v[60:63], v[92:95], v[176:191]\n"
    "buffer_load_dwordx4 %[l16], %[rs], %[w13] offen offset:0 sc1 lds\n"
    "s_waitcnt lgkmcnt(1)\n"
    "v_exp_f32 v101, v101\n"
    "v_exp_f32 v102, v102\n"
    "v_exp_f32 v103, v103\n"
    "v_exp_f32 v104, v104\n"
    "v_exp_f32 v105, v105\n"
    "v_exp_f32 v106, v106\n"
    "v_smfmac_f32_32x32x32_bf16 v[128:143], v[48:51], v[96:103], %[idx]\n"
    "buffer_load_dwordx4 v[60:63], %[l16], %[rs], %[kv] offen offset:2048 sc1\n"
    "v_exp_f32 v107, v107\n"
    "v_exp_f32 v108, v108\n"
    "v_exp_f32 v109, v109\n"
    "v_exp_f32 v110, v110\n"
    "v_exp_f32 v111, v111\n"
    "v_exp_f32 v112, v112\n"
    "v_exp_f32 v113, v113\n"
    "s_waitcnt vmcnt(17) lgkmcnt(0)\n"
    "v_smfmac_f32_32x32x32_bf16 v[128:143], v[52:55], v[104:111], %[idx]\n"
    "s_add_u32 m0, %[ring], 0xfffff820\n"
    "buffer_load_dwordx4 v[232:235], %[lwo], %[rs], %[wo] offen offset:2048 sc1\n"
    "v_exp_f32 v114, v114\n"
    "v_exp_f32 v115, v115\n"
    "v_exp_f32 v116, v116\n"
    "v_mfma_f32_32x32x16_bf16 v[96:111], v[64:67], v[88:91], v[160:175]\n"
    "buffer_load_dwordx4 %[l16], %[rs], %[kv] offen offset:3072 sc1 lds\n"
    "v_exp_f32 v117, v117\n"
    "v_exp_f32 v118, v118\n"
    "v_exp_f32 v119, v119\n"
    "v_exp_f32 v120, v120\n"
    "v_exp_f32 v121, v121\n"
    "v_exp_f32 v122, v122\n"
    "v_smfmac_f32_32x32x32_bf16 v[144:159], v[48:51], v[112:119], %[idx]\n"
    "ds_read_b128 v[48:51], %[va0] offset:2112\n"
    "v_exp_f32 v123, v123\n"
    "v_exp_f32 v124, v124\n"
    "v_exp_f32 v125, v125\n"
    "v_exp_f32 v126, v126\n"
    "v_exp_f32 v127, v127\n"
    "v_exp_f32 v96, v96\n"
    "v_exp_f32 v97, v97\n"
    "v_smfmac_f32_32x32x32_bf16 v[144:159], v[52:55], v[120:127], %[idx]\n"
    "ds_read_b128 v[52:55], %[va1] offset:2112\n"
    "v_exp_f32 v98, v98\n"
    "v_exp_f32 v99, v99\n"
    "v_exp_f32 v100, v100\n"
    "v_mfma_f32_32x32x16_bf16 v[112:127], v[64:67], v[92:95], v[176:191]\n"
    "s_waitcnt lgkmcnt(1)\n"
    "v_exp_f32 v101, v101\n"
    "v_exp_f32 v102, v102\n"
    "v_exp_f32 v103, v103\n"
    "v_exp_f32 v104, v104\n"
    "v_exp_f32 v105, v105\n"
    "v_exp_f32 v106, v106\n"
    "v_smfmac_f32_32x32x32_bf16 v[128:143], v[48:51], v[96:103], %[idx]\n"
    "s_add_u32 %[kv], %[kv], 0x1000\n"
    "buffer_load_dwordx4 v[64:67], %[l16], %[rs], %[kv] offen offset:0 sc1\n"
    "v_exp_f32 v107, v107\n"
    "v_exp_f32 v108, v108\n"
    "v_exp_f32 v109, v109\n"
    "v_exp_f32 v110, v110\n"
    "v_exp_f32 v111, v111\n"
    "v_exp_f32 v112, v112\n"
    "v_exp_f32 v113, v113\n"
    "s_waitcnt vmcnt(16) lgkmcnt(0)\n"
    "v_smfmac_f32_32x32x32_bf16 v[128:143], v[52:55], v[104:111], %[idx]\n"
    "s_add_u32 m0, %[ring], 0x440\n"
    "buffer_load_dwordx4 v[236:239], %[lwo], %[rs], %[wo] offen offset:3072 sc1\n"
    "v_exp_f32 v114, v114\n"
    "v_exp_f32 v115, v115\n"
    "v_exp_f32 v116, v116\n"
    "v_mfma_f32_32x32x16_bf16 v[96:111], v[68:71], v[88:91], v[160:175]\n"
    "buffer_load_dwordx4 %[l16], %[rs], %[kv] offen offset:1024 sc1 lds\n"
    "v_exp_f32 v117, v117\n"
    "v_exp_f32 v118, v118\n"
    "v_exp_f32 v119, v119\n"
    "v_exp_f32 v120, v120\n"
    "v_exp_f32 v121, v121\n"
    "v_exp_f32 v122, v122\n"
    "v_smfmac_f32_32x32x32_bf16 v[144:159], v[48:51], v[112:119], %[idx]\n"
    "ds_read_b128 v[48:51], %[va0] offset:3168\n"
    "v_exp_f32 v123, v123\n"
    "v_exp_f32 v124, v124\n"
    "v_exp_f32 v125, v125\n"
    "v_exp_f32 v126, v126\n"
    "v_exp_f32 v127, v127\n"
    "v_exp_f32 v96, v96\n"
    "v_exp_f32 v97, v97\n"
    "v_smfmac_f32_32x32x32_bf16 v[144:159], v[52:55], v[120:127], %[idx]\n"
    "s_add_u32 m0, %[pl], 0x2000\n"
    "ds_read_b128 v[52:55], %[va1] offset:3168\n"
    "v_exp_f32 v98, v98\n"
    "v_exp_f32 v99, v99\n"
    "v_exp_f32 v100, v100\n"
    "v_mfma_f32_32x32x16_bf16 v[112:127], v[68:71], v[92:95], v[176:191]\n"
    "buffer_load_dwordx4 %[l16], %[rs], %[w13] offen offset:1024 sc1 lds\n"
    "s_waitcnt lgkmcnt(1)\n"
    "v_exp_f32 v101, v101\n"
    "v_exp_f32 v102, v102\n"
    "v_exp_f32 v103, v103\n"
    "v_exp_f32 v104, v104\n"
    "v_exp_f32 v105, v105\n"
    "v_exp_f32 v106, v106\n"
    "v_smfmac_f32_32x32x32_bf16 v[128:143], v[48:51], v[96:103], %[idx]\n"
    "buffer_load_dwordx4 v[68:71], %[l16], %[rs], %[kv] offen offset:2048 sc1\n"
    "v_exp_f32 v107, v107\n"
    "v_exp_f32 v108, v108\n"
    "v_exp_f32 v109, v109\n"
    "v_exp_f32 v110, v110\n"
    "v_exp_f32 v111, v111\n"
    "v_exp_f32 v112, v112\n"
    "v_exp_f32 v113, v113\n"
    "s_waitcnt vmcnt(17) lgkmcnt(0)\n"
    "v_smfmac_f32_32x32x32_bf16 v[128:143], v[52:55], v[104:111], %[idx]\n"
    "s_add_u32 m0, %[ring], 0x60\n"
    "s_add_u32 %[wo], %[wo], 0x1000\n"
    "buffer_load_dwordx4 v[240:243], %[lwo], %[rs], %[wo] offen offset:0 sc1\n"
    "v_exp_f32 v114, v114\n"
    "v_exp_f32 v115, v115\n"
    "v_exp_f32 v116, v116\n"
    "v_mfma_f32_32x32x16_bf16 v[96:111], v[72:75], v[88:91], v[160:175]\n"
    "buffer_load_dwordx4 %[l16], %[rs], %[kv] offen offset:3072 sc1 lds\n"
    "v_exp_f32 v117, v117\n"
    "v_exp_f32 v118, v118\n"
    "v_exp_f32 v119, v119\n"
    "v_exp_f32 v120, v120\n"
    "v_exp_f32 v121, v121\n"
    "v_exp_f32 v122, v122\n"
    "v_smfmac_f32_32x32x32_bf16 v[144:159], v[48:51], v[112:119], %[idx]\n"
    "ds_read_b128 v[48:51], %[va0] offset:4224\n"
    "v_exp_f32 v123, v123\n"
    "v_exp_f32 v124, v124\n"
    "v_exp_f32 v125, v125\n"
    "v_exp_f32 v126, v126\n"
    "v_exp_f32 v127, v127\n"
    "v_exp_f32 v96, v96\n"
    "v_exp_f32 v97, v97\n"
    "v_smfmac_f32_32x32x32_bf16 v[144:159], v[52:55], v[120:127], %[idx]\n"
    "s_add_u32 m0, %[pl], 0x2000\n"
    "ds_read_b128 v[52:55], %[va1] offset:4224\n"
    "v_exp_f32 v98, v98\n"
    "v_exp_f32 v99, v99\n"
    "v_exp_f32 v100, v100\n"
    "v_mfma_f32_32x32x16_bf16 v[112:127], v[72:75], v[92:95], v[176:191]\n"
    "buffer_load_dwordx4 %[l16], %[rs], %[w13] offen offset:2048 sc1 lds\n"
    "s_waitcnt lgkmcnt(1)\n"
    "v_exp_f32 v101, v101\n"
    "v_exp_f32 v102, v102\n"
    "v_exp_f32 v103, v103\n"
    "v_exp_f32 v104, v104\n"
    "v_exp_f32 v105, v105\n"
    "v_exp_f32 v106, v106\n"
    "v_smfmac_f32_32x32x32_bf16 v[128:143], v[48:51], v[96:103], %[idx]\n"
    "s_add_u32 %[kv], %[kv], 0x1000\n"
    "buffer_load_dwordx4 v[72:75], %[l16], %[rs], %[kv] offen offset:0 sc1\n"
    "s_cmp_eq_u32 %[tmp], 1\n"
    "s_cbranch_scc0 2f\n"
    "s_setprio 0\n"
    "2:\n"
    "v_exp_f32 v107, v107\n"
    "v_exp_f32 v108, v108\n"
    "v_exp_f32 v109, v109\n"
    "v_exp_f32 v110, v110\n"
    "v_exp_f32 v111, v111\n"
    "v_exp_f32 v112, v112\n"
    "v_exp_f32 v113, v113\n"
    "s_waitcnt vmcnt(17) lgkmcnt(0)\n"
    "v_smfmac_f32_32x32x32_bf16 v[128:143], v[52:55], v[104:111], %[idx]\n"
    "s_add_u32 m0, %[ring], 0xc80\n"
    "buffer_load_dwordx4 v[244:247], %[lwo], %[rs], %[wo] offen offset:1024 sc1\n"
    "v_exp_f32 v114, v114\n"
    "v_exp_f32 v115, v115\n"
    "v_exp_f32 v116, v116\n"
    "v_mfma_f32_32x32x16_bf16 v[96:111], v[76:79], v[88:91], v[160:175]\n"
    "buffer_load_dwordx4 %[l16], %[rs], %[kv] offen offset:1024 sc1 lds\n"
    "v_exp_f32 v117, v117\n"
    "v_exp_f32 v118, v118\n"
    "v_exp_f32 v119, v119\n"
    "v_exp_f32 v120, v120\n"
    "v_exp_f32 v121, v121\n"
    "v_exp_f32 v122, v122\n"
    "v_smfmac_f32_32x32x32_bf16 v[144:159], v[48:51], v[112:119], %[idx]\n"
    "ds_read_b128 v[48:51], %[va0] offset:5280\n"
    "v_exp_f32 v123, v123\n"
    "v_exp_f32 v124, v124\n"
    "v_exp_f32 v125, v125\n"
    "v_exp_f32 v126, v126\n"
    "v_exp_f32 v127, v127\n"
    "v_exp_f32 v96, v96\n"
    "v_exp_f32 v97, v97\n"
    "v_smfmac_f32_32x32x32_bf16 v[144:159], v[52:55], v[120:127], %[idx]\n"
    "s_add_u32 m0, %[pl], 0x2000\n"
    "ds_read_b128 v[52:55], %[va1] offset:5280\n"
    "v_exp_f32 v98, v98\n"
    "v_exp_f32 v99, v99\n"
    "v_exp_f32 v100, v100\n"
    "v_mfma_f32_32x32x16_bf16 v[112:127], v[76:79], v[92:95], v[176:191]\n"
    "buffer_load_dwordx4 %[l16], %[rs], %[w13] offen offset:3072 sc1 lds\n"
    "s_waitcnt lgkmcnt(1)\n"
    "v_exp_f32 v101, v101\n"
    "v_exp_f32 v102, v102\n"
    "v_exp_f32 v103, v103\n"
    "v_exp_f32 v104, v104\n"
    "v_exp_f32 v105, v105\n"
    "v_exp_f32 v106, v106\n"
    "v_smfmac_f32_32x32x32_bf16 v[128:143], v[48:51], v[96:103], %[idx]\n"
    "buffer_load_dwordx4 v[76:79], %[l16], %[rs], %[kv] offen offset:2048 sc1\n"
    "v_exp_f32 v107, v107\n"
    "v_exp_f32 v108, v108\n"
    "v_exp_f32 v109, v109\n"
    "v_exp_f32 v110, v110\n"
    "v_exp_f32 v111, v111\n"
    "v_exp_f32 v112, v112\n"
    "v_exp_f32 v113, v113\n"
    "s_waitcnt vmcnt(17) lgkmcnt(0)\n"
    "v_smfmac_f32_32x32x32_bf16 v[128:143], v[52:55], v[104:111], %[idx]\n"
    "s_add_u32 m0, %[ring], 0x8a0\n"
    "buffer_load_dwordx4 v[248:251], %[lwo], %[rs], %[wo] offen offset:2048 sc1\n"
    "v_exp_f32 v114, v114\n"
    "v_exp_f32 v115, v115\n"
    "v_exp_f32 v116, v116\n"
    "v_mfma_f32_32x32x16_bf16 v[96:111], v[56:59], v[88:91], v[160:175]\n"
    "buffer_load_dwordx4 %[l16], %[rs], %[kv] offen offset:3072 sc1 lds\n"
    "v_exp_f32 v117, v117\n"
    "v_exp_f32 v118, v118\n"
    "v_exp_f32 v119, v119\n"
    "v_exp_f32 v120, v120\n"
    "v_exp_f32 v121, v121\n"
    "v_exp_f32 v122, v122\n"
    "v_smfmac_f32_32x32x32_bf16 v[144:159], v[48:51], v[112:119], %[idx]\n"
    "ds_read_b128 v[48:51], %[va0] offset:0\n"
    "v_exp_f32 v123, v123\n"
    "v_exp_f32 v124, v124\n"
    "v_exp_f32 v125, v125\n"
    "v_exp_f32 v126, v126\n"
    "v_exp_f32 v127, v127\n"
    "v_exp_f32 v96, v96\n"
    "v_exp_f32 v97, v97\n"
    "v_smfmac_f32_32x32x32_bf16 v[144:159], v[52:55], v[120:127], %[idx]\n"
    "ds_read_b128 v[52:55], %[va1] offset:0\n"
    "v_exp_f32 v98, v98\n"
    "v_exp_f32 v99, v99\n"
    "v_exp_f32 v100, v100\n"
    "v_mfma_f32_32x32x16_bf16 v[112:127], v[56:59], v[92:95], v[176:191]\n"
    "s_waitcnt lgkmcnt(1)\n"
    "v_exp_f32 v101, v101\n"
    "v_exp_f32 v102, v102\n"
    "v_exp_f32 v103, v103\n"
    "v_exp_f32 v104, v104\n"
    "v_exp_f32 v105, v105\n"
    "v_exp_f32 v106, v106\n"
    "v_smfmac_f32_32x32x32_bf16 v[128:143], v[48:51], v[96:103], %[idx]\n"
    "s_add_u32 %[kv], %[kv], 0x1000\n"
    "buffer_load_dwordx4 v[56:59], %[l16], %[rs], %[kv] offen offset:0 sc1\n"
    "v_exp_f32 v107, v107\n"
    "v_exp_f32 v108, v108\n"
    "v_exp_f32 v109, v109\n"
    "v_exp_f32 v110, v110\n"
    "v_exp_f32 v111, v111\n"
    "v_exp_f32 v112, v112\n"
    "v_exp_f32 v113, v113\n"
    "s_waitcnt vmcnt(16) lgkmcnt(0)\n"
    "v_smfmac_f32_32x32x32_bf16 v[128:143], v[52:55], v[104:111], %[idx]\n"
    "s_add_u32 m0, %[ring], 0xfffffc00\n"
    "buffer_load_dwordx4 v[252:255], %[lwo], %[rs], %[wo] offen offset:3072 sc1\n"
    "v_exp_f32 v114, v114\n"
    "v_exp_f32 v115, v115\n"
    "v_exp_f32 v116, v116\n"
    "v_mfma_f32_32x32x16_bf16 v[96:111], v[60:63], v[88:91], v[160:175]\n"
    "buffer_load_dwordx4 %[l16], %[rs], %[kv] offen offset:1024 sc1 lds\n"
    "v_exp_f32 v117, v117\n"
    "v_exp_f32 v118, v118\n"
    "v_exp_f32 v119, v119\n"
    "v_exp_f32 v120, v120\n"
    "v_exp_f32 v121, v121\n"
    "v_exp_f32 v122, v122\n"
    "v_smfmac_f32_32x32x32_bf16 v[144:159], v[48:51], v[112:119], %[idx]\n"
    "ds_read_b128 v[48:51], %[va0] offset:1056\n"
    "v_exp_f32 v123, v123\n"
    "v_exp_f32 v124, v124\n"
    "v_exp_f32 v125, v125\n"
    "v_exp_f32 v126, v126\n"
    "v_exp_f32 v127, v127\n"
    "v_exp_f32 v96, v96\n"
    "v_exp_f32 v97, v97\n"
    "v_smfmac_f32_32x32x32_bf16 v[144:159], v[52:55], v[120:127], %[idx]\n"
    "ds_read_b128 v[52:55], %[va1] offset:1056\n"
    "v_exp_f32 v98, v98\n"
    "v_exp_f32 v99, v99\n"
    "v_exp_f32 v100, v100\n"
    "v_mfma_f32_32x32x16_bf16 v[112:127], v[60:63], v[92:95], v[176:191]\n"
    "s_waitcnt lgkmcnt(1)\n"
    "v_exp_f32 v101, v101\n"
    "v_exp_f32 v102, v102\n"
    "v_exp_f32 v103, v103\n"
    "v_exp_f32 v104, v104\n"
    "v_exp_f32 v105, v105\n"
    "v_exp_f32 v106, v106\n"
    "v_smfmac_f32_32x32x32_bf16 v[128:143], v[48:51], v[96:103], %[idx]\n"
    "buffer_load_dwordx4 v[60:63], %[l16], %[rs], %[kv] offen offset:2048 sc1\n"
    "v_exp_f32 v107, v107\n"
    "v_exp_f32 v108, v108\n"
    "v_exp_f32 v109, v109\n"
    "v_exp_f32 v110, v110\n"
    "v_exp_f32 v111, v111\n"
    "v_exp_f32 v112, v112\n"
    "v_exp_f32 v113, v113\n"
    "s_waitcnt vmcnt(16) lgkmcnt(0)\n"
    "v_smfmac_f32_32x32x32_bf16 v[128:143], v[52:55], v[104:111], %[idx]\n"
    "s_add_u32 m0, %[ring], 0xfffff820\n"
    "v_exp_f32 v114, v114\n"
    "v_exp_f32 v115, v115\n"
    "v_exp_f32 v116, v116\n"
    "v_mfma_f32_32x32x16_bf16 v[96:111], v[64:67], v[88:91], v[160:175]\n"
    "buffer_load_dwordx4 %[l16], %[rs], %[kv] offen offset:3072 sc1 lds\n"
    "v_exp_f32 v117, v117\n"
    "v_exp_f32 v118, v118\n"
    "v_exp_f32 v119, v119\n"
    "v_exp_f32 v120, v120\n"
    "v_exp_f32 v121, v121\n"
    "v_exp_f32 v122, v122\n"
    "v_smfmac_f32_32x32x32_bf16 v[144:159], v[48:51], v[112:119], %[idx]\n"
    "ds_read_b128 v[48:51], %[va0] offset:2112\n"
    "v_exp_f32 v123, v123\n"
    "v_exp_f32 v124, v124\n"
    "v_exp_f32 v125, v125\n"
    "v_exp_f32 v126, v126\n"
    "v_exp_f32 v127, v127\n"
    "v_exp_f32 v96, v96\n"
    "v_exp_f32 v97, v97\n"
    "v_smfmac_f32_32x32x32_bf16 v[144:159], v[52:55], v[120:127], %[idx]\n"
    "ds_read_b128 v[52:55], %[va1] offset:2112\n"
    "v_exp_f32 v98, v98\n"
    "v_exp_f32 v99, v99\n"
    "v_exp_f32 v100, v100\n"
    "v_mfma_f32_32x32x16_bf16 v[112:127], v[64:67], v[92:95], v[176:191]\n"
    "s_waitcnt lgkmcnt(1)\n"
    "v_exp_f32 v101, v101\n"
    "v_exp_f32 v102, v102\n"
    "v_exp_f32 v103, v103\n"
    "v_exp_f32 v104, v104\n"
    "v_exp_f32 v105, v105\n"
    "v_exp_f32 v106, v106\n"
    "v_smfmac_f32_32x32x32_bf16 v[128:143], v[48:51], v[96:103], %[idx]\n"
    "v_exp_f32 v107, v107\n"
    "v_exp_f32 v108, v108\n"
    "v_exp_f32 v109, v109\n"
    "v_exp_f32 v110, v110\n"
    "v_exp_f32 v111, v111\n"
    "v_exp_f32 v112, v112\n"
    "v_exp_f32 v113, v113\n"
    "s_waitcnt vmcnt(13) lgkmcnt(0)\n"
    "v_smfmac_f32_32x32x32_bf16 v[128:143], v[52:55], v[104:111], %[idx]\n"
    "v_exp_f32 v114, v114\n"
    "v_exp_f32 v115, v115\n"
    "v_exp_f32 v116, v116\n"
    "v_mfma_f32_32x32x16_bf16 v[96:111], v[68:71], v[88:91], v[160:175]\n"
    "v_exp_f32 v117, v117\n"
    "v_exp_f32 v118, v118\n"
    "v_exp_f32 v119, v119\n"
    "v_exp_f32 v120, v120\n"
    "v_exp_f32 v121, v121\n"
    "v_exp_f32 v122, v122\n"
    "v_smfmac_f32_32x32x32_bf16 v[144:159], v[48:51], v[112:119], %[idx]\n"
    "ds_read_b128 v[48:51], %[va0] offset:3168\n"
    "v_exp_f32 v123, v123\n"
    "v_exp_f32 v124, v124\n"
    "v_exp_f32 v125, v125\n"
    "v_exp_f32 v126, v126\n"
    "v_exp_f32 v127, v127\n"
    "v_exp_f32 v96, v96\n"
    "v_exp_f32 v97, v97\n"
    "v_smfmac_f32_32x32x32_bf16 v[144:159], v[52:55], v[120:127], %[idx]\n"
    "ds_read_b128 v[52:55], %[va1] offset:3168\n"
    "v_exp_f32 v98, v98\n"
    "v_exp_f32 v99, v99\n"
    "v_exp_f32 v100, v100\n"
    "v_mfma_f32_32x32x16_bf16 v[112:127], v[68:71], v[92:95], v[176:191]\n"
    "s_waitcnt lgkmcnt(1)\n"
    "v_exp_f32 v101, v101\n"
    "v_exp_f32 v102, v102\n"
    "v_exp_f32 v103, v103\n"
    "v_exp_f32 v104, v104\n"
    "v_exp_f32 v105, v105\n"
    "v_exp_f32 v106, v106\n"
    "v_smfmac_f32_32x32x32_bf16 v[128:143], v[48:51], v[96:103], %[idx]\n"
    "v_exp_f32 v107, v107\n"
    "v_exp_f32 v108, v108\n"
    "v_exp_f32 v109, v109\n"
    "v_exp_f32 v110, v110\n"
    "v_exp_f32 v111, v111\n"
    "v_exp_f32 v112, v112\n"
    "v_exp_f32 v113, v113\n"
    "s_waitcnt vmcnt(9) lgkmcnt(0)\n"
    "v_smfmac_f32_32x32x32_bf16 v[128:143], v[52:55], v[104:111], %[idx]\n"
    "v_exp_f32 v114, v114\n"
    "v_exp_f32 v115, v115\n"
    "v_exp_f32 v116, v116\n"
    "v_mfma_f32_32x32x16_bf16 v[96:111], v[72:75], v[88:91], v[160:175]\n"
    "v_exp_f32 v117, v117\n"
    "v_exp_f32 v118, v118\n"
    "v_exp_f32 v119, v119\n"
    "v_exp_f32 v120, v120\n"
    "v_exp_f32 v121, v121\n"
    "v_exp_f32 v122, v122\n"
    "v_smfmac_f32_32x32x32_bf16 v[144:159], v[48:51], v[112:119], %[idx]\n"
    "ds_read_b128 v[48:51], %[va0] offset:4224\n"
    "v_exp_f32 v123, v123\n"
    "v_exp_f32 v124, v124\n"
    "v_exp_f32 v125, v125\n"
    "v_exp_f32 v126, v126\n"
    "v_exp_f32 v127, v127\n"
    "v_exp_f32 v96, v96\n"
    "v_exp_f32 v97, v97\n"
    "v_smfmac_f32_32x32x32_bf16 v[144:159], v[52:55], v[120:127], %[idx]\n"
    "ds_read_b128 v[52:55], %[va1] offset:4224\n"
    "v_exp_f32 v98, v98\n"
    "v_exp_f32 v99, v99\n"
    "v_exp_f32 v100, v100\n"
    "v_mfma_f32_32x32x16_bf16 v[112:127], v[72:75], v[92:95], v[176:191]\n"
    "s_waitcnt lgkmcnt(1)\n"
    "v_exp_f32 v101, v101\n"
    "v_exp_f32 v102, v102\n"
    "v_exp_f32 v103, v103\n"
    "v_exp_f32 v104, v104\n"
    "v_exp_f32 v105, v105\n"
    "v_exp_f32 v106, v106\n"
    "v_smfmac_f32_32x32x32_bf16 v[128:143], v[48:51], v[96:103], %[idx]\n"
    "v_exp_f32 v107, v107\n"
    "v_exp_f32 v108, v108\n"
    "v_exp_f32 v109, v109\n"
    "v_exp_f32 v110, v110\n"
    "v_exp_f32 v111, v111\n"
    "v_exp_f32 v112, v112\n"
    "v_exp_f32 v113, v113\n"
    "s_waitcnt vmcnt(5) lgkmcnt(0)\n"
    "v_smfmac_f32_32x32x32_bf16 v[128:143], v[52:55], v[104:111], %[idx]\n"
    "v_exp_f32 v114, v114\n"
    "v_exp_f32 v115, v115\n"
    "v_exp_f32 v116, v116\n"
    "v_mfma_f32_32x32x16_bf16 v[96:111], v[76:79], v[88:91], v[160:175]\n"
    "v_exp_f32 v117, v117\n"
    "v_exp_f32 v118, v118\n"
    "v_exp_f32 v119, v119\n"
    "v_exp_f32 v120, v120\n"
    "v_exp_f32 v121, v121\n"
    "v_exp_f32 v122, v122\n"
    "v_smfmac_f32_32x32x32_bf16 v[144:159], v[48:51], v[112:119], %[idx]\n"
    "ds_read_b128 v[48:51], %[va0] offset:5280\n"
    "v_exp_f32 v123, v123\n"
    "v_exp_f32 v124, v124\n"
    "v_exp_f32 v125, v125\n"
    "v_exp_f32 v126, v126\n"
    "v_exp_f32 v127, v127\n"
    "v_exp_f32 v96, v96\n"
    "v_exp_f32 v97, v97\n"
    "v_smfmac_f32_32x32x32_bf16 v[144:159], v[52:55], v[120:127], %[idx]\n"
    "ds_read_b128 v[52:55], %[va1] offset:5280\n"
    "v_exp_f32 v98, v98\n"
    "v_exp_f32 v99, v99\n"
    "v_exp_f32 v100, v100\n"
    "v_mfma_f32_32x32x16_bf16 v[112:127], v[76:79], v[92:95], v[176:191]\n"
    "s_waitcnt lgkmcnt(1)\n"
    "v_exp_f32 v101, v101\n"
    "v_exp_f32 v102, v102\n"
    "v_exp_f32 v103, v103\n"
    "v_exp_f32 v104, v104\n"
    "v_exp_f32 v105, v105\n"
    "v_exp_f32 v106, v106\n"
    "v_smfmac_f32_32x32x32_bf16 v[128:143], v[48:51], v[96:103], %[idx]\n"
    "v_exp_f32 v107, v107\n"
    "v_exp_f32 v108, v108\n"
    "v_exp_f32 v109, v109\n"
    "v_exp_f32 v110, v110\n"
    "v_exp_f32 v111, v111\n"
    "v_exp_f32 v112, v112\n"
    "v_exp_f32 v113, v113\n"
    "s_waitcnt vmcnt(2) lgkmcnt(0)\n"
    "v_smfmac_f32_32x32x32_bf16 v[128:143], v[52:55], v[104:111], %[idx]\n"
    "v_exp_f32 v114, v114\n"
    "v_exp_f32 v115, v115\n"
    "v_exp_f32 v116, v116\n"
    "v_mfma_f32_32x32x16_bf16 v[96:111], v[56:59], v[88:91], v[160:175]\n"
    "v_exp_f32 v117, v117\n"
    "v_exp_f32 v118, v118\n"
    "v_exp_f32 v119, v119\n"
    "v_exp_f32 v120, v120\n"
    "v_exp_f32 v121, v121\n"
    "v_exp_f32 v122, v122\n"
    "v_smfmac_f32_32x32x32_bf16 v[144:159], v[48:51], v[112:119], %[idx]\n"
    "ds_read_b128 v[48:51], %[va0] offset:0\n"
    "v_exp_f32 v123, v123\n"
    "v_exp_f32 v124, v124\n"
    "v_exp_f32 v125, v125\n"
    "v_exp_f32 v126, v126\n"
    "v_exp_f32 v127, v127\n"
    "v_exp_f32 v96, v96\n"
    "v_exp_f32 v97, v97\n"
    "v_smfmac_f32_32x32x32_bf16 v[144:159], v[52:55], v[120:127], %[idx]\n"
    "ds_read_b128 v[52:55], %[va1] offset:0\n"
    "v_exp_f32 v98, v98\n"
    "v_exp_f32 v99, v99\n"
    "v_exp_f32 v100, v100\n"
    "v_mfma_f32_32x32x16_bf16 v[112:127], v[56:59], v[92:95], v[176:191]\n"
    "s_waitcnt lgkmcnt(1)\n"
    "v_exp_f32 v101, v101\n"
    "v_exp_f32 v102, v102\n"
    "v_exp_f32 v103, v103\n"
    "v_exp_f32 v104, v104\n"
    "v_exp_f32 v105, v105\n"
    "v_exp_f32 v106, v106\n"
    "v_smfmac_f32_32x32x32_bf16 v[128:143], v[48:51], v[96:103], %[idx]\n"
    "v_exp_f32 v107, v107\n"
    "v_exp_f32 v108, v108\n"
    "v_exp_f32 v109, v109\n"
    "v_exp_f32 v110, v110\n"
    "v_exp_f32 v111, v111\n"
    "v_exp_f32 v112, v112\n"
    "v_exp_f32 v113, v113\n"
    "s_waitcnt vmcnt(0) lgkmcnt(0)\n"
    "v_smfmac_f32_32x32x32_bf16 v[128:143], v[52:55], v[104:111], %[idx]\n"
    "v_exp_f32 v114, v114\n"
    "v_exp_f32 v115, v115\n"
    "v_exp_f32 v116, v116\n"
    "v_mfma_f32_32x32x16_bf16 v[96:111], v[60:63], v[88:91], v[160:175]\n"
    "v_exp_f32 v117, v117\n"
    "v_exp_f32 v118, v118\n"
    "v_exp_f32 v119, v119\n"
    "v_exp_f32 v120, v120\n"
    "v_exp_f32 v121, v121\n"
    "v_exp_f32 v122, v122\n"
    "v_smfmac_f32_32x32x32_bf16 v[144:159], v[48:51], v[112:119], %[idx]\n"
    "ds_read_b128 v[48:51], %[va0] offset:1056\n"
    "v_exp_f32 v123, v123\n"
    "v_exp_f32 v124, v124\n"
    "v_exp_f32 v125, v125\n"
    "v_exp_f32 v126, v126\n"
    "v_exp_f32 v127, v127\n"
    "v_exp_f32 v96, v96\n"
    "v_exp_f32 v97, v97\n"
    "v_smfmac_f32_32x32x32_bf16 v[144:159], v[52:55], v[120:127], %[idx]\n"
    "ds_read_b128 v[52:55], %[va1] offset:1056\n"
    "v_exp_f32 v98, v98\n"
    "v_exp_f32 v99, v99\n"
    "v_exp_f32 v100, v100\n"
    "v_mfma_f32_32x32x16_bf16 v[112:127], v[60:63], v[92:95], v[176:191]\n"
    "s_waitcnt lgkmcnt(1)\n"
    "v_exp_f32 v101, v101\n"
    "v_exp_f32 v102, v102\n"
    "v_exp_f32 v103, v103\n"
    "v_exp_f32 v104, v104\n"
    "v_exp_f32 v105, v105\n"
    "v_exp_f32 v106, v106\n"
    "v_smfmac_f32_32x32x32_bf16 v[128:143], v[48:51], v[96:103], %[idx]\n"
    "v_exp_f32 v107, v107\n"
    "v_exp_f32 v108, v108\n"
    "v_exp_f32 v109, v109\n"
    "v_exp_f32 v110, v110\n"
    "v_exp_f32 v111, v111\n"
    "v_exp_f32 v112, v112\n"
    "v_exp_f32 v113, v113\n"
    "s_waitcnt lgkmcnt(0)\n"
    "v_smfmac_f32_32x32x32_bf16 v[128:143], v[52:55], v[104:111], %[idx]\n"
    "v_exp_f32 v114, v114\n"
    "v_exp_f32 v115, v115\n"
    "v_exp_f32 v116, v116\n"
    "v_exp_f32 v117, v117\n"
    "v_exp_f32 v118, v118\n"
    "v_exp_f32 v119, v119\n"
    "v_exp_f32 v120, v120\n"
    "v_exp_f32 v121, v121\n"
    "v_exp_f32 v122, v122\n"
    "v_smfmac_f32_32x32x32_bf16 v[144:159], v[48:51], v[112:119], %[idx]\n"
    "v_exp_f32 v123, v123\n"
    "v_exp_f32 v124, v124\n"
    "v_exp_f32 v125, v125\n"
    "v_exp_f32 v126, v126\n"
    "v_exp_f32 v127, v127\n"
    "s_nop 1\n"
    "v_smfmac_f32_32x32x32_bf16 v[144:159], v[52:55], v[120:127], %[idx]\n"
    "s_setprio 0\n"
    "s_waitcnt vmcnt(0)\n"
    "s_nop 10\n"
        : "=&{v[128:143]}"(o0), "=&{v[144:159]}"(o1), "=&{v[192:207]}"(wo0), "=&{v[208:223]}"(wo1), "=&{v[224:239]}"(wo2), "=&{v[240:255]}"(wo3),
          [kv] "+s"(kv), [wo] "+s"(swo), [w13] "+s"(s13), [tmp] "=&s"(tmp), [cnt] "=&s"(cnt), [t2] "=&s"(t2)
        : "{v[88:91]}"(q0), "{v[92:95]}"(q1), [m0] "v"(m0), [m1] "v"(m1), [lwo] "v"(lwo), [idx] "v"(0xDDDDu), [l16] "v"(l16), [rs] "s"(rS), [ring] "s"(ring),
          [va0] "v"(va0), [va1] "v"(va1), [wv] "s"(w), [fr] "s"(WS_FR), [tag] "s"((u32)tag), [pl] "s"(spl)
        : "v48", "v49", "v50", "v51", "v52", "v53", "v54", "v55", "v56", "v57", "v58", "v59", "v60", "v61", "v62", "v63", "v64", "v65", "v66", "v67", "v68", "v69", "v70", "v71", "v72", "v73", "v74", "v75", "v76", "v77", "v78", "v79", "v96", "v97", "v98", "v99", "v100", "v101", "v102", "v103", "v104", "v105", "v106", "v107", "v108", "v109", "v110", "v111", "v112", "v113", "v114", "v115", "v116", "v117", "v118", "v119", "v120", "v121", "v122", "v123", "v124", "v125", "v126", "v127", "v160", "v161", "v162", "v163", "v164", "v165", "v166", "v167", "v168", "v169", "v170", "v171", "v172", "v173", "v174", "v175", "v176", "v177", "v178", "v179", "v180", "v181", "v182", "v183", "v184", "v185", "v186", "v187", "v188", "v189", "v190", "v191", "scc", "vcc", "memory");
      float c0 = o0[8] * 0x1p60f, c1 = o1[8] * 0x1p60f;
#pragma unroll
      for (int i = 0; i < 8; ++i) { c0 = fma0(o0[i], c0); c1 = fma0(o1[i], c1); }
      bool bad = !(c0 < __builtin_inff()) || !(c1 < __builtin_inff());
      if (attempt == 1 || !__any(bad)) break;
      m0 = -3e38f; m1 = -3e38f;
#pragma unroll 1
      for (int i = 0; i < 32; ++i) { const u32x4 kf = ld4(rS, l16, kv0 + i * 2048);
        m0 = fmaxf(m0, max16(mf32(kf, q0, splat16(0.f)))); m1 = fmaxf(m1, max16(mf32(kf, q1, splat16(0.f)))); }
      m0 = swap32_max(m0); m1 = swap32_max(m1);
    }
    STAMP(7);
    lds4fw(ring + l16, f32x4{o1[0], o1[1], o1[2], o1[3]}); lds4fw(ring + 1024 + l16, f32x4{o1[4], o1[5], o1[6], o1[7]});
    *(LAS f32x2*)(ring + 2048 + lane * 8) = f32x2{o1[8], m1};
    BAR();
    const u32 pr = L + LQ + (w ^ 4) * RING;
    const f32x2 sc = *(LAS const f32x2*)(pr + 2048 + lane * 8); const f32x4 sa = lds4f(pr + l16), sb = lds4f(pr + 1024 + l16);
    const float mm = fmaxf(m0, sc[1]), fa = __builtin_amdgcn_exp2f(m0 - mm), fb = __builtin_amdgcn_exp2f(sc[1] - mm);
    const float inv = __builtin_amdgcn_rcpf(o0[8] * fa + sc[0] * fb), ia = fa * inv, ib = fb * inv;
    u32x4 v;
#pragma unroll
    for (int i = 0; i < 2; ++i) { v[i] = pk2(o0[2 * i] * ia + sa[2 * i] * ib, o0[2 * i + 1] * ia + sa[2 * i + 1] * ib);
      v[2 + i] = pk2(o0[4 + 2 * i] * ia + sb[2 * i] * ib, o0[5 + 2 * i] * ia + sb[2 * i + 1] * ib); }
    st4(rS, v, l16, WS_O + ((2 * rb + kh) * 32 + 4 * quad + head) * 1024);
    if (w < 6) {
      float c = 0.f;
#pragma unroll
      for (int i = 0; i < 16; ++i) c = dsum(lds4(L + w * 16384 + i * 1024 + l16), c);
      c = swap32_add(swap16_add(c));
      if (lane < 16) *(LAS float*)(L + LD_CS + (w * 16 + lane) * 4) = c;
    }
    WAITVM_BAR(0);
    if (w == 0 && lane == 0) set_flag(ws, WS_FB + (rb * 8 + quad) * 128, tag);
    STAMP(8);
  }

  {
    LANEVALS();
    poll(ws, WS_FB + ((ctb >> 1) * 8 + (lane & 7)) * 128, tag);
    STAMP(9);
#pragma unroll
    for (int p = 0; p < 4; ++p) dma_sc1(rS, l16, WS_O + (ctb * 32 + 4 * w + p) * 1024, L + LQ + (4 * w + p) * 1024);
    WAITVM_BAR(0);
    STAMP(10);
    u32x4 of[16];
#pragma unroll
    for (int i = 0; i < 16; ++i) of[i] = lds4(L + LQ + (16 * kh + i) * 1024 + l16);
    SCHED();
    f32x16 acc = splat16(0.f);
#define WOF(V, i) u32x4{V[4 * (i)], V[4 * (i) + 1], V[4 * (i) + 2], V[4 * (i) + 3]}
#pragma unroll
    for (int i = 0; i < 4; ++i) acc = mf32(WOF(wo0, i), of[i], acc);
#pragma unroll
    for (int i = 0; i < 4; ++i) acc = mf32(WOF(wo1, i), of[4 + i], acc);
#pragma unroll
    for (int i = 0; i < 4; ++i) acc = mf32(WOF(wo2, i), of[8 + i], acc);
#pragma unroll
    for (int i = 0; i < 4; ++i) acc = mf32(WOF(wo3, i), of[12 + i], acc);
    lds4fw(L + LC_X + w * 2048 + l16, f32x4{acc[8], acc[9], acc[10], acc[11]}); lds4fw(L + LC_X + w * 2048 + 1024 + l16, f32x4{acc[12], acc[13], acc[14], acc[15]});
    STAMP(11);
    BAR();
    const f32x4 sa = lds4f(L + LC_X + (w ^ 4) * 2048 + l16), sb = lds4f(L + LC_X + (w ^ 4) * 2048 + 1024 + l16);
    u32x4 v;
#pragma unroll
    for (int i = 0; i < 2; ++i) { v[i] = pk2(acc[2 * i] + blo(xr0[i]) + sa[2 * i], acc[2 * i + 1] + bhi(xr0[i]) + sa[2 * i + 1]);
      v[2 + i] = pk2(acc[4 + 2 * i] + blo(xr1[i]) + sb[2 * i], acc[5 + 2 * i] + bhi(xr1[i]) + sb[2 * i + 1]); }
    st4(rS, v, blk * 16384 + h * 256 + t15 * 16, WS_H + (2 * ctb * 16 + 4 * cg + w3_) * 1024 + kh * 512);
    float s1 = 0.f, s2 = 0.f, s3 = 0.f, s4 = 0.f;
#pragma unroll
    for (int i = 0; i < 4; ++i) stat1(v[i], s1, s2, s3, s4);
    *(LAS f32x2*)(L + LC_ST + r32 * 128 + (2 * w + h) * 8) = f32x2{s1 + s3, s2 + s4};
    WAITVM_BAR(0);
    if (w == 0 && lane == 0) set_flag(ws, WS_FC + (ctb * 4 + cg) * 128, tag);
    if (w == 7) {
      const u32 a = L + LC_ST + r32 * 128 + h * 64;
      const f32x4 t = (lds4f(a) + lds4f(a + 16)) + (lds4f(a + 32) + lds4f(a + 48));
      const float t1 = swap32_add(t[0] + t[2]), t2 = swap32_add(t[1] + t[3]);
      st4(rS, u32x4{__float_as_uint(t1), (u32)tag, __float_as_uint(t2), (u32)tag}, h ? OOB : blk * 1024 + t15 * 16, WS_ST + 2 * ctb * 1024 + cg * 256);
      if (b == 0 && lane == 0) set_flag(ws, WS_EPOCH, tag);
    }
    STAMP(12);
  }

  const u32 w2b = WS_W2T + ecb * 43 * 4096;
  u32x4 wr[7][4];
  {
    LANEVALS();
#pragma unroll
    for (int p = 0; p < 7; ++p) { const int pc = 7 * w + p; dma_sc1(rS, l16, w2b + (kh ? 60 : 0) * 1024 + pc * 1024, L + LQ + pc * 1024); }
    const int tb = 8 * mt + w;
    f32x4 acc[6][2]; u32x4 sg[2];
    if (dact) {
      poll(ws, WS_FC + (tb * 4 + (lane & 3)) * 128, tag);
      STAMP(13);
      if (w < 4) __builtin_amdgcn_s_setprio(1);
      constexpr int WIN = DWIN, WD = 4;
      u32x4 hf[WIN + 1][2], wf[WD + 1];
      const u32 hb = WS_H + 2 * tb * 16384;
#pragma unroll
      for (int i = 0; i < WIN; ++i) { hf[i][0] = ld4(rS, l16, hb + i * 1024); hf[i][1] = ld4(rS, l16, hb + 16384 + i * 1024); }
#pragma unroll
      for (int n = 0; n < WD; ++n) wf[n] = lds4(L + n * 16384 + l16);
#pragma unroll
      for (int q = 0; q < 6; ++q) acc[q][0] = acc[q][1] = f32x4{0.f, 0.f, 0.f, 0.f};
      SCHED();
#pragma unroll
      for (int n = 0; n < 96; ++n) {
        const int i = n / 6, q = n % 6, m = n + WD;
        acc[q][0] = mf16(wf[n % (WD + 1)], hf[i % (WIN + 1)][0], acc[q][0]);
        SCHED();
        if (m < 96) wf[m % (WD + 1)] = lds4(L + (m % 6) * 16384 + (m / 6) * 1024 + l16);
        SCHED();
        acc[q][1] = mf16(wf[n % (WD + 1)], hf[i % (WIN + 1)][1], acc[q][1]);
        SCHED();
        if ((q == 1 || q == 4) && i + WIN < 16) hf[(i + WIN) % (WIN + 1)][q >> 2] = ld4(rS, l16, hb + (q >> 2) * 16384 + (i + WIN) * 1024);
        if ((q == 1 || q == 4) && i + WIN == 16) sg[q >> 2] = ld4(rS, l16, WS_ST + (2 * tb + (q >> 2)) * 1024);
        SCHED();
      }
      __builtin_amdgcn_s_setprio(0);
    }
    STAMP(14);
    if (w >= 4) WAITVM_BAR(0);
    if (dact) {
#pragma unroll 1
      for (u32 n = 0; n < (1u << 16); ++n) {
        if (__all(sg[0][1] == (u32)tag && sg[0][3] == (u32)tag && sg[1][1] == (u32)tag && sg[1][3] == (u32)tag)) break;
        __builtin_amdgcn_s_sleep(1);
        sg[0] = ld4(rS, l16, WS_ST + 2 * tb * 1024); sg[1] = ld4(rS, l16, WS_ST + (2 * tb + 1) * 1024);
        asm volatile("" ::: "memory");
      }
      float nm[2], ne[2], ir[2];
#pragma unroll
      for (int a = 0; a < 2; ++a) {
        const float mean = swap32_add(swap16_add(__uint_as_float(sg[a][0]))) * (1.f / 512.f);
        const float var = fmaxf(swap32_add(swap16_add(__uint_as_float(sg[a][2]))) * (1.f / 512.f) - mean * mean, 0.f) + EPS;
        nm[a] = -mean; ne[a] = -__builtin_amdgcn_rsqf(var) * LOG2E; ir[a] = var;
      }
#pragma unroll
      for (int u = 0; u < 3; ++u) {
        const f32x4 csg = lds4f(L + LD_CS + u * 128 + g16 * 16), csu = lds4f(L + LD_CS + u * 128 + 64 + g16 * 16);
        u32 pk[2][2];
#pragma unroll
        for (int a = 0; a < 2; ++a) { float gv[4];
#pragma unroll
          for (int i = 0; i < 4; ++i) gv[i] = swiglu(acc[2 * u][a][i], acc[2 * u + 1][a][i], csg[i], csu[i], nm[a], ne[a], ir[a]);
          pk[a][0] = pk2(gv[0], gv[1]); pk[a][1] = pk2(gv[2], gv[3]); }
        const auto s0 = __builtin_amdgcn_permlane16_swap(pk[0][0], pk[1][0], false, false), s1 = __builtin_amdgcn_permlane16_swap(pk[0][1], pk[1][1], false, false);
        const int uu = u0 + u;
        st4(rS, u32x4{s0[0], s1[0], s0[1], s1[1]}, blk * 43 * 1024 + h * 256 + t15 * 16, WS_G + (2 * tb * 43 + (uu >> 1)) * 1024 + (uu & 1) * 512);
      }
    }
    if (w < 4) {
      WAITVM_BAR(0);
#pragma unroll
      for (int p = 0; p < 24; ++p) { const int pc = 24 * w + p; dma_sc1(rS, l16, w2b + (w >= 2 ? 68 : 28) * 1024 + pc * 1024, L + pc * 1024); }
    }
#pragma unroll
    for (int i = 0; i < 7; ++i)
#pragma unroll
      for (int n = 0; n < 4; ++n) wr[i][n] = lds4(((L + LQ + 7 * kh * 4096 + (n & 2) * 1024 + l16) ^ (kh * 2048)) + i * 4096 + (n & 1) * 1024);
    if (w >= 4) WAITVM(0);
    BAR();
    if (dact && w == 4 && lane == 0) set_flag(ws, WS_FD + (mt * 32 + nt) * 128, tag);
    if (w < 4) {
#pragma unroll
      for (int p = 0; p < 5; ++p) { const int pc = 5 * w + p; dma_sc1(rS, l16, w2b + (pc >= 12 ? 152 : 76) * 1024 + pc * 1024, L + LQ + pc * 1024); }
    }
    STAMP(15);
  }

  {
    LANEVALS();
    poll(ws, WS_FD + ((erb >> 2) * 32 + (r32 < 29 ? r32 : 0)) * 128, tag);
    STAMP(16);
    constexpr int WIN = EWIN;
    const u32 gb = WS_G + ((4 * erb + w3_) * 43 + 22 * kh) * 1024;
    u32x4 gf[WIN + 1];
#pragma unroll
    for (int i = 0; i < WIN; ++i) gf[i] = ld4(rS, l16, gb + i * 1024);
    BAR();
    const u32 la = L + kh * 12 * 4096 + l16, lb = L + LQ + kh * 3 * 4096 + l16;
    const u32 ho = WS_H + ((4 * erb + w3_) * 16 + 2 * ecb + kh) * 1024 + blk * 256 + t15 * 16 + h * 8;
    u32x2 hr0, hr1;
    f32x4 acc[4];
#pragma unroll
    for (int n = 0; n < 4; ++n) acc[n] = f32x4{0.f, 0.f, 0.f, 0.f};
    SCHED();
#pragma unroll
    for (int i = 0; i < 22; ++i) {
      if (i + WIN < 21) gf[(i + WIN) % (WIN + 1)] = ld4(rS, l16, gb + (i + WIN) * 1024);
      if (i + WIN == 21) gf[(i + WIN) % (WIN + 1)] = ld4(rS, kh ? OOB : l16, gb + 21 * 1024);
      if (i + WIN == 22) { hr0 = __builtin_amdgcn_raw_buffer_load_b64(rS, ho, 0, SC1); hr1 = __builtin_amdgcn_raw_buffer_load_b64(rS, ho + 512, 0, SC1); }
      SCHED();
      if (i < 21 || kh == 0) {
#pragma unroll
        for (int n = 0; n < 4; ++n) {
          acc[n] = mf16(wr[i % 7][n], gf[i % (WIN + 1)], acc[n]);
          SCHED();
          if (i + 7 < 22) wr[i % 7][n] = lds4((((i < 12 ? la : lb) + (n & 2) * 1024) ^ (kh * 2048)) + (i < 12 ? i : i - 12) * 4096 + (n & 1) * 1024);
          SCHED();
        }
      }
    }
    STAMP(17);
    const u32 xe = L + LQ + 6 * 4096;
    lds4fw(xe + w * 2048 + l16, acc[2]); lds4fw(xe + w * 2048 + 1024 + l16, acc[3]);
    BAR();
    const f32x4 a0 = acc[0] + lds4f(xe + (w ^ 4) * 2048 + l16), a1 = acc[1] + lds4f(xe + (w ^ 4) * 2048 + 1024 + l16);
    const u32 p0 = pk2(a0[0] + blo(hr0[0]), a0[1] + bhi(hr0[0])), p1 = pk2(a0[2] + blo(hr0[1]), a0[3] + bhi(hr0[1]));
    const u32 p2 = pk2(a1[0] + blo(hr1[0]), a1[1] + bhi(hr1[0])), p3 = pk2(a1[2] + blo(hr1[1]), a1[3] + bhi(hr1[1]));
    const auto s0 = __builtin_amdgcn_permlane16_swap(p0, p2, false, false), s1 = __builtin_amdgcn_permlane16_swap(p1, p3, false, false);
    const rsrc_t rO = mk(out, 2048 * 1024);
    __builtin_amdgcn_raw_buffer_store_b128(u32x4{s0[0], s1[0], s0[1], s1[1]}, rO, (64 * erb + 16 * w3_ + t15) * 1024 + ecb * 128 + kh * 64 + blk * 32 + h * 16, 0, OUTAUX);
  }
}

extern "C" void kernel_launch(void* const* d_in, const int* in_sizes, int n_in, void* d_out, int out_size, void* d_ws, size_t ws_size, hipStream_t stream) {
  block_fwd<<<256, 512, 0, stream>>>((const char*)d_in[0], (const char*)d_in[1], (const char*)d_in[2], (const char*)d_in[4], (const char*)d_in[5], (const char*)d_in[6], (char*)d_out, (char*)d_ws);
}
```

```cpp
#include <hip/hip_runtime.h>

__global__ __launch_bounds__(512, 2) void block_fwd(const void* x, const void* wqkv, const void* wo, const void* w1, const void* w3, const void* w2, void* out, void* ws) {
  __shared__ __attribute__((aligned(4096))) char lds[163840];
  auto karg = __builtin_amdgcn_kernarg_segment_ptr();
  unsigned blk = blockIdx.x, tid = threadIdx.x;
  asm volatile("" :: "v"((__attribute__((address_space(3))) char*)lds));
  asm volatile(
    ".Lk_start:\n"
    "s_load_dwordx16 s[4:19], s[0:1], 0x0\n"
    "s_getpc_b64 s[28:29]\n"
    ".Lk_pcref:\n"
    "s_bfe_u32 s41, s2, 0x40003\n"
    "s_lshl_b32 s41, s41, 12\n"
    "v_lshrrev_b32 v91, 4, v0\n"
    "v_and_or_b32 v91, v0, 3, v91\n"
    "v_lshlrev_b32 v91, 7, v91\n"
    "s_and_b32 s29, s29, 0xffff\n"
    "s_mov_b32 s30, .Lk_end-.Lk_pcref\n"
    "s_mov_b32 s31, 0x20000\n"
    "s_mov_b64 exec, 15\n"
    "buffer_load_dword v90, v91, s[28:31], s41 offen\n"
    "s_mov_b64 exec, -1\n"
    "v_readfirstlane_b32 s3, v0\n"
    "v_and_b32 v0, 63, v0\n"
    "s_and_b32 s56, s2, 7\n"
    "s_lshr_b32 s33, s2, 3\n"
    "s_lshr_b32 s3, s3, 6\n"
    "s_and_b32 s41, s2, 1\n"
    "s_lshl_b32 s41, s41, 2\n"
    "s_and_b32 s34, s33, 3\n"
    "s_or_b32 s34, s34, s41\n"
    "s_lshr_b32 s41, s56, 1\n"
    "s_lshl_b32 s41, s41, 3\n"
    "s_lshr_b32 s35, s33, 2\n"
    "s_add_u32 s35, s35, s41\n"
    "s_and_b32 s39, s3, 3\n"
    "s_lshr_b32 s40, s3, 2\n"
    "s_cmp_ge_u32 s3, 4\n"
    "s_cbranch_scc1 .Lk_consumer\n"
    "v_lshrrev_b32 v2, 3, v0\n"
    "v_and_b32 v3, 7, v0\n"
    "v_lshrrev_b32 v4, 4, v0\n"
    "v_xor_b32 v4, v4, v3\n"
    "v_lshlrev_b32 v4, 4, v4\n"
    "v_lshl_add_u32 v16, v2, 10, v4\n"
    "v_xor_b32 v17, 64, v16\n"
    "v_add_u32 v17, 7168, v17\n"
    "s_lshl_b32 s41, s35, 2\n"
    "s_add_u32 s41, s41, s3\n"
    "s_lshl_b32 s41, s41, 14\n"
    "s_lshl_b32 s43, s3, 11\n"
    "s_add_u32 s43, s43, 16384\n"
    "v_lshrrev_b32 v4, 2, v0\n"
    "v_and_b32 v4, 4, v4\n"
    "v_xor_b32 v4, v4, v3\n"
    "v_lshlrev_b32 v4, 4, v4\n"
    "v_mul_u32_u24 v2, 0xc00, v2\n"
    "v_add_u32 v20, v2, v4\n"
    "v_add_u32 v21, 23552, v20\n"
    "v_add_u32 v18, 0x400, v20\n"
    "v_add_u32 v19, 0x6000, v20\n"
    "s_mul_i32 s42, s3, 49152\n"
    "s_lshl_b32 s45, s34, 7\n"
    "s_add_u32 s42, s42, s45\n"
    "s_lshl_b32 s44, s3, 12\n"
    "s_add_u32 s44, s44, 81920\n"
    "s_waitcnt lgkmcnt(0)\n"
    "s_mov_b32 s24, s4\n"
    "s_and_b32 s25, s5, 0xffff\n"
    "s_mov_b32 s26, 0x200000\n"
    "s_mov_b32 s27, 0x20000\n"
    "s_mov_b32 s28, s6\n"
    "s_and_b32 s29, s7, 0xffff\n"
    "s_mov_b32 s30, 0x180000\n"
    "s_mov_b32 s31, 0x20000\n"
    "s_lshr_b32 s46, s34, 1\n"
    "s_add_u32 s46, s46, s35\n"
    "s_lshl_b32 s46, s46, 1\n"
    "s_add_u32 s47, s46, 0\n"
    "s_and_b32 s47, s47, 7\n"
    "s_add_u32 m0, s43, 0\n"
    "s_lshl_b32 s48, s47, 7\n"
    "s_add_u32 s48, s48, s41\n"
    "buffer_load_dwordx4 v16, s[24:27], s48 offen lds\n"
    "buffer_load_dwordx4 v17, s[24:27], s48 offen offset:1024 lds\n"
    "s_add_u32 s47, s46, 0\n"
    "s_and_b32 s47, s47, 7\n"
    "s_add_u32 m0, s44, 0\n"
    "s_mul_i32 s45, s47, 196608\n"
    "s_add_u32 s45, s45, s42\n"
    "buffer_load_dwordx4 v18, s[28:31], s45 offen lds\n"
    "buffer_load_dwordx4 v19, s[28:31], s45 offen offset:1024 lds\n"
    "buffer_load_dwordx4 v20, s[28:31], s45 offen offset:2048 lds\n"
    "buffer_load_dwordx4 v21, s[28:31], s45 offen offset:3072 lds\n"
    "s_add_u32 s47, s46, 1\n"
    "s_and_b32 s47, s47, 7\n"
    "s_add_u32 m0, s43, 8192\n"
    "s_lshl_b32 s48, s47, 7\n"
    "s_add_u32 s48, s48, s41\n"
    "buffer_load_dwordx4 v16, s[24:27], s48 offen lds\n"
    "buffer_load_dwordx4 v17, s[24:27], s48 offen offset:1024 lds\n"
    "s_add_u32 s47, s46, 1\n"
    "s_and_b32 s47, s47, 7\n"
    "s_add_u32 m0, s44, 16384\n"
    "s_mul_i32 s45, s47, 196608\n"
    "s_add_u32 s45, s45, s42\n"
    "buffer_load_dwordx4 v18, s[28:31], s45 offen lds\n"
    "buffer_load_dwordx4 v19, s[28:31], s45 offen offset:1024 lds\n"
    "buffer_load_dwordx4 v20, s[28:31], s45 offen offset:2048 lds\n"
    "buffer_load_dwordx4 v21, s[28:31], s45 offen offset:3072 lds\n"
    "s_add_u32 s47, s46, 2\n"
    "s_and_b32 s47, s47, 7\n"
    "s_add_u32 m0, s43, 16384\n"
    "s_lshl_b32 s48, s47, 7\n"
    "s_add_u32 s48, s48, s41\n"
    "buffer_load_dwordx4 v16, s[24:27], s48 offen lds\n"
    "buffer_load_dwordx4 v17, s[24:27], s48 offen offset:1024 lds\n"
    "s_add_u32 s47, s46, 2\n"
    "s_and_b32 s47, s47, 7\n"
    "s_add_u32 m0, s44, 32768\n"
    "s_mul_i32 s45, s47, 196608\n"
    "s_add_u32 s45, s45, s42\n"
    "buffer_load_dwordx4 v18, s[28:31], s45 offen lds\n"
    "v_add_u32 v22, 194560, v20\n"
    "v_add_u32 v23, 194560, v21\n"
    "v_and_b32 v2, 15, v0\n"
    "v_lshrrev_b32 v3, 4, v0\n"
    "v_lshrrev_b32 v4, 1, v2\n"
    "v_xor_b32 v3, v3, v4\n"
    "v_lshlrev_b32 v3, 4, v3\n"
    "v_lshl_add_u32 v3, v2, 7, v3\n"
    "v_add_u32 v24, s43, v3\n"
    "v_xor_b32 v25, 64, v24\n"
    "v_and_b32 v2, 2, v0\n"
    "v_cmp_eq_u32 vcc, 0, v2\n"
    "v_mov_b32 v3, 0x3f803f80\n"
    "v_and_b32 v2, 1, v0\n"
    "v_mul_u32_u24 v26, 0xaaaa, v2\n"
    "v_cndmask_b32 v36, 0, v3, vcc\n"
    "v_add_u32 v26, 0x4444, v26\n"
    "v_mov_b32 v37, v36\n"
    "v_mov_b32 v38, v36\n"
    "v_mov_b32 v39, v36\n"
    "v_mov_b32 v32, 0\n"
    "v_mov_b32 v33, 0\n"
    "v_mov_b32 v34, 0\n"
    "v_mov_b32 v35, 0\n"
    "s_mov_b32 s20, s18\n"
    "s_and_b32 s21, s19, 0xffff\n"
    "s_mov_b32 s22, 0x13e8000\n"
    "s_mov_b32 s23, 0x20000\n"
    "v_and_b32 v56, 1, v0\n"
    "v_cmp_eq_u32 s[70:71], 1, v56\n"
    "v_and_b32 v56, 2, v0\n"
    "v_cmp_eq_u32 s[72:73], 2, v56\n"
    "s_lshl_b32 s49, s3, 6\n"
    "v_and_b32 v27, 15, v0\n"
    "v_lshl_add_u32 v27, v27, 2, s49\n"
    "s_setprio 2\n"
    "s_waitcnt vmcnt(9)\n"
    "s_barrier\n"
    "ds_read_b128 v[40:43], v24\n"
    "ds_read_b128 v[44:47], v25\n"
    "buffer_load_dwordx4 v19, s[28:31], s45 offen offset:1024 lds\n"
    "buffer_load_dwordx4 v20, s[28:31], s45 offen offset:2048 lds\n"
    "buffer_load_dwordx4 v21, s[28:31], s45 offen offset:3072 lds\n"
    "s_add_u32 s47, s46, 3\n"
    "s_and_b32 s47, s47, 7\n"
    "s_add_u32 m0, s43, 24576\n"
    "s_lshl_b32 s48, s47, 7\n"
    "s_add_u32 s48, s48, s41\n"
    "buffer_load_dwordx4 v16, s[24:27], s48 offen lds\n"
    "buffer_load_dwordx4 v17, s[24:27], s48 offen offset:1024 lds\n"
    "s_add_u32 s47, s46, 3\n"
    "s_and_b32 s47, s47, 7\n"
    "s_add_u32 m0, s44, 49152\n"
    "s_mul_i32 s45, s47, 196608\n"
    "s_add_u32 s45, s45, s42\n"
    "buffer_load_dwordx4 v18, s[28:31], s45 offen lds\n"
    "s_waitcnt lgkmcnt(1)\n"
    "v_mfma_f32_16x16x32_bf16 v[28:31], v[40:43], v[40:43], 0\n"
    "s_waitcnt lgkmcnt(0)\n"
    "v_mfma_f32_16x16x32_bf16 v[28:31], v[44:47], v[44:47], v[28:31]\n"
    "v_smfmac_f32_16x16x64_bf16 v[32:35], v[36:39], v[40:47], v26\n"
    "s_waitcnt vmcnt(9)\n"
    "s_barrier\n"
    "ds_read_b128 v[40:43], v24 offset:8192\n"
    "ds_read_b128 v[44:47], v25 offset:8192\n"
    "buffer_load_dwordx4 v19, s[28:31], s45 offen offset:1024 lds\n"
    "buffer_load_dwordx4 v20, s[28:31], s45 offen offset:2048 lds\n"
    "buffer_load_dwordx4 v21, s[28:31], s45 offen offset:3072 lds\n"
    "s_add_u32 s47, s46, 4\n"
    "s_and_b32 s47, s47, 7\n"
    "s_add_u32 m0, s43, 32768\n"
    "s_lshl_b32 s48, s47, 7\n"
    "s_add_u32 s48, s48, s41\n"
    "buffer_load_dwordx4 v16, s[24:27], s48 offen lds\n"
    "buffer_load_dwordx4 v17, s[24:27], s48 offen offset:1024 lds\n"
    "s_add_u32 s47, s46, 4\n"
    "s_and_b32 s47, s47, 7\n"
    "s_add_u32 m0, s44, 65536\n"
    "s_mul_i32 s45, s47, 196608\n"
    "s_add_u32 s45, s45, s42\n"
    "buffer_load_dwordx4 v18, s[28:31], s45 offen lds\n"
    "s_waitcnt lgkmcnt(1)\n"
    "v_mfma_f32_16x16x32_bf16 v[28:31], v[40:43], v[40:43], v[28:31]\n"
    "s_waitcnt lgkmcnt(0)\n"
    "v_mfma_f32_16x16x32_bf16 v[28:31], v[44:47], v[44:47], v[28:31]\n"
    "v_smfmac_f32_16x16x64_bf16 v[32:35], v[36:39], v[40:47], v26\n"
    "s_waitcnt vmcnt(9)\n"
    "s_barrier\n"
    "ds_read_b128 v[40:43], v24 offset:16384\n"
    "ds_read_b128 v[44:47], v25 offset:16384\n"
    "buffer_load_dwordx4 v19, s[28:31], s45 offen offset:1024 lds\n"
    "buffer_load_dwordx4 v20, s[28:31], s45 offen offset:2048 lds\n"
    "buffer_load_dwordx4 v21, s[28:31], s45 offen offset:3072 lds\n"
    "s_add_u32 s47, s46, 5\n"
    "s_and_b32 s47, s47, 7\n"
    "s_add_u32 m0, s43, 40960\n"
    "s_lshl_b32 s48, s47, 7\n"
    "s_add_u32 s48, s48, s41\n"
    "buffer_load_dwordx4 v16, s[24:27], s48 offen lds\n"
    "buffer_load_dwordx4 v17, s[24:27], s48 offen offset:1024 lds\n"
    "s_add_u32 s47, s46, 5\n"
    "s_and_b32 s47, s47, 7\n"
    "s_add_u32 m0, s44, 0\n"
    "s_mul_i32 s45, s47, 196608\n"
    "s_add_u32 s45, s45, s42\n"
    "buffer_load_dwordx4 v18, s[28:31], s45 offen lds\n"
    "s_waitcnt lgkmcnt(1)\n"
    "v_mfma_f32_16x16x32_bf16 v[28:31], v[40:43], v[40:43], v[28:31]\n"
    "s_waitcnt lgkmcnt(0)\n"
    "v_mfma_f32_16x16x32_bf16 v[28:31], v[44:47], v[44:47], v[28:31]\n"
    "v_smfmac_f32_16x16x64_bf16 v[32:35], v[36:39], v[40:47], v26\n"
    "s_waitcnt vmcnt(9)\n"
    "s_barrier\n"
    "ds_read_b128 v[40:43], v24 offset:24576\n"
    "ds_read_b128 v[44:47], v25 offset:24576\n"
    "buffer_load_dwordx4 v19, s[28:31], s45 offen offset:1024 lds\n"
    "buffer_load_dwordx4 v20, s[28:31], s45 offen offset:2048 lds\n"
    "buffer_load_dwordx4 v21, s[28:31], s45 offen offset:3072 lds\n"
    "s_add_u32 s47, s46, 6\n"
    "s_and_b32 s47, s47, 7\n"
    "s_add_u32 m0, s43, 49152\n"
    "s_lshl_b32 s48, s47, 7\n"
    "s_add_u32 s48, s48, s41\n"
    "buffer_load_dwordx4 v16, s[24:27], s48 offen lds\n"
    "buffer_load_dwordx4 v17, s[24:27], s48 offen offset:1024 lds\n"
    "s_add_u32 s47, s46, 7\n"
    "s_and_b32 s47, s47, 7\n"
    "s_add_u32 m0, s43, 57344\n"
    "s_lshl_b32 s48, s47, 7\n"
    "s_add_u32 s48, s48, s41\n"
    "buffer_load_dwordx4 v16, s[24:27], s48 offen lds\n"
    "s_waitcnt lgkmcnt(1)\n"
    "v_mfma_f32_16x16x32_bf16 v[28:31], v[40:43], v[40:43], v[28:31]\n"
    "s_waitcnt lgkmcnt(0)\n"
    "v_mfma_f32_16x16x32_bf16 v[28:31], v[44:47], v[44:47], v[28:31]\n"
    "v_smfmac_f32_16x16x64_bf16 v[32:35], v[36:39], v[40:47], v26\n"
    "s_waitcnt vmcnt(9)\n"
    "s_barrier\n"
    "ds_read_b128 v[40:43], v24 offset:32768\n"
    "ds_read_b128 v[44:47], v25 offset:32768\n"
    "buffer_load_dwordx4 v17, s[24:27], s48 offen offset:1024 lds\n"
    "s_add_u32 s47, s46, 6\n"
    "s_and_b32 s47, s47, 7\n"
    "s_add_u32 m0, s44, 16384\n"
    "s_mul_i32 s45, s47, 196608\n"
    "s_add_u32 s45, s45, s42\n"
    "buffer_load_dwordx4 v18, s[28:31], s45 offen lds\n"
    "buffer_load_dwordx4 v19, s[28:31], s45 offen offset:1024 lds\n"
    "buffer_load_dwordx4 v20, s[28:31], s45 offen offset:2048 lds\n"
    "buffer_load_dwordx4 v21, s[28:31], s45 offen offset:3072 lds\n"
    "s_add_u32 s47, s46, 7\n"
    "s_and_b32 s47, s47, 7\n"
    "s_add_u32 m0, s44, 32768\n"
    "s_mul_i32 s45, s47, 196608\n"
    "s_add_u32 s45, s45, s42\n"
    "buffer_load_dwordx4 v18, s[28:31], s45 offen lds\n"
    "s_waitcnt lgkmcnt(1)\n"
    "v_mfma_f32_16x16x32_bf16 v[28:31], v[40:43], v[40:43], v[28:31]\n"
    "s_waitcnt lgkmcnt(0)\n"
    "v_mfma_f32_16x16x32_bf16 v[28:31], v[44:47], v[44:47], v[28:31]\n"
    "v_smfmac_f32_16x16x64_bf16 v[32:35], v[36:39], v[40:47], v26\n"
    "v_mov_b32 v14, 0\n"
    "buffer_load_dwordx2 v[14:15], v14, s[20:23], 0 offen sc1\n"
    "s_waitcnt vmcnt(10)\n"
    "s_barrier\n"
    "ds_read_b128 v[40:43], v24 offset:40960\n"
    "ds_read_b128 v[44:47], v25 offset:40960\n"
    "buffer_load_dwordx4 v19, s[28:31], s45 offen offset:1024 lds\n"
    "buffer_load_dwordx4 v20, s[28:31], s45 offen offset:2048 lds\n"
    "buffer_load_dwordx4 v21, s[28:31], s45 offen offset:3072 lds\n"
    "s_add_u32 s47, s46, 0\n"
    "s_and_b32 s47, s47, 7\n"
    "s_add_u32 m0, s44, 49152\n"
    "s_mul_i32 s45, s47, 196608\n"
    "s_add_u32 s45, s45, s42\n"
    "buffer_load_dwordx4 v20, s[28:31], s45 offen lds\n"
    "buffer_load_dwordx4 v21, s[28:31], s45 offen offset:1024 lds\n"
    "buffer_load_dwordx4 v22, s[28:31], s45 offen offset:2048 lds\n"
    "buffer_load_dwordx4 v23, s[28:31], s45 offen offset:3072 lds\n"
    "s_waitcnt lgkmcnt(1)\n"
    "v_mfma_f32_16x16x32_bf16 v[28:31], v[40:43], v[40:43], v[28:31]\n"
    "s_waitcnt lgkmcnt(0)\n"
    "v_mfma_f32_16x16x32_bf16 v[28:31], v[44:47], v[44:47], v[28:31]\n"
    "v_smfmac_f32_16x16x64_bf16 v[32:35], v[36:39], v[40:47], v26\n"
    "s_waitcnt vmcnt(9)\n"
    "s_barrier\n"
    "ds_read_b128 v[40:43], v24 offset:49152\n"
    "ds_read_b128 v[44:47], v25 offset:49152\n"
    "ds_read_b128 v[48:51], v24 offset:57344\n"
    "ds_read_b128 v[52:55], v25 offset:57344\n"
    "s_add_u32 s47, s46, 2\n"
    "s_and_b32 s47, s47, 7\n"
    "s_add_u32 m0, s44, 65536\n"
    "s_mul_i32 s45, s47, 196608\n"
    "s_add_u32 s45, s45, s42\n"
    "buffer_load_dwordx4 v20, s[28:31], s45 offen lds\n"
    "buffer_load_dwordx4 v21, s[28:31], s45 offen offset:1024 lds\n"
    "buffer_load_dwordx4 v22, s[28:31], s45 offen offset:2048 lds\n"
    "buffer_load_dwordx4 v23, s[28:31], s45 offen offset:3072 lds\n"
    "s_waitcnt lgkmcnt(3)\n"
    "v_mfma_f32_16x16x32_bf16 v[28:31], v[40:43], v[40:43], v[28:31]\n"
    "s_waitcnt lgkmcnt(2)\n"
    "v_mfma_f32_16x16x32_bf16 v[28:31], v[44:47], v[44:47], v[28:31]\n"
    "v_smfmac_f32_16x16x64_bf16 v[32:35], v[36:39], v[40:47], v26\n"
    "s_waitcnt lgkmcnt(1)\n"
    "v_mfma_f32_16x16x32_bf16 v[28:31], v[48:51], v[48:51], v[28:31]\n"
    "s_waitcnt lgkmcnt(0)\n"
    "v_mfma_f32_16x16x32_bf16 v[28:31], v[52:55], v[52:55], v[28:31]\n"
    "v_smfmac_f32_16x16x64_bf16 v[32:35], v[36:39], v[48:55], v26\n"
    "s_nop 6\n"
    "v_cndmask_b32 v28, v28, v29, s[70:71]\n"
    "v_cndmask_b32 v30, v30, v31, s[70:71]\n"
    "v_add_f32 v32, v32, v33\n"
    "v_mul_f32 v32, 0x3b000000, v32\n"
    "v_cndmask_b32 v28, v28, v30, s[72:73]\n"
    "v_mul_f32 v28, 0x3b000000, v28\n"
    "v_fma_f32 v28, -v32, v32, v28\n"
    "v_max_f32 v28, 0, v28\n"
    "v_add_f32 v28, 0x3727c5ac, v28\n"
    "v_rsq_f32 v28, v28\n"
    "s_mov_b32 exec_lo, 0x00f0000f\n"
    "s_mov_b32 exec_hi, 0xf0000f00\n"
    "ds_write_b32 v27, v32\n"
    "ds_write_b32 v27, v28 offset:256\n"
    "s_mov_b64 exec, -1\n"
    "s_waitcnt vmcnt(8) lgkmcnt(0)\n"
    "s_barrier\n"
    "s_add_u32 s47, s46, 4\n"
    "s_and_b32 s47, s47, 7\n"
    "s_add_u32 m0, s44, 0\n"
    "s_mul_i32 s45, s47, 196608\n"
    "s_add_u32 s45, s45, s42\n"
    "buffer_load_dwordx4 v20, s[28:31], s45 offen lds\n"
    "buffer_load_dwordx4 v21, s[28:31], s45 offen offset:1024 lds\n"
    "buffer_load_dwordx4 v22, s[28:31], s45 offen offset:2048 lds\n"
    "buffer_load_dwordx4 v23, s[28:31], s45 offen offset:3072 lds\n"
    "s_barrier\n"
    "s_add_u32 s47, s46, 6\n"
    "s_and_b32 s47, s47, 7\n"
    "s_add_u32 m0, s44, 16384\n"
    "s_mul_i32 s45, s47, 196608\n"
    "s_add_u32 s45, s45, s42\n"
    "buffer_load_dwordx4 v20, s[28:31], s45 offen lds\n"
    "buffer_load_dwordx4 v21, s[28:31], s45 offen offset:1024 lds\n"
    "buffer_load_dwordx4 v22, s[28:31], s45 offen offset:2048 lds\n"
    "buffer_load_dwordx4 v23, s[28:31], s45 offen offset:3072 lds\n"
    "s_setprio 0\n"
    "v_lshlrev_b32 v1, 4, v0\n"
    "s_and_b32 s51, s3, 1\n"
    "s_lshl_b32 s51, s51, 5\n"
    "s_bfe_u32 s52, s3, 0x10001\n"
    "v_and_or_b32 v56, v0, 31, s51\n"
    "v_lshlrev_b32 v13, 2, v56\n"
    "v_lshlrev_b32 v56, 7, v56\n"
    "v_add_u32 v56, 16384, v56\n"
    "v_bfe_u32 v57, v0, 1, 3\n"
    "v_lshrrev_b32 v58, 5, v0\n"
    "v_xor_b32 v57, v57, v58\n"
    "v_lshl_add_u32 v2, v57, 4, v56\n"
    "v_xor_b32 v3, 32, v2\n"
    "v_xor_b32 v4, 64, v2\n"
    "v_xor_b32 v5, 96, v2\n"
    "v_bfe_u32 v56, v0, 2, 2\n"
    "v_lshl_add_u32 v56, v58, 3, v56\n"
    "v_lshlrev_b32 v56, 7, v56\n"
    "v_bfe_u32 v57, v0, 3, 1\n"
    "v_xor_b32 v59, s52, v57\n"
    "v_lshl_add_u32 v56, v59, 6, v56\n"
    "v_bfe_u32 v59, v0, 4, 1\n"
    "v_lshl_add_u32 v56, v59, 5, v56\n"
    "v_and_b32 v60, 3, v0\n"
    "v_lshl_add_u32 v56, v60, 3, v56\n"
    "v_add_u32 v6, 81920, v56\n"
    "v_add_u32 v7, 147456, v56\n"
    "v_bfe_u32 v60, v0, 1, 1\n"
    "v_xor_b32 v60, v60, v58\n"
    "v_xor_b32 v59, v59, v57\n"
    "v_or_b32 v59, v59, v60\n"
    "v_bfe_u32 v60, v0, 2, 1\n"
    "v_mov_b32 v56, 0x3f803f80\n"
    "v_or_b32 v57, v59, v60\n"
    "v_cmp_eq_u32 vcc, 0, v57\n"
    "v_xor_b32 v60, 1, v60\n"
    "v_or_b32 v57, v59, v60\n"
    "v_cndmask_b32 v8, 0, v56, vcc\n"
    "v_cmp_eq_u32 vcc, 0, v57\n"
    "v_mov_b32 v9, v8\n"
    "v_and_b32 v57, 1, v0\n"
    "v_cndmask_b32 v10, 0, v56, vcc\n"
    "v_mul_u32_u24 v12, 0xaaaa, v57\n"
    "v_mov_b32 v11, v10\n"
    "v_add_u32 v12, 0x4444, v12\n"
    "s_mul_i32 s57, s3, 6336\n"
    "s_add_u32 s57, s57, 98304\n"
    "s_mul_i32 s59, s39, 24576\n"
    "s_lshr_b32 s60, s33, 2\n"
    "s_and_b32 s61, s33, 3\n"
    "s_lshl_b32 s58, s34, 2\n"
    "s_add_u32 s58, s58, s39\n"
    "s_lshl_b32 s58, s58, 1\n"
    "s_add_u32 s58, s58, s40\n"
    "s_lshl_b32 s58, s58, 16\n"
    "s_add_u32 s58, s58, 2097152\n"
    "s_and_b32 s62, s56, 6\n"
    "s_and_b32 s53, s33, 1\n"
    "s_add_u32 s62, s62, s53\n"
    "s_lshr_b32 s53, s33, 1\n"
    "s_and_b32 s63, s56, 1\n"
    "s_mul_i32 s63, s63, 15\n"
    "s_add_u32 s63, s63, s53\n"
    "s_cmp_lt_u32 s53, 15\n"
    "s_cselect_b32 s63, s63, 31\n"
    "s_cmp_lt_u32 s63, 29\n"
    "s_cselect_b32 s65, 1, 0\n"
    "s_mul_i32 s64, s63, 3\n"
    "s_min_u32 s64, s64, 83\n"
    "s_lshl_b32 s67, s61, 3\n"
    "s_add_u32 s67, s67, s3\n"
    "s_lshl_b32 s66, s67, 5\n"
    "s_lshl_b32 s67, s67, 14\n"
    "s_add_u32 s67, s67, 16121856\n"
    "s_lshl_b32 s68, s64, 15\n"
    "s_add_u32 s68, s68, s59\n"
    "s_add_u32 s68, s68, 16646144\n"
    "s_lshl_b32 s53, s56, 4\n"
    "s_add_u32 s53, s53, s60\n"
    "s_lshl_b32 s53, s53, 14\n"
    "s_add_u32 s66, s66, s53\n"
    "s_lshl_b32 s53, s39, 1\n"
    "s_add_u32 s53, s53, s40\n"
    "s_lshl_b32 s53, s53, 10\n"
    "v_lshl_add_u32 v176, v0, 4, s53\n"
    "v_lshrrev_b32 v56, 1, v0\n"
    "v_mul_u32_u24 v56, 1056, v56\n"
    "v_and_b32 v57, 1, v0\n"
    "v_lshl_add_u32 v56, v57, 4, v56\n"
    "v_add_u32 v177, s57, v56\n"
    "v_add_u32 v57, -1, v57\n"
    "v_and_b32 v178, 0x3f803f80, v57\n"
    "v_and_b32 v56, 15, v0\n"
    "v_lshrrev_b32 v57, 4, v0\n"
    "v_lshlrev_b32 v57, 3, v57\n"
    "v_lshl_or_b32 v86, v56, 10, v57\n"
    "v_lshlrev_b32 v56, 4, v56\n"
    "v_lshrrev_b32 v57, 5, v0\n"
    "v_lshl_or_b32 v56, v57, 8, v56\n"
    "v_add_u32 v57, -1, v57\n"
    "v_and_b32 v87, -1.0, v57\n"
    "v_add_u32 v57, 0x200, v56\n"
    "v_and_b32 v58, 11, v0\n"
    "v_min_u32 v58, 1, v58\n"
    "v_lshlrev_b32 v58, 4, v58\n"
    "v_add_u32 v58, 0x400, v58\n"
    "v_and_b32 v59, 16, v0\n"
    "v_cmp_eq_u32 vcc, 0, v59\n"
    "v_mov_b32 v128, 0\n"
    "v_mov_b32 v129, 0\n"
    "v_mov_b32 v130, 0\n"
    "v_mov_b32 v131, 0\n"
    "v_mov_b32 v132, 0\n"
    "v_mov_b32 v133, 0\n"
    "v_mov_b32 v134, 0\n"
    "v_mov_b32 v135, 0\n"
    "v_mov_b32 v136, 0\n"
    "v_mov_b32 v137, 0\n"
    "v_mov_b32 v138, 0\n"
    "v_mov_b32 v139, 0\n"
    "v_mov_b32 v140, 0\n"
    "v_mov_b32 v141, 0\n"
    "v_mov_b32 v142, 0\n"
    "v_mov_b32 v143, 0\n"
    "v_mov_b32 v144, 0\n"
    "v_mov_b32 v145, 0\n"
    "v_mov_b32 v146, 0\n"
    "v_mov_b32 v147, 0\n"
    "v_mov_b32 v148, 0\n"
    "v_mov_b32 v149, 0\n"
    "v_mov_b32 v150, 0\n"
    "v_mov_b32 v151, 0\n"
    "v_mov_b32 v152, 0\n"
    "v_mov_b32 v153, 0\n"
    "v_mov_b32 v154, 0\n"
    "v_mov_b32 v155, 0\n"
    "v_mov_b32 v156, 0\n"
    "v_mov_b32 v157, 0\n"
    "v_mov_b32 v158, 0\n"
    "v_mov_b32 v159, 0\n"
    "v_cndmask_b32 v56, v58, v56, vcc\n"
    "v_cndmask_b32 v57, v58, v57, vcc\n"
    "v_add_u32 v84, s57, v56\n"
    "v_add_u32 v85, s57, v57\n"
    "s_setprio 0\n"
    "ds_read_b32 v26, v13\n"
    "ds_read_b32 v27, v13 offset:256\n"
    "v_mov_b32 v28, 0\n"
    "v_mov_b32 v29, 0\n"
    "v_mov_b32 v30, 0\n"
    "v_mov_b32 v31, 0\n"
    "v_readfirstlane_b32 s53, v14\n"
    "v_readfirstlane_b32 s54, v15\n"
    "s_mul_hi_u32 s55, s53, 0x4c957f2d\n"
    "s_mul_i32 s54, s54, 0x4c957f2d\n"
    "s_add_u32 s54, s54, s55\n"
    "s_mul_i32 s55, s53, 0x5851f42d\n"
    "s_add_u32 s54, s54, s55\n"
    "s_mul_i32 s53, s53, 0x4c957f2d\n"
    "s_add_u32 s36, s53, 0xf767814f\n"
    "s_addc_u32 s37, s54, 0x14057b7e\n"
    "s_or_b32 s38, s36, 1\n"
    "s_waitcnt vmcnt(0)\n"
    "s_barrier\n"
    "ds_read_b64_tr_b16 v[56:57], v6 offset:49152\n"
    "ds_read_b64_tr_b16 v[58:59], v6 offset:49664\n"
    "ds_read_b128 v[32:35], v2\n"
    "ds_read_b64_tr_b16 v[60:61], v6 offset:53248\n"
    "ds_read_b64_tr_b16 v[62:63], v6 offset:53760\n"
    "ds_read_b128 v[36:39], v3\n"
    "ds_read_b64_tr_b16 v[64:65], v6 offset:57344\n"
    "ds_read_b64_tr_b16 v[66:67], v6 offset:57856\n"
    "ds_read_b128 v[40:43], v4\n"
    "ds_read_b64_tr_b16 v[68:69], v6 offset:61440\n"
    "ds_read_b64_tr_b16 v[70:71], v6 offset:61952\n"
    "ds_read_b128 v[44:47], v5\n"
    "s_waitcnt lgkmcnt(9)\n"
    "v_mfma_f32_32x32x16_bf16 v[96:111], v[56:59], v[32:35], 0\n"
    "ds_read_b64_tr_b16 v[72:73], v6 offset:51200\n"
    "ds_read_b64_tr_b16 v[74:75], v6 offset:51712\n"
    "ds_read_b128 v[48:51], v2 offset:8192\n"
    "s_waitcnt lgkmcnt(9)\n"
    "v_mfma_f32_32x32x16_bf16 v[96:111], v[60:63], v[36:39], v[96:111]\n"
    "ds_read_b64_tr_b16 v[76:77], v6 offset:55296\n"
    "ds_read_b64_tr_b16 v[78:79], v6 offset:55808\n"
    "ds_read_b128 v[52:55], v3 offset:8192\n"
    "v_smfmac_f32_16x16x64_bf16 v[28:31], v[8:11], v[56:63], v12\n"
    "s_waitcnt lgkmcnt(9)\n"
    "v_mfma_f32_32x32x16_bf16 v[96:111], v[64:67], v[40:43], v[96:111]\n"
    "ds_read_b64_tr_b16 v[56:57], v6 offset:59392\n"
    "ds_read_b64_tr_b16 v[58:59], v6 offset:59904\n"
    "ds_read_b128 v[32:35], v4 offset:8192\n"
    "s_waitcnt lgkmcnt(9)\n"
    "v_mfma_f32_32x32x16_bf16 v[96:111], v[68:71], v[44:47], v[96:111]\n"
    "ds_read_b64_tr_b16 v[60:61], v6 offset:63488\n"
    "ds_read_b64_tr_b16 v[62:63], v6 offset:64000\n"
    "ds_read_b128 v[36:39], v5 offset:8192\n"
    "v_smfmac_f32_16x16x64_bf16 v[28:31], v[8:11], v[64:71], v12\n"
    "s_waitcnt lgkmcnt(9)\n"
    "v_mfma_f32_32x32x16_bf16 v[96:111], v[72:75], v[48:51], v[96:111]\n"
    "ds_read_b64_tr_b16 v[64:65], v7\n"
    "ds_read_b64_tr_b16 v[66:67], v7 offset:512\n"
    "ds_read_b128 v[40:43], v2 offset:16384\n"
    "s_waitcnt lgkmcnt(9)\n"
    "v_mfma_f32_32x32x16_bf16 v[96:111], v[76:79], v[52:55], v[96:111]\n"
    "ds_read_b64_tr_b16 v[68:69], v7 offset:4096\n"
    "ds_read_b64_tr_b16 v[70:71], v7 offset:4608\n"
    "ds_read_b128 v[44:47], v3 offset:16384\n"
    "v_smfmac_f32_16x16x64_bf16 v[28:31], v[8:11], v[72:79], v12\n"
    "s_waitcnt lgkmcnt(9)\n"
    "v_mfma_f32_32x32x16_bf16 v[96:111], v[56:59], v[32:35], v[96:111]\n"
    "ds_read_b64_tr_b16 v[72:73], v7 offset:8192\n"
    "ds_read_b64_tr_b16 v[74:75], v7 offset:8704\n"
    "ds_read_b128 v[48:51], v4 offset:16384\n"
    "s_waitcnt lgkmcnt(9)\n"
    "v_mfma_f32_32x32x16_bf16 v[96:111], v[60:63], v[36:39], v[96:111]\n"
    "ds_read_b64_tr_b16 v[76:77], v7 offset:12288\n"
    "ds_read_b64_tr_b16 v[78:79], v7 offset:12800\n"
    "ds_read_b128 v[52:55], v5 offset:16384\n"
    "v_smfmac_f32_16x16x64_bf16 v[28:31], v[8:11], v[56:63], v12\n"
    "s_waitcnt lgkmcnt(9)\n"
    "v_mfma_f32_32x32x16_bf16 v[96:111], v[64:67], v[40:43], v[96:111]\n"
    "ds_read_b64_tr_b16 v[56:57], v7 offset:2048\n"
    "ds_read_b64_tr_b16 v[58:59], v7 offset:2560\n"
    "ds_read_b128 v[32:35], v2 offset:24576\n"
    "s_waitcnt lgkmcnt(9)\n"
    "v_mfma_f32_32x32x16_bf16 v[96:111], v[68:71], v[44:47], v[96:111]\n"
    "ds_read_b64_tr_b16 v[60:61], v7 offset:6144\n"
    "ds_read_b64_tr_b16 v[62:63], v7 offset:6656\n"
    "ds_read_b128 v[36:39], v3 offset:24576\n"
    "v_smfmac_f32_16x16x64_bf16 v[28:31], v[8:11], v[64:71], v12\n"
    "s_waitcnt lgkmcnt(9)\n"
    "v_mfma_f32_32x32x16_bf16 v[96:111], v[72:75], v[48:51], v[96:111]\n"
    "ds_read_b64_tr_b16 v[64:65], v7 offset:10240\n"
    "ds_read_b64_tr_b16 v[66:67], v7 offset:10752\n"
    "ds_read_b128 v[40:43], v4 offset:24576\n"
    "s_waitcnt lgkmcnt(9)\n"
    "v_mfma_f32_32x32x16_bf16 v[96:111], v[76:79], v[52:55], v[96:111]\n"
    "ds_read_b64_tr_b16 v[68:69], v7 offset:14336\n"
    "ds_read_b64_tr_b16 v[70:71], v7 offset:14848\n"
    "ds_read_b128 v[44:47], v5 offset:24576\n"
    "v_smfmac_f32_16x16x64_bf16 v[28:31], v[8:11], v[72:79], v12\n"
    "s_waitcnt lgkmcnt(9)\n"
    "v_mfma_f32_32x32x16_bf16 v[96:111], v[56:59], v[32:35], v[96:111]\n"
    "ds_read_b64_tr_b16 v[72:73], v6\n"
    "ds_read_b64_tr_b16 v[74:75], v6 offset:512\n"
    "ds_read_b128 v[48:51], v2 offset:32768\n"
    "s_waitcnt lgkmcnt(9)\n"
    "v_mfma_f32_32x32x16_bf16 v[96:111], v[60:63], v[36:39], v[96:111]\n"
    "ds_read_b64_tr_b16 v[76:77], v6 offset:4096\n"
    "ds_read_b64_tr_b16 v[78:79], v6 offset:4608\n"
    "ds_read_b128 v[52:55], v3 offset:32768\n"
    "v_smfmac_f32_16x16x64_bf16 v[28:31], v[8:11], v[56:63], v12\n"
    "s_waitcnt lgkmcnt(9)\n"
    "v_mfma_f32_32x32x16_bf16 v[96:111], v[64:67], v[40:43], v[96:111]\n"
    "ds_read_b64_tr_b16 v[56:57], v6 offset:8192\n"
    "ds_read_b64_tr_b16 v[58:59], v6 offset:8704\n"
    "ds_read_b128 v[32:35], v4 offset:32768\n"
    "s_waitcnt lgkmcnt(9)\n"
    "v_mfma_f32_32x32x16_bf16 v[96:111], v[68:71], v[44:47], v[96:111]\n"
    "ds_read_b64_tr_b16 v[60:61], v6 offset:12288\n"
    "ds_read_b64_tr_b16 v[62:63], v6 offset:12800\n"
    "ds_read_b128 v[36:39], v5 offset:32768\n"
    "v_smfmac_f32_16x16x64_bf16 v[28:31], v[8:11], v[64:71], v12\n"
    "s_waitcnt lgkmcnt(9)\n"
    "v_mfma_f32_32x32x16_bf16 v[96:111], v[72:75], v[48:51], v[96:111]\n"
    "ds_read_b64_tr_b16 v[64:65], v6 offset:2048\n"
    "ds_read_b64_tr_b16 v[66:67], v6 offset:2560\n"
    "ds_read_b128 v[40:43], v2 offset:40960\n"
    "s_waitcnt lgkmcnt(9)\n"
    "v_mfma_f32_32x32x16_bf16 v[96:111], v[76:79], v[52:55], v[96:111]\n"
    "ds_read_b64_tr_b16 v[68:69], v6 offset:6144\n"
    "ds_read_b64_tr_b16 v[70:71], v6 offset:6656\n"
    "ds_read_b128 v[44:47], v3 offset:40960\n"
    "v_smfmac_f32_16x16x64_bf16 v[28:31], v[8:11], v[72:79], v12\n"
    "s_waitcnt lgkmcnt(9)\n"
    "v_mfma_f32_32x32x16_bf16 v[96:111], v[56:59], v[32:35], v[96:111]\n"
    "ds_read_b64_tr_b16 v[72:73], v6 offset:10240\n"
    "ds_read_b64_tr_b16 v[74:75], v6 offset:10752\n"
    "ds_read_b128 v[48:51], v4 offset:40960\n"
    "s_waitcnt lgkmcnt(9)\n"
    "v_mfma_f32_32x32x16_bf16 v[96:111], v[60:63], v[36:39], v[96:111]\n"
    "ds_read_b64_tr_b16 v[76:77], v6 offset:14336\n"
    "ds_read_b64_tr_b16 v[78:79], v6 offset:14848\n"
    "ds_read_b128 v[52:55], v5 offset:40960\n"
    "v_smfmac_f32_16x16x64_bf16 v[28:31], v[8:11], v[56:63], v12\n"
    "s_waitcnt lgkmcnt(9)\n"
    "v_mfma_f32_32x32x16_bf16 v[96:111], v[64:67], v[40:43], v[96:111]\n"
    "ds_read_b64_tr_b16 v[56:57], v6 offset:16384\n"
    "ds_read_b64_tr_b16 v[58:59], v6 offset:16896\n"
    "ds_read_b128 v[32:35], v2 offset:49152\n"
    "s_waitcnt lgkmcnt(9)\n"
    "v_mfma_f32_32x32x16_bf16 v[96:111], v[68:71], v[44:47], v[96:111]\n"
    "ds_read_b64_tr_b16 v[60:61], v6 offset:20480\n"
    "ds_read_b64_tr_b16 v[62:63], v6 offset:20992\n"
    "ds_read_b128 v[36:39], v3 offset:49152\n"
    "v_smfmac_f32_16x16x64_bf16 v[28:31], v[8:11], v[64:71], v12\n"
    "s_waitcnt lgkmcnt(9)\n"
    "v_mfma_f32_32x32x16_bf16 v[96:111], v[72:75], v[48:51], v[96:111]\n"
    "ds_read_b64_tr_b16 v[64:65], v6 offset:24576\n"
    "ds_read_b64_tr_b16 v[66:67], v6 offset:25088\n"
    "ds_read_b128 v[40:43], v4 offset:49152\n"
    "s_waitcnt lgkmcnt(9)\n"
    "v_mfma_f32_32x32x16_bf16 v[96:111], v[76:79], v[52:55], v[96:111]\n"
    "ds_read_b64_tr_b16 v[68:69], v6 offset:28672\n"
    "ds_read_b64_tr_b16 v[70:71], v6 offset:29184\n"
    "ds_read_b128 v[44:47], v5 offset:49152\n"
    "v_smfmac_f32_16x16x64_bf16 v[28:31], v[8:11], v[72:79], v12\n"
    "s_waitcnt lgkmcnt(9)\n"
    "v_mfma_f32_32x32x16_bf16 v[96:111], v[56:59], v[32:35], v[96:111]\n"
    "ds_read_b64_tr_b16 v[72:73], v6 offset:18432\n"
    "ds_read_b64_tr_b16 v[74:75], v6 offset:18944\n"
    "ds_read_b128 v[48:51], v2 offset:57344\n"
    "s_waitcnt lgkmcnt(9)\n"
    "v_mfma_f32_32x32x16_bf16 v[96:111], v[60:63], v[36:39], v[96:111]\n"
    "ds_read_b64_tr_b16 v[76:77], v6 offset:22528\n"
    "ds_read_b64_tr_b16 v[78:79], v6 offset:23040\n"
    "ds_read_b128 v[52:55], v3 offset:57344\n"
    "v_smfmac_f32_16x16x64_bf16 v[28:31], v[8:11], v[56:63], v12\n"
    "s_waitcnt lgkmcnt(9)\n"
    "v_mfma_f32_32x32x16_bf16 v[96:111], v[64:67], v[40:43], v[96:111]\n"
    "ds_read_b64_tr_b16 v[56:57], v6 offset:26624\n"
    "ds_read_b64_tr_b16 v[58:59], v6 offset:27136\n"
    "ds_read_b128 v[32:35], v4 offset:57344\n"
    "s_waitcnt lgkmcnt(9)\n"
    "v_mfma_f32_32x32x16_bf16 v[96:111], v[68:71], v[44:47], v[96:111]\n"
    "ds_read_b64_tr_b16 v[60:61], v6 offset:30720\n"
    "ds_read_b64_tr_b16 v[62:63], v6 offset:31232\n"
    "ds_read_b128 v[36:39], v5 offset:57344\n"
    "v_smfmac_f32_16x16x64_bf16 v[28:31], v[8:11], v[64:71], v12\n"
    "s_waitcnt lgkmcnt(9)\n"
    "v_mfma_f32_32x32x16_bf16 v[96:111], v[72:75], v[48:51], v[96:111]\n"
    "s_waitcnt lgkmcnt(6)\n"
    "v_mfma_f32_32x32x16_bf16 v[96:111], v[76:79], v[52:55], v[96:111]\n"
    "v_smfmac_f32_16x16x64_bf16 v[28:31], v[8:11], v[72:79], v12\n"
    "s_waitcnt lgkmcnt(3)\n"
    "v_mfma_f32_32x32x16_bf16 v[96:111], v[56:59], v[32:35], v[96:111]\n"
    "s_waitcnt lgkmcnt(0)\n"
    "v_mfma_f32_32x32x16_bf16 v[96:111], v[60:63], v[36:39], v[96:111]\n"
    "v_smfmac_f32_16x16x64_bf16 v[28:31], v[8:11], v[56:63], v12\n"
    "s_nop 7\n"
    "v_add_f32 v28, v28, v29\n"
    "v_add_f32 v30, v30, v31\n"
    "v_xor_b32 v26, 0x80000000, v26\n"
    "v_add_f32 v28, v28, v30\n"
    "s_nop 1\n"
    "v_mfma_f32_32x32x2_f32 v[96:111], v28, v26, v[96:111]\n"
    "v_mul_f32 v27, 0x3eb8aa3b, v27\n"
    "s_lshr_b32 s46, s3, 1\n"
    "s_lshl_b32 s46, s46, 12\n"
    "s_and_b32 s47, s3, 1\n"
    "s_lshl_b32 s47, s47, 10\n"
    "s_add_u32 s46, s46, s47\n"
    "v_add_u32 v29, s46, v1\n"
    "s_nop 10\n"
    "v_mul_f32 v96, v27, v96\n"
    "v_mul_f32 v97, v27, v97\n"
    "v_mul_f32 v98, v27, v98\n"
    "v_mul_f32 v99, v27, v99\n"
    "v_mul_f32 v100, v27, v100\n"
    "v_mul_f32 v101, v27, v101\n"
    "v_mul_f32 v102, v27, v102\n"
    "v_mul_f32 v103, v27, v103\n"
    "v_cvt_pk_bf16_f32 v32, v96, v97\n"
    "v_cvt_pk_bf16_f32 v33, v98, v99\n"
    "v_cvt_pk_bf16_f32 v34, v100, v101\n"
    "v_cvt_pk_bf16_f32 v35, v102, v103\n"
    "ds_write_b128 v29, v[32:35] offset:8192\n"
    "v_mul_f32 v104, v27, v104\n"
    "v_mul_f32 v105, v27, v105\n"
    "v_mul_f32 v106, v27, v106\n"
    "v_mul_f32 v107, v27, v107\n"
    "v_mul_f32 v108, v27, v108\n"
    "v_mul_f32 v109, v27, v109\n"
    "v_mul_f32 v110, v27, v110\n"
    "v_mul_f32 v111, v27, v111\n"
    "v_cvt_pk_bf16_f32 v36, v104, v105\n"
    "v_cvt_pk_bf16_f32 v37, v106, v107\n"
    "v_cvt_pk_bf16_f32 v38, v108, v109\n"
    "v_cvt_pk_bf16_f32 v39, v110, v111\n"
    "ds_write_b128 v29, v[36:39] offset:10240\n"
    "s_branch .Lk_a_done\n"
    ".Lk_consumer:\n"
    "s_setprio 1\n"
    "v_lshlrev_b32 v1, 4, v0\n"
    "s_and_b32 s51, s3, 1\n"
    "s_lshl_b32 s51, s51, 5\n"
    "s_bfe_u32 s52, s3, 0x10001\n"
    "v_and_or_b32 v44, v0, 31, s51\n"
    "v_lshlrev_b32 v13, 2, v44\n"
    "v_lshlrev_b32 v44, 7, v44\n"
    "v_add_u32 v44, 16384, v44\n"
    "v_bfe_u32 v45, v0, 1, 3\n"
    "v_lshrrev_b32 v46, 5, v0\n"
    "v_xor_b32 v45, v45, v46\n"
    "v_lshl_add_u32 v2, v45, 4, v44\n"
    "v_xor_b32 v3, 32, v2\n"
    "v_xor_b32 v4, 64, v2\n"
    "v_xor_b32 v5, 96, v2\n"
    "v_bfe_u32 v44, v0, 2, 2\n"
    "v_lshl_add_u32 v44, v46, 3, v44\n"
    "v_lshlrev_b32 v44, 7, v44\n"
    "v_bfe_u32 v45, v0, 3, 1\n"
    "v_xor_b32 v47, s52, v45\n"
    "v_lshl_add_u32 v44, v47, 6, v44\n"
    "v_bfe_u32 v47, v0, 4, 1\n"
    "v_lshl_add_u32 v44, v47, 5, v44\n"
    "v_and_b32 v48, 3, v0\n"
    "v_lshl_add_u32 v44, v48, 3, v44\n"
    "v_add_u32 v6, 81920, v44\n"
    "v_add_u32 v7, 147456, v44\n"
    "v_bfe_u32 v48, v0, 1, 1\n"
    "v_xor_b32 v48, v48, v46\n"
    "v_xor_b32 v47, v47, v45\n"
    "v_or_b32 v47, v47, v48\n"
    "v_bfe_u32 v48, v0, 2, 1\n"
    "v_mov_b32 v44, 0x3f803f80\n"
    "v_or_b32 v45, v47, v48\n"
    "v_cmp_eq_u32 vcc, 0, v45\n"
    "v_xor_b32 v48, 1, v48\n"
    "v_or_b32 v45, v47, v48\n"
    "v_cndmask_b32 v8, 0, v44, vcc\n"
    "v_cmp_eq_u32 vcc, 0, v45\n"
    "v_mov_b32 v9, v8\n"
    "v_and_b32 v45, 1, v0\n"
    "v_cndmask_b32 v10, 0, v44, vcc\n"
    "v_mul_u32_u24 v12, 0xaaaa, v45\n"
    "v_mov_b32 v11, v10\n"
    "v_add_u32 v12, 0x4444, v12\n"
    "s_waitcnt lgkmcnt(0)\n"
    "s_mov_b32 s20, s18\n"
    "s_and_b32 s21, s19, 0xffff\n"
    "s_mov_b32 s22, 0x13e8000\n"
    "s_mov_b32 s23, 0x20000\n"
    "v_mov_b32 v14, 0\n"
    "buffer_load_dwordx2 v[14:15], v14, s[20:23], 0 offen sc1\n"
    "v_mov_b32 v16, 0\n"
    "v_mov_b32 v17, 0\n"
    "v_mov_b32 v18, 0\n"
    "v_mov_b32 v19, 0\n"
    "ds_write_b64 v16, v[16:17] offset:512\n"
    "s_lshl_b32 s48, s2, 2\n"
    "s_add_u32 s48, s48, s39\n"
    "v_lshrrev_b32 v44, 3, v0\n"
    "v_and_b32 v45, 7, v0\n"
    "v_lshlrev_b32 v45, 4, v45\n"
    "v_lshl_add_u32 v21, v44, 10, v45\n"
    "v_mov_b32 v20, v21\n"
    "s_mul_i32 s53, s39, 24576\n"
    "v_bfe_u32 v46, v0, 2, 2\n"
    "v_and_b32 v47, 3, v0\n"
    "v_lshlrev_b32 v47, 3, v47\n"
    "v_lshl_add_u32 v46, v46, 7, v47\n"
    "v_add_u32 v46, s53, v46\n"
    "v_lshrrev_b32 v47, 4, v0\n"
    "v_lshl_add_u32 v24, v47, 10, v46\n"
    "v_add_u32 v25, 0x200, v24\n"
    "v_mov_b32 v22, v24\n"
    "v_mov_b32 v23, v25\n"
    "v_add_u32 v26, s53, v1\n"
    "s_mov_b32 s71, 0x2000\n"
    "s_mov_b32 s74, 4\n"
    "s_mov_b32 s26, 1409024\n"
    "s_mov_b32 s27, 0x20000\n"
    "s_and_b32 s50, s48, 7\n"
    "s_cmp_lt_u32 s48, 704\n"
    "s_cbranch_scc1 .Lk_j13\n"
    "s_cmp_lt_u32 s48, 832\n"
    "s_cbranch_scc1 .Lk_jwo\n"
    "s_sub_u32 s49, s48, 832\n"
    "s_lshr_b32 s49, s49, 3\n"
    "s_mov_b64 s[24:25], s[14:15]\n"
    "s_mul_i32 s72, s50, 43\n"
    "s_add_u32 s72, s72, s49\n"
    "s_lshl_b32 s72, s72, 12\n"
    "s_add_u32 s72, s72, 19464192\n"
    "s_mov_b32 s73, 0x400\n"
    "s_lshl_b32 s70, s49, 15\n"
    "s_branch .Lk_jend\n"
    ".Lk_jwo:\n"
    "s_sub_u32 s49, s48, 704\n"
    "s_lshr_b32 s49, s49, 3\n"
    "s_mov_b64 s[24:25], s[8:9]\n"
    "s_mov_b32 s26, 524288\n"
    "s_lshl_b32 s72, s50, 6\n"
    "s_add_u32 s72, s72, s49\n"
    "s_lshl_b32 s72, s72, 10\n"
    "s_add_u32 s72, s72, 16121856\n"
    "s_mov_b32 s73, 0x4000\n"
    "s_lshl_b32 s70, s49, 15\n"
    "v_and_b32 v47, 16, v0\n"
    "v_lshlrev_b32 v47, 5, v47\n"
    "v_sub_u32 v22, v24, v47\n"
    "v_add_u32 v23, 0x400, v22\n"
    "s_branch .Lk_jend\n"
    ".Lk_j13:\n"
    "s_mul_i32 s49, s48, 2979\n"
    "s_lshr_b32 s49, s49, 16\n"
    "s_mul_i32 s50, s49, 22\n"
    "s_sub_u32 s50, s48, s50\n"
    "s_cmp_ge_u32 s49, 16\n"
    "s_cselect_b64 s[24:25], s[12:13], s[10:11]\n"
    "s_cmp_eq_u32 s50, 21\n"
    "s_cselect_b32 s74, 2, 4\n"
    "s_lshl_b32 s72, s50, 7\n"
    "s_add_u32 s72, s72, s49\n"
    "s_lshl_b32 s72, s72, 10\n"
    "s_add_u32 s72, s72, 16646144\n"
    "s_mov_b32 s73, 0x8000\n"
    "s_and_b32 s70, s49, 15\n"
    "s_mul_i32 s70, s70, 88064\n"
    "s_mov_b32 s71, 22016\n"
    "v_mul_u32_u24 v44, 0xac0, v44\n"
    "v_add_u32 v20, v44, v45\n"
    ".Lk_jend:\n"
    "s_lshl_b32 s50, s50, 7\n"
    "s_add_u32 s70, s70, s50\n"
    "s_and_b32 s25, s25, 0xffff\n"
    "s_add_u32 s49, s2, 192\n"
    "s_lshr_b32 s49, s49, 3\n"
    "s_mul_i32 s76, s56, 43\n"
    "s_add_u32 s76, s76, s49\n"
    "s_lshl_b32 s76, s76, 12\n"
    "s_add_u32 s76, s76, 19464192\n"
    "s_lshl_b32 s75, s49, 15\n"
    "s_lshl_b32 s50, s56, 7\n"
    "s_add_u32 s75, s75, s50\n"
    "s_and_b32 s50, s33, 3\n"
    "s_cmp_eq_u32 s50, s39\n"
    "s_cselect_b32 s48, s2, 0xff\n"
    "s_cmp_lt_u32 s48, 152\n"
    "s_cselect_b32 s48, 1, 0\n"
    "s_cmp_ge_u32 s39, 2\n"
    "s_cbranch_scc0 .Lk_c_touch\n"
    "s_lshl_b32 s49, s2, 1\n"
    "s_add_u32 s49, s49, s39\n"
    "s_lshl_b32 s49, s49, 10\n"
    "s_add_u32 s49, s49, 1046528\n"
    "buffer_store_dwordx4 v[16:19], v1, s[20:23], s49 offen sc1\n"
    "s_branch .Lk_c_go\n"
    ".Lk_c_touch:\n"
    ".Lk_c_go:\n"
    "s_mov_b32 s28, s14\n"
    "s_and_b32 s29, s15, 0xffff\n"
    "s_mov_b32 s30, 0x158000\n"
    "s_mov_b32 s31, 0x20000\n"
    "v_mov_b32 v28, 0\n"
    "v_mov_b32 v29, 0\n"
    "v_mov_b32 v30, 0\n"
    "v_mov_b32 v31, 0\n"
    "s_and_b32 s49, s3, 1\n"
    "s_lshl_b32 s49, s49, 7\n"
    "v_lshrrev_b32 v34, 5, v0\n"
    "v_lshl_add_u32 v34, v34, 4, s49\n"
    "v_and_b32 v45, 51, v0\n"
    "v_and_b32 v46, 4, v0\n"
    "v_lshl_or_b32 v45, v46, 1, v45\n"
    "v_bfe_u32 v46, v0, 3, 1\n"
    "v_lshl_or_b32 v45, v46, 2, v45\n"
    "v_lshlrev_b32 v35, 4, v45\n"
    "v_and_b32 v45, 15, v0\n"
    "v_lshlrev_b32 v45, 4, v45\n"
    "v_lshrrev_b32 v46, 5, v0\n"
    "v_lshl_or_b32 v45, v46, 8, v45\n"
    "v_bfe_u32 v46, v0, 4, 1\n"
    "v_lshl_or_b32 v36, v46, 17, v45\n"
    "s_lshl_b32 s49, s34, 2\n"
    "s_and_b32 s50, s3, 2\n"
    "s_add_u32 s49, s49, s50\n"
    "s_lshl_b32 s49, s49, 5\n"
    "s_add_u32 s49, s49, s35\n"
    "s_lshl_b32 s49, s49, 1\n"
    "s_and_b32 s50, s3, 1\n"
    "s_add_u32 s49, s49, s50\n"
    "s_lshl_b32 s49, s49, 11\n"
    "s_add_u32 s49, s49, 2097152\n"
    "s_add_u32 s50, s49, 0x20000\n"
    "s_lshl_b32 s51, s34, 10\n"
    "s_add_u32 s51, s51, s35\n"
    "s_lshl_b32 s51, s51, 3\n"
    "s_add_u32 s51, s51, 65536\n"
    "v_lshlrev_b32 v37, 10, v0\n"
    "s_and_b32 s52, s3, 2\n"
    "s_lshl_b32 s52, s52, 10\n"
    "v_add_u32 v38, s52, v1\n"
    "s_waitcnt lgkmcnt(0)\n"
    "s_barrier\n"
    "ds_read_b128 v[48:51], v2\n"
    "ds_read_b64_tr_b16 v[40:41], v6\n"
    "ds_read_b64_tr_b16 v[42:43], v6 offset:512\n"
    "ds_read_b64_tr_b16 v[44:45], v6 offset:2048\n"
    "ds_read_b64_tr_b16 v[46:47], v6 offset:2560\n"
    "ds_read_b128 v[60:63], v3\n"
    "ds_read_b64_tr_b16 v[52:53], v6 offset:4096\n"
    "ds_read_b64_tr_b16 v[54:55], v6 offset:4608\n"
    "ds_read_b64_tr_b16 v[56:57], v6 offset:6144\n"
    "ds_read_b64_tr_b16 v[58:59], v6 offset:6656\n"
    "s_waitcnt lgkmcnt(7)\n"
    "v_mfma_f32_32x32x16_bf16 v[96:111], v[40:43], v[48:51], 0\n"
    "ds_read_b128 v[72:75], v4\n"
    "ds_read_b64_tr_b16 v[64:65], v6 offset:8192\n"
    "ds_read_b64_tr_b16 v[66:67], v6 offset:8704\n"
    "s_waitcnt lgkmcnt(8)\n"
    "v_mfma_f32_32x32x16_bf16 v[112:127], v[48:51], v[44:47], 0\n"
    "ds_read_b64_tr_b16 v[68:69], v6 offset:10240\n"
    "ds_read_b64_tr_b16 v[70:71], v6 offset:10752\n"
    "v_smfmac_f32_16x16x64_bf16 v[28:31], v[8:11], v[40:47], v12\n"
    "s_waitcnt lgkmcnt(7)\n"
    "v_mfma_f32_32x32x16_bf16 v[96:111], v[52:55], v[60:63], v[96:111]\n"
    "ds_read_b128 v[48:51], v5\n"
    "ds_read_b64_tr_b16 v[40:41], v6 offset:12288\n"
    "ds_read_b64_tr_b16 v[42:43], v6 offset:12800\n"
    "s_waitcnt lgkmcnt(8)\n"
    "v_mfma_f32_32x32x16_bf16 v[112:127], v[60:63], v[56:59], v[112:127]\n"
    "ds_read_b64_tr_b16 v[44:45], v6 offset:14336\n"
    "ds_read_b64_tr_b16 v[46:47], v6 offset:14848\n"
    "v_smfmac_f32_16x16x64_bf16 v[28:31], v[8:11], v[52:59], v12\n"
    "s_barrier\n"
    "s_waitcnt lgkmcnt(7)\n"
    "v_mfma_f32_32x32x16_bf16 v[96:111], v[64:67], v[72:75], v[96:111]\n"
    "ds_read_b128 v[60:63], v2 offset:8192\n"
    "ds_read_b64_tr_b16 v[52:53], v6 offset:16384\n"
    "ds_read_b64_tr_b16 v[54:55], v6 offset:16896\n"
    "s_waitcnt lgkmcnt(8)\n"
    "v_mfma_f32_32x32x16_bf16 v[112:127], v[72:75], v[68:71], v[112:127]\n"
    "ds_read_b64_tr_b16 v[56:57], v6 offset:18432\n"
    "ds_read_b64_tr_b16 v[58:59], v6 offset:18944\n"
    "v_smfmac_f32_16x16x64_bf16 v[28:31], v[8:11], v[64:71], v12\n"
    "s_waitcnt lgkmcnt(7)\n"
    "v_mfma_f32_32x32x16_bf16 v[96:111], v[40:43], v[48:51], v[96:111]\n"
    "ds_read_b128 v[72:75], v3 offset:8192\n"
    "ds_read_b64_tr_b16 v[64:65], v6 offset:20480\n"
    "ds_read_b64_tr_b16 v[66:67], v6 offset:20992\n"
    "s_waitcnt lgkmcnt(8)\n"
    "v_mfma_f32_32x32x16_bf16 v[112:127], v[48:51], v[44:47], v[112:127]\n"
    "ds_read_b64_tr_b16 v[68:69], v6 offset:22528\n"
    "ds_read_b64_tr_b16 v[70:71], v6 offset:23040\n"
    "v_smfmac_f32_16x16x64_bf16 v[28:31], v[8:11], v[40:47], v12\n"
    "s_waitcnt lgkmcnt(7)\n"
    "v_mfma_f32_32x32x16_bf16 v[96:111], v[52:55], v[60:63], v[96:111]\n"
    "ds_read_b128 v[48:51], v4 offset:8192\n"
    "ds_read_b64_tr_b16 v[40:41], v6 offset:24576\n"
    "ds_read_b64_tr_b16 v[42:43], v6 offset:25088\n"
    "s_waitcnt lgkmcnt(8)\n"
    "v_mfma_f32_32x32x16_bf16 v[112:127], v[60:63], v[56:59], v[112:127]\n"
    "ds_read_b64_tr_b16 v[44:45], v6 offset:26624\n"
    "ds_read_b64_tr_b16 v[46:47], v6 offset:27136\n"
    "v_smfmac_f32_16x16x64_bf16 v[28:31], v[8:11], v[52:59], v12\n"
    "s_waitcnt lgkmcnt(7)\n"
    "v_mfma_f32_32x32x16_bf16 v[96:111], v[64:67], v[72:75], v[96:111]\n"
    "ds_read_b128 v[60:63], v5 offset:8192\n"
    "ds_read_b64_tr_b16 v[52:53], v6 offset:28672\n"
    "ds_read_b64_tr_b16 v[54:55], v6 offset:29184\n"
    "s_waitcnt lgkmcnt(8)\n"
    "v_mfma_f32_32x32x16_bf16 v[112:127], v[72:75], v[68:71], v[112:127]\n"
    "ds_read_b64_tr_b16 v[56:57], v6 offset:30720\n"
    "ds_read_b64_tr_b16 v[58:59], v6 offset:31232\n"
    "v_smfmac_f32_16x16x64_bf16 v[28:31], v[8:11], v[64:71], v12\n"
    "s_barrier\n"
    "s_waitcnt lgkmcnt(7)\n"
    "v_mfma_f32_32x32x16_bf16 v[96:111], v[40:43], v[48:51], v[96:111]\n"
    "ds_read_b128 v[72:75], v2 offset:16384\n"
    "ds_read_b64_tr_b16 v[64:65], v6 offset:32768\n"
    "ds_read_b64_tr_b16 v[66:67], v6 offset:33280\n"
    "s_waitcnt lgkmcnt(8)\n"
    "v_mfma_f32_32x32x16_bf16 v[112:127], v[48:51], v[44:47], v[112:127]\n"
    "ds_read_b64_tr_b16 v[68:69], v6 offset:34816\n"
    "ds_read_b64_tr_b16 v[70:71], v6 offset:35328\n"
    "v_smfmac_f32_16x16x64_bf16 v[28:31], v[8:11], v[40:47], v12\n"
    "s_waitcnt lgkmcnt(7)\n"
    "v_mfma_f32_32x32x16_bf16 v[96:111], v[52:55], v[60:63], v[96:111]\n"
    "ds_read_b128 v[48:51], v3 offset:16384\n"
    "ds_read_b64_tr_b16 v[40:41], v6 offset:36864\n"
    "ds_read_b64_tr_b16 v[42:43], v6 offset:37376\n"
    "s_waitcnt lgkmcnt(8)\n"
    "v_mfma_f32_32x32x16_bf16 v[112:127], v[60:63], v[56:59], v[112:127]\n"
    "ds_read_b64_tr_b16 v[44:45], v6 offset:38912\n"
    "ds_read_b64_tr_b16 v[46:47], v6 offset:39424\n"
    "v_smfmac_f32_16x16x64_bf16 v[28:31], v[8:11], v[52:59], v12\n"
    "s_waitcnt lgkmcnt(7)\n"
    "v_mfma_f32_32x32x16_bf16 v[96:111], v[64:67], v[72:75], v[96:111]\n"
    "ds_read_b128 v[60:63], v4 offset:16384\n"
    "ds_read_b64_tr_b16 v[52:53], v6 offset:40960\n"
    "ds_read_b64_tr_b16 v[54:55], v6 offset:41472\n"
    "s_waitcnt lgkmcnt(8)\n"
    "v_mfma_f32_32x32x16_bf16 v[112:127], v[72:75], v[68:71], v[112:127]\n"
    "ds_read_b64_tr_b16 v[56:57], v6 offset:43008\n"
    "ds_read_b64_tr_b16 v[58:59], v6 offset:43520\n"
    "v_smfmac_f32_16x16x64_bf16 v[28:31], v[8:11], v[64:71], v12\n"
    "s_waitcnt lgkmcnt(7)\n"
    "v_mfma_f32_32x32x16_bf16 v[96:111], v[40:43], v[48:51], v[96:111]\n"
    "ds_read_b128 v[72:75], v5 offset:16384\n"
    "ds_read_b64_tr_b16 v[64:65], v6 offset:45056\n"
    "ds_read_b64_tr_b16 v[66:67], v6 offset:45568\n"
    "s_waitcnt lgkmcnt(8)\n"
    "v_mfma_f32_32x32x16_bf16 v[112:127], v[48:51], v[44:47], v[112:127]\n"
    "ds_read_b64_tr_b16 v[68:69], v6 offset:47104\n"
    "ds_read_b64_tr_b16 v[70:71], v6 offset:47616\n"
    "v_smfmac_f32_16x16x64_bf16 v[28:31], v[8:11], v[40:47], v12\n"
    "s_barrier\n"
    "s_waitcnt lgkmcnt(7)\n"
    "v_mfma_f32_32x32x16_bf16 v[96:111], v[52:55], v[60:63], v[96:111]\n"
    "ds_read_b128 v[48:51], v2 offset:24576\n"
    "ds_read_b64_tr_b16 v[40:41], v6 offset:49152\n"
    "ds_read_b64_tr_b16 v[42:43], v6 offset:49664\n"
    "s_waitcnt lgkmcnt(8)\n"
    "v_mfma_f32_32x32x16_bf16 v[112:127], v[60:63], v[56:59], v[112:127]\n"
    "ds_read_b64_tr_b16 v[44:45], v6 offset:51200\n"
    "ds_read_b64_tr_b16 v[46:47], v6 offset:51712\n"
    "v_smfmac_f32_16x16x64_bf16 v[28:31], v[8:11], v[52:59], v12\n"
    "s_waitcnt lgkmcnt(7)\n"
    "v_mfma_f32_32x32x16_bf16 v[96:111], v[64:67], v[72:75], v[96:111]\n"
    "ds_read_b128 v[60:63], v3 offset:24576\n"
    "ds_read_b64_tr_b16 v[52:53], v6 offset:53248\n"
    "ds_read_b64_tr_b16 v[54:55], v6 offset:53760\n"
    "s_waitcnt lgkmcnt(8)\n"
    "v_mfma_f32_32x32x16_bf16 v[112:127], v[72:75], v[68:71], v[112:127]\n"
    "ds_read_b64_tr_b16 v[56:57], v6 offset:55296\n"
    "ds_read_b64_tr_b16 v[58:59], v6 offset:55808\n"
    "v_smfmac_f32_16x16x64_bf16 v[28:31], v[8:11], v[64:71], v12\n"
    "s_waitcnt lgkmcnt(7)\n"
    "v_mfma_f32_32x32x16_bf16 v[96:111], v[40:43], v[48:51], v[96:111]\n"
    "ds_read_b128 v[72:75], v4 offset:24576\n"
    "ds_read_b64_tr_b16 v[64:65], v6 offset:57344\n"
    "ds_read_b64_tr_b16 v[66:67], v6 offset:57856\n"
    "s_waitcnt lgkmcnt(8)\n"
    "v_mfma_f32_32x32x16_bf16 v[112:127], v[48:51], v[44:47], v[112:127]\n"
    "ds_read_b64_tr_b16 v[68:69], v6 offset:59392\n"
    "ds_read_b64_tr_b16 v[70:71], v6 offset:59904\n"
    "v_smfmac_f32_16x16x64_bf16 v[28:31], v[8:11], v[40:47], v12\n"
    "s_waitcnt lgkmcnt(7)\n"
    "v_mfma_f32_32x32x16_bf16 v[96:111], v[52:55], v[60:63], v[96:111]\n"
    "ds_read_b128 v[48:51], v5 offset:24576\n"
    "ds_read_b64_tr_b16 v[40:41], v6 offset:61440\n"
    "ds_read_b64_tr_b16 v[42:43], v6 offset:61952\n"
    "s_waitcnt lgkmcnt(8)\n"
    "v_mfma_f32_32x32x16_bf16 v[112:127], v[60:63], v[56:59], v[112:127]\n"
    "ds_read_b64_tr_b16 v[44:45], v6 offset:63488\n"
    "ds_read_b64_tr_b16 v[46:47], v6 offset:64000\n"
    "v_smfmac_f32_16x16x64_bf16 v[28:31], v[8:11], v[52:59], v12\n"
    "s_barrier\n"
    "s_waitcnt lgkmcnt(7)\n"
    "v_mfma_f32_32x32x16_bf16 v[96:111], v[64:67], v[72:75], v[96:111]\n"
    "ds_read_b128 v[60:63], v2 offset:32768\n"
    "ds_read_b64_tr_b16 v[52:53], v7\n"
    "ds_read_b64_tr_b16 v[54:55], v7 offset:512\n"
    "s_waitcnt lgkmcnt(8)\n"
    "v_mfma_f32_32x32x16_bf16 v[112:127], v[72:75], v[68:71], v[112:127]\n"
    "ds_read_b64_tr_b16 v[56:57], v7 offset:2048\n"
    "ds_read_b64_tr_b16 v[58:59], v7 offset:2560\n"
    "v_smfmac_f32_16x16x64_bf16 v[28:31], v[8:11], v[64:71], v12\n"
    "s_waitcnt vmcnt(0)\n"
    "v_readfirstlane_b32 s53, v14\n"
    "v_readfirstlane_b32 s54, v15\n"
    "s_mul_hi_u32 s55, s53, 0x4c957f2d\n"
    "s_mul_i32 s54, s54, 0x4c957f2d\n"
    "s_add_u32 s54, s54, s55\n"
    "s_mul_i32 s55, s53, 0x5851f42d\n"
    "s_add_u32 s54, s54, s55\n"
    "s_mul_i32 s53, s53, 0x4c957f2d\n"
    "s_add_u32 s36, s53, 0xf767814f\n"
    "s_addc_u32 s37, s54, 0x14057b7e\n"
    "s_or_b32 s38, s36, 1\n"
    "s_waitcnt lgkmcnt(7)\n"
    "v_mfma_f32_32x32x16_bf16 v[96:111], v[40:43], v[48:51], v[96:111]\n"
    "ds_read_b128 v[72:75], v3 offset:32768\n"
    "ds_read_b64_tr_b16 v[64:65], v7 offset:4096\n"
    "ds_read_b64_tr_b16 v[66:67], v7 offset:4608\n"
    "s_waitcnt lgkmcnt(8)\n"
    "v_mfma_f32_32x32x16_bf16 v[112:127], v[48:51], v[44:47], v[112:127]\n"
    "ds_read_b64_tr_b16 v[68:69], v7 offset:6144\n"
    "ds_read_b64_tr_b16 v[70:71], v7 offset:6656\n"
    "v_smfmac_f32_16x16x64_bf16 v[28:31], v[8:11], v[40:47], v12\n"
    "s_waitcnt lgkmcnt(7)\n"
    "v_mfma_f32_32x32x16_bf16 v[96:111], v[52:55], v[60:63], v[96:111]\n"
    "ds_read_b128 v[48:51], v4 offset:32768\n"
    "ds_read_b64_tr_b16 v[40:41], v7 offset:8192\n"
    "ds_read_b64_tr_b16 v[42:43], v7 offset:8704\n"
    "s_waitcnt lgkmcnt(8)\n"
    "v_mfma_f32_32x32x16_bf16 v[112:127], v[60:63], v[56:59], v[112:127]\n"
    "ds_read_b64_tr_b16 v[44:45], v7 offset:10240\n"
    "ds_read_b64_tr_b16 v[46:47], v7 offset:10752\n"
    "v_smfmac_f32_16x16x64_bf16 v[28:31], v[8:11], v[52:59], v12\n"
    "s_waitcnt lgkmcnt(7)\n"
    "v_mfma_f32_32x32x16_bf16 v[96:111], v[64:67], v[72:75], v[96:111]\n"
    "ds_read_b128 v[60:63], v5 offset:32768\n"
    "ds_read_b64_tr_b16 v[52:53], v7 offset:12288\n"
    "ds_read_b64_tr_b16 v[54:55], v7 offset:12800\n"
    "s_waitcnt lgkmcnt(8)\n"
    "v_mfma_f32_32x32x16_bf16 v[112:127], v[72:75], v[68:71], v[112:127]\n"
    "ds_read_b64_tr_b16 v[56:57], v7 offset:14336\n"
    "ds_read_b64_tr_b16 v[58:59], v7 offset:14848\n"
    "v_smfmac_f32_16x16x64_bf16 v[28:31], v[8:11], v[64:71], v12\n"
    "s_barrier\n"
    "s_waitcnt lgkmcnt(7)\n"
    "v_mfma_f32_32x32x16_bf16 v[96:111], v[40:43], v[48:51], v[96:111]\n"
    "ds_read_b128 v[72:75], v2 offset:40960\n"
    "ds_read_b64_tr_b16 v[64:65], v6\n"
    "ds_read_b64_tr_b16 v[66:67], v6 offset:512\n"
    "s_waitcnt lgkmcnt(8)\n"
    "v_mfma_f32_32x32x16_bf16 v[112:127], v[48:51], v[44:47], v[112:127]\n"
    "ds_read_b64_tr_b16 v[68:69], v6 offset:2048\n"
    "ds_read_b64_tr_b16 v[70:71], v6 offset:2560\n"
    "v_smfmac_f32_16x16x64_bf16 v[28:31], v[8:11], v[40:47], v12\n"
    "s_waitcnt lgkmcnt(7)\n"
    "v_mfma_f32_32x32x16_bf16 v[96:111], v[52:55], v[60:63], v[96:111]\n"
    "ds_read_b128 v[48:51], v3 offset:40960\n"
    "ds_read_b64_tr_b16 v[40:41], v6 offset:4096\n"
    "ds_read_b64_tr_b16 v[42:43], v6 offset:4608\n"
    "s_waitcnt lgkmcnt(8)\n"
    "v_mfma_f32_32x32x16_bf16 v[112:127], v[60:63], v[56:59], v[112:127]\n"
    "ds_read_b64_tr_b16 v[44:45], v6 offset:6144\n"
    "ds_read_b64_tr_b16 v[46:47], v6 offset:6656\n"
    "v_smfmac_f32_16x16x64_bf16 v[28:31], v[8:11], v[52:59], v12\n"
    "s_waitcnt lgkmcnt(7)\n"
    "v_mfma_f32_32x32x16_bf16 v[96:111], v[64:67], v[72:75], v[96:111]\n"
    "ds_read_b128 v[60:63], v4 offset:40960\n"
    "ds_read_b64_tr_b16 v[52:53], v6 offset:8192\n"
    "ds_read_b64_tr_b16 v[54:55], v6 offset:8704\n"
    "s_waitcnt lgkmcnt(8)\n"
    "v_mfma_f32_32x32x16_bf16 v[112:127], v[72:75], v[68:71], v[112:127]\n"
    "ds_read_b64_tr_b16 v[56:57], v6 offset:10240\n"
    "ds_read_b64_tr_b16 v[58:59], v6 offset:10752\n"
    "v_smfmac_f32_16x16x64_bf16 v[28:31], v[8:11], v[64:71], v12\n"
    "s_waitcnt lgkmcnt(7)\n"
    "v_mfma_f32_32x32x16_bf16 v[96:111], v[40:43], v[48:51], v[96:111]\n"
    "ds_read_b128 v[72:75], v5 offset:40960\n"
    "ds_read_b64_tr_b16 v[64:65], v6 offset:12288\n"
    "ds_read_b64_tr_b16 v[66:67], v6 offset:12800\n"
    "s_waitcnt lgkmcnt(8)\n"
    "v_mfma_f32_32x32x16_bf16 v[112:127], v[48:51], v[44:47], v[112:127]\n"
    "ds_read_b64_tr_b16 v[68:69], v6 offset:14336\n"
    "ds_read_b64_tr_b16 v[70:71], v6 offset:14848\n"
    "v_smfmac_f32_16x16x64_bf16 v[28:31], v[8:11], v[40:47], v12\n"
    "s_barrier\n"
    "s_waitcnt lgkmcnt(7)\n"
    "v_mfma_f32_32x32x16_bf16 v[96:111], v[52:55], v[60:63], v[96:111]\n"
    "ds_read_b128 v[48:51], v2 offset:49152\n"
    "ds_read_b64_tr_b16 v[40:41], v6 offset:16384\n"
    "ds_read_b64_tr_b16 v[42:43], v6 offset:16896\n"
    "s_waitcnt lgkmcnt(8)\n"
    "v_mfma_f32_32x32x16_bf16 v[112:127], v[60:63], v[56:59], v[112:127]\n"
    "ds_read_b64_tr_b16 v[44:45], v6 offset:18432\n"
    "ds_read_b64_tr_b16 v[46:47], v6 offset:18944\n"
    "v_smfmac_f32_16x16x64_bf16 v[28:31], v[8:11], v[52:59], v12\n"
    "s_waitcnt lgkmcnt(7)\n"
    "v_mfma_f32_32x32x16_bf16 v[96:111], v[64:67], v[72:75], v[96:111]\n"
    "ds_read_b128 v[60:63], v3 offset:49152\n"
    "ds_read_b64_tr_b16 v[52:53], v6 offset:20480\n"
    "ds_read_b64_tr_b16 v[54:55], v6 offset:20992\n"
    "s_waitcnt lgkmcnt(8)\n"
    "v_mfma_f32_32x32x16_bf16 v[112:127], v[72:75], v[68:71], v[112:127]\n"
    "ds_read_b64_tr_b16 v[56:57], v6 offset:22528\n"
    "ds_read_b64_tr_b16 v[58:59], v6 offset:23040\n"
    "v_smfmac_f32_16x16x64_bf16 v[28:31], v[8:11], v[64:71], v12\n"
    "s_waitcnt lgkmcnt(7)\n"
    "v_mfma_f32_32x32x16_bf16 v[96:111], v[40:43], v[48:51], v[96:111]\n"
    "ds_read_b128 v[72:75], v4 offset:49152\n"
    "ds_read_b64_tr_b16 v[64:65], v6 offset:24576\n"
    "ds_read_b64_tr_b16 v[66:67], v6 offset:25088\n"
    "s_waitcnt lgkmcnt(8)\n"
    "v_mfma_f32_32x32x16_bf16 v[112:127], v[48:51], v[44:47], v[112:127]\n"
    "ds_read_b64_tr_b16 v[68:69], v6 offset:26624\n"
    "ds_read_b64_tr_b16 v[70:71], v6 offset:27136\n"
    "v_smfmac_f32_16x16x64_bf16 v[28:31], v[8:11], v[40:47], v12\n"
    "s_waitcnt lgkmcnt(7)\n"
    "v_mfma_f32_32x32x16_bf16 v[96:111], v[52:55], v[60:63], v[96:111]\n"
    "ds_read_b128 v[48:51], v5 offset:49152\n"
    "ds_read_b64_tr_b16 v[40:41], v6 offset:28672\n"
    "ds_read_b64_tr_b16 v[42:43], v6 offset:29184\n"
    "s_waitcnt lgkmcnt(8)\n"
    "v_mfma_f32_32x32x16_bf16 v[112:127], v[60:63], v[56:59], v[112:127]\n"
    "ds_read_b64_tr_b16 v[44:45], v6 offset:30720\n"
    "ds_read_b64_tr_b16 v[46:47], v6 offset:31232\n"
    "v_smfmac_f32_16x16x64_bf16 v[28:31], v[8:11], v[52:59], v12\n"
    "s_barrier\n"
    "s_waitcnt lgkmcnt(7)\n"
    "v_mfma_f32_32x32x16_bf16 v[96:111], v[64:67], v[72:75], v[96:111]\n"
    "ds_read_b128 v[60:63], v2 offset:57344\n"
    "ds_read_b64_tr_b16 v[52:53], v6 offset:32768\n"
    "ds_read_b64_tr_b16 v[54:55], v6 offset:33280\n"
    "s_waitcnt lgkmcnt(8)\n"
    "v_mfma_f32_32x32x16_bf16 v[112:127], v[72:75], v[68:71], v[112:127]\n"
    "ds_read_b64_tr_b16 v[56:57], v6 offset:34816\n"
    "ds_read_b64_tr_b16 v[58:59], v6 offset:35328\n"
    "ds_read_b128 v[160:163], v34 offset:256\n"
    "ds_read_b128 v[164:167], v34 offset:288\n"
    "v_smfmac_f32_16x16x64_bf16 v[28:31], v[8:11], v[64:71], v12\n"
    "s_waitcnt lgkmcnt(9)\n"
    "v_mfma_f32_32x32x16_bf16 v[96:111], v[40:43], v[48:51], v[96:111]\n"
    "ds_read_b128 v[72:75], v3 offset:57344\n"
    "ds_read_b64_tr_b16 v[64:65], v6 offset:36864\n"
    "ds_read_b64_tr_b16 v[66:67], v6 offset:37376\n"
    "s_waitcnt lgkmcnt(10)\n"
    "v_mfma_f32_32x32x16_bf16 v[112:127], v[48:51], v[44:47], v[112:127]\n"
    "ds_read_b64_tr_b16 v[68:69], v6 offset:38912\n"
    "ds_read_b64_tr_b16 v[70:71], v6 offset:39424\n"
    "ds_read_b128 v[168:171], v34 offset:320\n"
    "ds_read_b128 v[172:175], v34 offset:352\n"
    "v_smfmac_f32_16x16x64_bf16 v[28:31], v[8:11], v[40:47], v12\n"
    "s_barrier\n"
    "s_waitcnt lgkmcnt(11)\n"
    "v_mfma_f32_32x32x16_bf16 v[96:111], v[52:55], v[60:63], v[96:111]\n"
    "ds_read_b128 v[48:51], v4 offset:57344\n"
    "ds_read_b64_tr_b16 v[40:41], v6 offset:40960\n"
    "ds_read_b64_tr_b16 v[42:43], v6 offset:41472\n"
    "s_waitcnt lgkmcnt(12)\n"
    "v_mfma_f32_32x32x16_bf16 v[112:127], v[60:63], v[56:59], v[112:127]\n"
    "ds_read_b64_tr_b16 v[44:45], v6 offset:43008\n"
    "ds_read_b64_tr_b16 v[46:47], v6 offset:43520\n"
    "ds_read_b32 v32, v13\n"
    "ds_read_b32 v33, v13 offset:256\n"
    "v_smfmac_f32_16x16x64_bf16 v[28:31], v[8:11], v[52:59], v12\n"
    "s_waitcnt lgkmcnt(11)\n"
    "v_mfma_f32_32x32x16_bf16 v[96:111], v[64:67], v[72:75], v[96:111]\n"
    "ds_read_b128 v[60:63], v5 offset:57344\n"
    "ds_read_b64_tr_b16 v[52:53], v6 offset:45056\n"
    "ds_read_b64_tr_b16 v[54:55], v6 offset:45568\n"
    "s_waitcnt lgkmcnt(12)\n"
    "v_mfma_f32_32x32x16_bf16 v[112:127], v[72:75], v[68:71], v[112:127]\n"
    "ds_read_b64_tr_b16 v[56:57], v6 offset:47104\n"
    "ds_read_b64_tr_b16 v[58:59], v6 offset:47616\n"
    "v_smfmac_f32_16x16x64_bf16 v[28:31], v[8:11], v[64:71], v12\n"
    "s_waitcnt lgkmcnt(9)\n"
    "v_mfma_f32_32x32x16_bf16 v[96:111], v[40:43], v[48:51], v[96:111]\n"
    "s_waitcnt lgkmcnt(7)\n"
    "v_mfma_f32_32x32x16_bf16 v[112:127], v[48:51], v[44:47], v[112:127]\n"
    "v_smfmac_f32_16x16x64_bf16 v[28:31], v[8:11], v[40:47], v12\n"
    "s_waitcnt lgkmcnt(2)\n"
    "v_mfma_f32_32x32x16_bf16 v[96:111], v[52:55], v[60:63], v[96:111]\n"
    "s_waitcnt lgkmcnt(0)\n"
    "v_mfma_f32_32x32x16_bf16 v[112:127], v[60:63], v[56:59], v[112:127]\n"
    "v_smfmac_f32_16x16x64_bf16 v[28:31], v[8:11], v[52:59], v12\n"
    "v_xor_b32 v32, 0x80000000, v32\n"
    "s_nop 6\n"
    "v_add_f32 v28, v28, v29\n"
    "v_add_f32 v30, v30, v31\n"
    "s_nop 0\n"
    "v_mfma_f32_32x32x2_f32 v[96:111], v28, v32, v[96:111]\n"
    "v_mfma_f32_32x32x2_f32 v[112:127], v32, v30, v[112:127]\n"
    "s_nop 15\n"
    "s_nop 0\n"
    "v_mul_f32 v96, v33, v96\n"
    "v_mul_f32 v97, v33, v97\n"
    "v_mul_f32 v98, v33, v98\n"
    "v_mul_f32 v99, v33, v99\n"
    "v_mul_f32 v100, v33, v100\n"
    "v_mul_f32 v101, v33, v101\n"
    "v_mul_f32 v102, v33, v102\n"
    "v_mul_f32 v103, v33, v103\n"
    "v_cvt_pk_bf16_f32 v40, v96, v97\n"
    "v_cvt_pk_bf16_f32 v41, v98, v99\n"
    "v_cvt_pk_bf16_f32 v42, v100, v101\n"
    "v_cvt_pk_bf16_f32 v43, v102, v103\n"
    "buffer_store_dwordx4 v[40:43], v35, s[20:23], s49 offen sc1\n"
    "v_mul_f32 v104, v33, v104\n"
    "v_mul_f32 v105, v33, v105\n"
    "v_mul_f32 v106, v33, v106\n"
    "v_mul_f32 v107, v33, v107\n"
    "v_mul_f32 v108, v33, v108\n"
    "v_mul_f32 v109, v33, v109\n"
    "v_mul_f32 v110, v33, v110\n"
    "v_mul_f32 v111, v33, v111\n"
    "v_cvt_pk_bf16_f32 v44, v104, v105\n"
    "v_cvt_pk_bf16_f32 v45, v106, v107\n"
    "v_cvt_pk_bf16_f32 v46, v108, v109\n"
    "v_cvt_pk_bf16_f32 v47, v110, v111\n"
    "buffer_store_dwordx4 v[44:47], v35, s[20:23], s50 offen sc1\n"
    "v_mul_f32 v112, v160, v112\n"
    "v_mul_f32 v113, v161, v113\n"
    "v_mul_f32 v114, v162, v114\n"
    "v_mul_f32 v115, v163, v115\n"
    "v_mul_f32 v116, v164, v116\n"
    "v_mul_f32 v117, v165, v117\n"
    "v_mul_f32 v118, v166, v118\n"
    "v_mul_f32 v119, v167, v119\n"
    "v_cvt_pk_bf16_f32 v48, v112, v113\n"
    "v_cvt_pk_bf16_f32 v49, v114, v115\n"
    "v_cvt_pk_bf16_f32 v50, v116, v117\n"
    "v_cvt_pk_bf16_f32 v51, v118, v119\n"
    "buffer_store_dwordx4 v[48:51], v36, s[20:23], s49 offen offset:1024 sc1\n"
    "v_mul_f32 v120, v168, v120\n"
    "v_mul_f32 v121, v169, v121\n"
    "v_mul_f32 v122, v170, v122\n"
    "v_mul_f32 v123, v171, v123\n"
    "v_mul_f32 v124, v172, v124\n"
    "v_mul_f32 v125, v173, v125\n"
    "v_mul_f32 v126, v174, v126\n"
    "v_mul_f32 v127, v175, v127\n"
    "v_cvt_pk_bf16_f32 v52, v120, v121\n"
    "v_cvt_pk_bf16_f32 v53, v122, v123\n"
    "v_cvt_pk_bf16_f32 v54, v124, v125\n"
    "v_cvt_pk_bf16_f32 v55, v126, v127\n"
    "buffer_store_dwordx4 v[52:55], v36, s[20:23], s49 offen offset:1536 sc1\n"
    "s_barrier\n"
    "s_bitcmp0_b32 s3, 0\n"
    "s_cbranch_scc0 .Lk_nok\n"
    "ds_write_b128 v38, v[40:43] offset:4096\n"
    "ds_write_b128 v38, v[44:47] offset:5120\n"
    ".Lk_nok:\n"
    "s_lshl_b32 s53, s34, 5\n"
    "s_add_u32 s53, s53, s35\n"
    "s_andn2_b32 s53, s53, 3\n"
    "s_lshl_b32 s53, s53, 2\n"
    "s_add_u32 s53, s53, s39\n"
    "s_lshl_b32 s53, s53, 6\n"
    "s_add_u32 s53, s53, 65536\n"
    "v_lshlrev_b32 v77, 3, v0\n"
    "s_cmp_eq_u32 s74, 4\n"
    "s_cselect_b64 vcc, -1, 0\n"
    "v_mov_b32 v76, 0x7f000000\n"
    "v_cndmask_b32 v27, v76, v1, vcc\n"
    "s_cmp_eq_u32 s48, 1\n"
    "s_cselect_b64 vcc, -1, 0\n"
    "s_cselect_b32 s30, s30, 0\n"
    "v_cndmask_b32 v28, v76, v1, vcc\n"
    "s_waitcnt vmcnt(0)\n"
    "s_mov_b64 exec, 1\n"
    "v_mov_b32 v78, 512\n"
    "v_mov_b32 v79, 1\n"
    "ds_add_rtn_u32 v79, v78, v79\n"
    "s_waitcnt lgkmcnt(0)\n"
    "v_readfirstlane_b32 s55, v79\n"
    "s_mov_b64 exec, -1\n"
    "s_cmp_eq_u32 s55, 3\n"
    "s_cbranch_scc0 .Lk_a_nosig\n"
    "s_mov_b64 exec, 0xff\n"
    "v_mov_b32 v78, s36\n"
    "v_mov_b32 v79, s37\n"
    "buffer_atomic_swap_x2 v[78:79], v37, s[20:23], s51 offen sc1\n"
    "s_mov_b64 exec, -1\n"
    ".Lk_a_nosig:\n"
    "buffer_load_dwordx4 v[192:195], v20, s[24:27], s70 offen nt\n"
    "s_add_u32 s70, s70, s71\n"
    "buffer_load_dwordx4 v[196:199], v20, s[24:27], s70 offen nt\n"
    "s_add_u32 s70, s70, s71\n"
    "buffer_load_dwordx4 v[200:203], v20, s[24:27], s70 offen nt\n"
    "s_add_u32 s70, s70, s71\n"
    "buffer_load_dwordx4 v[204:207], v20, s[24:27], s70 offen nt\n"
    "s_add_u32 s70, s70, s71\n"
    "s_cmp_eq_u32 s48, 1\n"
    "s_cbranch_scc0 .Lk_ja\n"
    "buffer_load_dwordx4 v[208:211], v21, s[28:31], s75 offen nt\n"
    "s_add_u32 s75, s75, 0x2000\n"
    "buffer_load_dwordx4 v[212:215], v21, s[28:31], s75 offen nt\n"
    "s_add_u32 s75, s75, 0x2000\n"
    "buffer_load_dwordx4 v[216:219], v21, s[28:31], s75 offen nt\n"
    "s_add_u32 s75, s75, 0x2000\n"
    "buffer_load_dwordx4 v[220:223], v21, s[28:31], s75 offen nt\n"
    "s_add_u32 s75, s75, 0x2000\n"
    ".Lk_ja:\n"
    "s_mov_b32 s52, s53\n"
    "s_mul_i32 s57, s3, 6336\n"
    "s_add_u32 s57, s57, 98304\n"
    "s_mul_i32 s59, s39, 24576\n"
    "s_lshr_b32 s60, s33, 2\n"
    "s_and_b32 s61, s33, 3\n"
    "s_lshl_b32 s58, s34, 2\n"
    "s_add_u32 s58, s58, s39\n"
    "s_lshl_b32 s58, s58, 1\n"
    "s_add_u32 s58, s58, s40\n"
    "s_lshl_b32 s58, s58, 16\n"
    "s_add_u32 s58, s58, 2097152\n"
    "s_and_b32 s62, s56, 6\n"
    "s_and_b32 s53, s33, 1\n"
    "s_add_u32 s62, s62, s53\n"
    "s_lshr_b32 s53, s33, 1\n"
    "s_and_b32 s63, s56, 1\n"
    "s_mul_i32 s63, s63, 15\n"
    "s_add_u32 s63, s63, s53\n"
    "s_cmp_lt_u32 s53, 15\n"
    "s_cselect_b32 s63, s63, 31\n"
    "s_cmp_lt_u32 s63, 29\n"
    "s_cselect_b32 s65, 1, 0\n"
    "s_mul_i32 s64, s63, 3\n"
    "s_min_u32 s64, s64, 83\n"
    "s_lshl_b32 s67, s61, 3\n"
    "s_add_u32 s67, s67, s3\n"
    "s_lshl_b32 s66, s67, 5\n"
    "s_lshl_b32 s67, s67, 14\n"
    "s_add_u32 s67, s67, 16121856\n"
    "s_lshl_b32 s68, s64, 15\n"
    "s_add_u32 s68, s68, s59\n"
    "s_add_u32 s68, s68, 16646144\n"
    "s_lshl_b32 s53, s56, 4\n"
    "s_add_u32 s53, s53, s60\n"
    "s_lshl_b32 s53, s53, 14\n"
    "s_add_u32 s66, s66, s53\n"
    "s_lshl_b32 s53, s39, 1\n"
    "s_add_u32 s53, s53, s40\n"
    "s_lshl_b32 s53, s53, 10\n"
    "v_lshl_add_u32 v176, v0, 4, s53\n"
    "v_lshrrev_b32 v60, 1, v0\n"
    "v_mul_u32_u24 v60, 1056, v60\n"
    "v_and_b32 v61, 1, v0\n"
    "v_lshl_add_u32 v60, v61, 4, v60\n"
    "v_add_u32 v177, s57, v60\n"
    "v_add_u32 v61, -1, v61\n"
    "v_and_b32 v178, 0x3f803f80, v61\n"
    "v_and_b32 v60, 15, v0\n"
    "v_lshrrev_b32 v61, 4, v0\n"
    "v_lshlrev_b32 v61, 3, v61\n"
    "v_lshl_or_b32 v86, v60, 10, v61\n"
    "v_lshlrev_b32 v60, 4, v60\n"
    "v_lshrrev_b32 v61, 5, v0\n"
    "v_lshl_or_b32 v60, v61, 8, v60\n"
    "v_add_u32 v61, -1, v61\n"
    "v_and_b32 v87, -1.0, v61\n"
    "v_add_u32 v61, 0x200, v60\n"
    "v_and_b32 v62, 11, v0\n"
    "v_min_u32 v62, 1, v62\n"
    "v_lshlrev_b32 v62, 4, v62\n"
    "v_add_u32 v62, 0x400, v62\n"
    "v_and_b32 v63, 16, v0\n"
    "v_cmp_eq_u32 vcc, 0, v63\n"
    "v_mov_b32 v128, 0\n"
    "v_mov_b32 v129, 0\n"
    "v_mov_b32 v130, 0\n"
    "v_mov_b32 v131, 0\n"
    "v_mov_b32 v132, 0\n"
    "v_mov_b32 v133, 0\n"
    "v_mov_b32 v134, 0\n"
    "v_mov_b32 v135, 0\n"
    "v_mov_b32 v136, 0\n"
    "v_mov_b32 v137, 0\n"
    "v_mov_b32 v138, 0\n"
    "v_mov_b32 v139, 0\n"
    "v_mov_b32 v140, 0\n"
    "v_mov_b32 v141, 0\n"
    "v_mov_b32 v142, 0\n"
    "v_mov_b32 v143, 0\n"
    "v_mov_b32 v144, 0\n"
    "v_mov_b32 v145, 0\n"
    "v_mov_b32 v146, 0\n"
    "v_mov_b32 v147, 0\n"
    "v_mov_b32 v148, 0\n"
    "v_mov_b32 v149, 0\n"
    "v_mov_b32 v150, 0\n"
    "v_mov_b32 v151, 0\n"
    "v_mov_b32 v152, 0\n"
    "v_mov_b32 v153, 0\n"
    "v_mov_b32 v154, 0\n"
    "v_mov_b32 v155, 0\n"
    "v_mov_b32 v156, 0\n"
    "v_mov_b32 v157, 0\n"
    "v_mov_b32 v158, 0\n"
    "v_mov_b32 v159, 0\n"
    "v_cndmask_b32 v60, v62, v60, vcc\n"
    "v_cndmask_b32 v61, v62, v61, vcc\n"
    "v_add_u32 v84, s57, v60\n"
    "v_add_u32 v85, s57, v61\n"
    "s_mov_b32 s53, s52\n"
    "s_mov_b64 exec, 0xff\n"
    "v_mov_b32 v78, 0\n"
    "v_mov_b32 v79, 0\n"
    "buffer_atomic_or_x2 v[78:79], v77, s[20:23], s53 offen sc0 sc1\n"
    "s_mov_b64 exec, -1\n"
    ".Lk_a_done:\n"
    "s_mov_b32 s46, 0\n"
    "s_mov_b32 s43, s58\n"
    "v_mov_b32 v29, v1\n"
    "v_mov_b32 v4, 0xdddd\n"
    "s_waitcnt lgkmcnt(0)\n"
    "s_barrier\n"
    "s_setprio 1\n"
    "s_lshl_b32 s54, s39, 10\n"
    "v_xor_b32 v13, 0x400, v176\n"
    "v_add_u32 v14, s54, v1\n"
    "ds_read_b128 v[48:51], v14 offset:4096\n"
    "ds_read_b128 v[88:91], v176 offset:8192\n"
    "ds_read_b128 v[92:95], v13 offset:8192\n"
    "v_mov_b32 v16, v178\n"
    "v_mov_b32 v17, v178\n"
    "v_mov_b32 v18, v178\n"
    "v_mov_b32 v19, v178\n"
    "s_mov_b64 exec, 0xfff\n"
    "ds_write_b128 v177, v[16:19] offset:1024\n"
    "s_mov_b64 exec, -1\n"
    "s_waitcnt lgkmcnt(2)\n"
    "v_mfma_f32_32x32x16_bf16 v[96:111], v[48:51], v[88:91], 0\n"
    "s_waitcnt lgkmcnt(1)\n"
    "v_mfma_f32_32x32x16_bf16 v[112:127], v[48:51], v[92:95], 0\n"
    "s_lshl_b32 s47, s39, 13\n"
    "s_add_u32 s47, s47, 32768\n"
    "v_lshlrev_b32 v42, 7, v0\n"
    "v_mov_b32 v43, 160256\n"
    "v_mov_b32 v44, 0\n"
    "v_mov_b32 v45, 0\n"
    "v_mov_b32 v46, 0\n"
    "v_mov_b32 v47, 0\n"
    "ds_write_b128 v43, v[44:47]\n"
    "ds_write_b128 v43, v[44:47] offset:16\n"
    "ds_write_b128 v43, v[44:47] offset:32\n"
    "v_max3_f32 v5, v96, v97, v98\n"
    "v_max3_f32 v5, v5, v99, v100\n"
    "v_max3_f32 v5, v5, v101, v102\n"
    "v_max3_f32 v5, v5, v103, v104\n"
    "v_max3_f32 v5, v5, v105, v106\n"
    "v_max3_f32 v5, v5, v107, v108\n"
    "v_max3_f32 v5, v5, v109, v110\n"
    "v_max_f32 v5, v5, v111\n"
    "v_max3_f32 v6, v112, v113, v114\n"
    "v_max3_f32 v6, v6, v115, v116\n"
    "v_max3_f32 v6, v6, v117, v118\n"
    "v_max3_f32 v6, v6, v119, v120\n"
    "v_max3_f32 v6, v6, v121, v122\n"
    "v_max3_f32 v6, v6, v123, v124\n"
    "v_max3_f32 v6, v6, v125, v126\n"
    "v_max_f32 v6, v6, v127\n"
    "v_mov_b32 v12, v5\n"
    "v_mov_b32 v13, v6\n"
    "s_nop 0\n"
    "v_permlane32_swap_b32 v5, v12\n"
    "v_permlane32_swap_b32 v6, v13\n"
    "v_max_f32 v5, v5, v12\n"
    "v_max_f32 v6, v6, v13\n"
    "s_cmp_lt_u32 s3, 4\n"
    "s_cbranch_scc1 .Lk_b_np\n"
    "s_mov_b64 exec, 0xff\n"
    "s_mov_b32 s55, 0x8000\n"
    "s_waitcnt vmcnt(0)\n"
    "v_cmp_ne_u64 vcc, s[36:37], v[78:79]\n"
    "s_cbranch_vccz .Lk_pd2\n"
    ".Lk_pl1:\n"
    "v_mov_b32 v78, 0\n"
    "v_mov_b32 v79, 0\n"
    "buffer_atomic_or_x2 v[78:79], v77, s[20:23], s53 offen sc0 sc1\n"
    "s_sub_u32 s55, s55, 1\n"
    "s_waitcnt vmcnt(0)\n"
    "v_cmp_ne_u64 vcc, s[36:37], v[78:79]\n"
    "s_cbranch_scc1 .Lk_pd2\n"
    "s_cbranch_vccnz .Lk_pl1\n"
    ".Lk_pd2:\n"
    "s_mov_b64 exec, -1\n"
    ".Lk_b_np:\n"
    "s_waitcnt lgkmcnt(0)\n"
    "s_barrier\n"
    ".Lk_b_try:\n"
    "s_add_u32 m0, s57, 0xfffffc00\n"
    "buffer_load_dwordx4 v[56:59], v1, s[20:23], s43 offen sc1\n"
    "buffer_load_dwordx4 v1, s[20:23], s43 offen offset:1024 sc1 lds\n"
    "s_add_u32 m0, s57, 0xfffff820\n"
    "buffer_load_dwordx4 v[60:63], v1, s[20:23], s43 offen offset:2048 sc1\n"
    "buffer_load_dwordx4 v1, s[20:23], s43 offen offset:3072 sc1 lds\n"
    ".Lk_b_m:\n"
    "v_mfma_f32_32x32x2_f32 v[160:175], v87, v5, 0\n"
    "v_mfma_f32_32x32x2_f32 v[176:191], v87, v6, 0\n"
    "s_mov_b32 s44, s67\n"
    "s_add_u32 m0, s57, 0x440\n"
    "s_add_u32 s43, s43, 0x1000\n"
    "buffer_load_dwordx4 v[64:67], v1, s[20:23], s43 offen sc1\n"
    "buffer_load_dwordx4 v1, s[20:23], s43 offen offset:1024 sc1 lds\n"
    "s_add_u32 m0, s57, 0x60\n"
    "buffer_load_dwordx4 v[68:71], v1, s[20:23], s43 offen offset:2048 sc1\n"
    "buffer_load_dwordx4 v1, s[20:23], s43 offen offset:3072 sc1 lds\n"
    "s_add_u32 m0, s57, 0xc80\n"
    "s_add_u32 s43, s43, 0x1000\n"
    "buffer_load_dwordx4 v[72:75], v1, s[20:23], s43 offen sc1\n"
    "buffer_load_dwordx4 v1, s[20:23], s43 offen offset:1024 sc1 lds\n"
    "s_add_u32 m0, s57, 0x8a0\n"
    "buffer_load_dwordx4 v[76:79], v1, s[20:23], s43 offen offset:2048 sc1\n"
    "buffer_load_dwordx4 v1, s[20:23], s43 offen offset:3072 sc1 lds\n"
    "s_cmp_lt_u32 s3, 4\n"
    "s_cbranch_scc0 .Lk_b_g1\n"
    "s_waitcnt vmcnt(10)\n"
    "ds_read_b128 v[48:51], v84\n"
    "v_mfma_f32_32x32x16_bf16 v[96:111], v[56:59], v[88:91], v[160:175]\n"
    "s_nop 11\n"
    "v_exp_f32 v96, v96\n"
    "v_exp_f32 v97, v97\n"
    "ds_read_b128 v[52:55], v85\n"
    "v_exp_f32 v98, v98\n"
    "v_exp_f32 v99, v99\n"
    "v_exp_f32 v100, v100\n"
    "v_mfma_f32_32x32x16_bf16 v[112:127], v[56:59], v[92:95], v[176:191]\n"
    "s_waitcnt lgkmcnt(1)\n"
    "v_exp_f32 v101, v101\n"
    "v_exp_f32 v102, v102\n"
    "v_exp_f32 v103, v103\n"
    "v_exp_f32 v104, v104\n"
    "v_exp_f32 v105, v105\n"
    "v_exp_f32 v106, v106\n"
    "v_smfmac_f32_32x32x32_bf16 v[128:143], v[48:51], v[96:103], v4\n"
    "s_add_u32 s43, s43, 0x1000\n"
    "buffer_load_dwordx4 v[56:59], v1, s[20:23], s43 offen sc1\n"
    "v_exp_f32 v107, v107\n"
    "v_exp_f32 v108, v108\n"
    "v_exp_f32 v109, v109\n"
    "v_exp_f32 v110, v110\n"
    "v_exp_f32 v111, v111\n"
    "v_exp_f32 v112, v112\n"
    "v_exp_f32 v113, v113\n"
    "s_waitcnt vmcnt(9) lgkmcnt(0)\n"
    "v_smfmac_f32_32x32x32_bf16 v[128:143], v[52:55], v[104:111], v4\n"
    "s_add_u32 m0, s57, 0xfffffc00\n"
    "v_exp_f32 v114, v114\n"
    "v_exp_f32 v115, v115\n"
    "v_exp_f32 v116, v116\n"
    "v_mfma_f32_32x32x16_bf16 v[96:111], v[60:63], v[88:91], v[160:175]\n"
    "buffer_load_dwordx4 v1, s[20:23], s43 offen offset:1024 sc1 lds\n"
    "v_exp_f32 v117, v117\n"
    "v_exp_f32 v118, v118\n"
    "v_exp_f32 v119, v119\n"
    "v_exp_f32 v120, v120\n"
    "v_exp_f32 v121, v121\n"
    "v_exp_f32 v122, v122\n"
    "v_smfmac_f32_32x32x32_bf16 v[144:159], v[48:51], v[112:119], v4\n"
    "ds_read_b128 v[48:51], v84 offset:1056\n"
    "v_exp_f32 v123, v123\n"
    "v_exp_f32 v124, v124\n"
    "v_exp_f32 v125, v125\n"
    "v_exp_f32 v126, v126\n"
    "v_exp_f32 v127, v127\n"
    "v_exp_f32 v96, v96\n"
    "v_exp_f32 v97, v97\n"
    "v_smfmac_f32_32x32x32_bf16 v[144:159], v[52:55], v[120:127], v4\n"
    "ds_read_b128 v[52:55], v85 offset:1056\n"
    "v_exp_f32 v98, v98\n"
    "v_exp_f32 v99, v99\n"
    "v_exp_f32 v100, v100\n"
    "v_mfma_f32_32x32x16_bf16 v[112:127], v[60:63], v[92:95], v[176:191]\n"
    "s_mov_b32 s24, s4\n"
    "s_and_b32 s25, s5, 0xffff\n"
    "s_mov_b32 s26, 0x200000\n"
    "s_mov_b32 s27, 0x20000\n"
    "buffer_load_dwordx2 v[8:9], v86, s[24:27], s66 offen\n"
    "s_waitcnt lgkmcnt(1)\n"
    "v_exp_f32 v101, v101\n"
    "v_exp_f32 v102, v102\n"
    "v_exp_f32 v103, v103\n"
    "v_exp_f32 v104, v104\n"
    "v_exp_f32 v105, v105\n"
    "v_exp_f32 v106, v106\n"
    "v_smfmac_f32_32x32x32_bf16 v[128:143], v[48:51], v[96:103], v4\n"
    "buffer_load_dwordx4 v[60:63], v1, s[20:23], s43 offen offset:2048 sc1\n"
    "v_exp_f32 v107, v107\n"
    "v_exp_f32 v108, v108\n"
    "v_exp_f32 v109, v109\n"
    "v_exp_f32 v110, v110\n"
    "v_exp_f32 v111, v111\n"
    "v_exp_f32 v112, v112\n"
    "v_exp_f32 v113, v113\n"
    "s_waitcnt vmcnt(10) lgkmcnt(0)\n"
    "v_smfmac_f32_32x32x32_bf16 v[128:143], v[52:55], v[104:111], v4\n"
    "s_add_u32 m0, s57, 0xfffff820\n"
    "v_exp_f32 v114, v114\n"
    "v_exp_f32 v115, v115\n"
    "v_exp_f32 v116, v116\n"
    "v_mfma_f32_32x32x16_bf16 v[96:111], v[64:67], v[88:91], v[160:175]\n"
    "buffer_load_dwordx4 v1, s[20:23], s43 offen offset:3072 sc1 lds\n"
    "v_exp_f32 v117, v117\n"
    "v_exp_f32 v118, v118\n"
    "v_exp_f32 v119, v119\n"
    "v_exp_f32 v120, v120\n"
    "v_exp_f32 v121, v121\n"
    "v_exp_f32 v122, v122\n"
    "v_smfmac_f32_32x32x32_bf16 v[144:159], v[48:51], v[112:119], v4\n"
    "ds_read_b128 v[48:51], v84 offset:2112\n"
    "v_exp_f32 v123, v123\n"
    "v_exp_f32 v124, v124\n"
    "v_exp_f32 v125, v125\n"
    "v_exp_f32 v126, v126\n"
    "v_exp_f32 v127, v127\n"
    "v_exp_f32 v96, v96\n"
    "v_exp_f32 v97, v97\n"
    "v_smfmac_f32_32x32x32_bf16 v[144:159], v[52:55], v[120:127], v4\n"
    "ds_read_b128 v[52:55], v85 offset:2112\n"
    "v_exp_f32 v98, v98\n"
    "v_exp_f32 v99, v99\n"
    "v_exp_f32 v100, v100\n"
    "v_mfma_f32_32x32x16_bf16 v[112:127], v[64:67], v[92:95], v[176:191]\n"
    "s_add_u32 s54, s66, 131072\n"
    "buffer_load_dwordx2 v[10:11], v86, s[24:27], s54 offen\n"
    "s_waitcnt lgkmcnt(1)\n"
    "v_exp_f32 v101, v101\n"
    "v_exp_f32 v102, v102\n"
    "v_exp_f32 v103, v103\n"
    "v_exp_f32 v104, v104\n"
    "v_exp_f32 v105, v105\n"
    "v_exp_f32 v106, v106\n"
    "v_smfmac_f32_32x32x32_bf16 v[128:143], v[48:51], v[96:103], v4\n"
    "s_add_u32 s43, s43, 0x1000\n"
    "buffer_load_dwordx4 v[64:67], v1, s[20:23], s43 offen sc1\n"
    "v_exp_f32 v107, v107\n"
    "v_exp_f32 v108, v108\n"
    "v_exp_f32 v109, v109\n"
    "v_exp_f32 v110, v110\n"
    "v_exp_f32 v111, v111\n"
    "v_exp_f32 v112, v112\n"
    "v_exp_f32 v113, v113\n"
    "s_waitcnt vmcnt(11) lgkmcnt(0)\n"
    "v_smfmac_f32_32x32x32_bf16 v[128:143], v[52:55], v[104:111], v4\n"
    "s_add_u32 m0, s57, 0x440\n"
    "v_exp_f32 v114, v114\n"
    "v_exp_f32 v115, v115\n"
    "v_exp_f32 v116, v116\n"
    "v_mfma_f32_32x32x16_bf16 v[96:111], v[68:71], v[88:91], v[160:175]\n"
    "buffer_load_dwordx4 v1, s[20:23], s43 offen offset:1024 sc1 lds\n"
    "v_exp_f32 v117, v117\n"
    "v_exp_f32 v118, v118\n"
    "v_exp_f32 v119, v119\n"
    "v_exp_f32 v120, v120\n"
    "v_exp_f32 v121, v121\n"
    "v_exp_f32 v122, v122\n"
    "v_smfmac_f32_32x32x32_bf16 v[144:159], v[48:51], v[112:119], v4\n"
    "ds_read_b128 v[48:51], v84 offset:3168\n"
    "v_exp_f32 v123, v123\n"
    "v_exp_f32 v124, v124\n"
    "v_exp_f32 v125, v125\n"
    "v_exp_f32 v126, v126\n"
    "v_exp_f32 v127, v127\n"
    "v_exp_f32 v96, v96\n"
    "v_exp_f32 v97, v97\n"
    "v_smfmac_f32_32x32x32_bf16 v[144:159], v[52:55], v[120:127], v4\n"
    "ds_read_b128 v[52:55], v85 offset:3168\n"
    "v_exp_f32 v98, v98\n"
    "v_exp_f32 v99, v99\n"
    "v_exp_f32 v100, v100\n"
    "v_mfma_f32_32x32x16_bf16 v[112:127], v[68:71], v[92:95], v[176:191]\n"
    "s_waitcnt lgkmcnt(1)\n"
    "v_exp_f32 v101, v101\n"
    "v_exp_f32 v102, v102\n"
    "v_exp_f32 v103, v103\n"
    "v_exp_f32 v104, v104\n"
    "v_exp_f32 v105, v105\n"
    "v_exp_f32 v106, v106\n"
    "v_smfmac_f32_32x32x32_bf16 v[128:143], v[48:51], v[96:103], v4\n"
    "buffer_load_dwordx4 v[68:71], v1, s[20:23], s43 offen offset:2048 sc1\n"
    "v_exp_f32 v107, v107\n"
    "v_exp_f32 v108, v108\n"
    "v_exp_f32 v109, v109\n"
    "v_exp_f32 v110, v110\n"
    "v_exp_f32 v111, v111\n"
    "v_exp_f32 v112, v112\n"
    "v_exp_f32 v113, v113\n"
    "s_waitcnt vmcnt(11) lgkmcnt(0)\n"
    "v_smfmac_f32_32x32x32_bf16 v[128:143], v[52:55], v[104:111], v4\n"
    "s_add_u32 m0, s57, 0x60\n"
    "v_exp_f32 v114, v114\n"
    "v_exp_f32 v115, v115\n"
    "v_exp_f32 v116, v116\n"
    "v_mfma_f32_32x32x16_bf16 v[96:111], v[72:75], v[88:91], v[160:175]\n"
    "buffer_load_dwordx4 v1, s[20:23], s43 offen offset:3072 sc1 lds\n"
    "v_exp_f32 v117, v117\n"
    "v_exp_f32 v118, v118\n"
    "v_exp_f32 v119, v119\n"
    "v_exp_f32 v120, v120\n"
    "v_exp_f32 v121, v121\n"
    "v_exp_f32 v122, v122\n"
    "v_smfmac_f32_32x32x32_bf16 v[144:159], v[48:51], v[112:119], v4\n"
    "ds_read_b128 v[48:51], v84 offset:4224\n"
    "v_exp_f32 v123, v123\n"
    "v_exp_f32 v124, v124\n"
    "v_exp_f32 v125, v125\n"
    "v_exp_f32 v126, v126\n"
    "v_exp_f32 v127, v127\n"
    "v_mov_b32 v30, -1\n"
    "v_mov_b32 v31, -1\n"
    "v_mov_b32 v32, -1\n"
    "v_mov_b32 v33, -1\n"
    "s_lshl_b32 s52, s35, 2\n"
    "s_add_u32 s52, s52, s39\n"
    "s_lshl_b32 s52, s52, 3\n"
    "s_add_u32 s52, s52, s34\n"
    "s_lshl_b32 s52, s52, 11\n"
    "s_add_u32 s52, s52, 6291456\n"
    "buffer_store_dwordx4 v[30:33], v1, s[20:23], s52 offen sc1\n"
    "v_exp_f32 v96, v96\n"
    "v_exp_f32 v97, v97\n"
    "v_smfmac_f32_32x32x32_bf16 v[144:159], v[52:55], v[120:127], v4\n"
    "ds_read_b128 v[52:55], v85 offset:4224\n"
    "v_exp_f32 v98, v98\n"
    "v_exp_f32 v99, v99\n"
    "v_exp_f32 v100, v100\n"
    "v_mfma_f32_32x32x16_bf16 v[112:127], v[72:75], v[92:95], v[176:191]\n"
    "s_waitcnt lgkmcnt(1)\n"
    "v_exp_f32 v101, v101\n"
    "v_exp_f32 v102, v102\n"
    "v_exp_f32 v103, v103\n"
    "v_exp_f32 v104, v104\n"
    "v_exp_f32 v105, v105\n"
    "v_exp_f32 v106, v106\n"
    "v_smfmac_f32_32x32x32_bf16 v[128:143], v[48:51], v[96:103], v4\n"
    "s_add_u32 s43, s43, 0x1000\n"
    "buffer_load_dwordx4 v[72:75], v1, s[20:23], s43 offen sc1\n"
    "v_exp_f32 v107, v107\n"
    "v_exp_f32 v108, v108\n"
    "v_exp_f32 v109, v109\n"
    "v_exp_f32 v110, v110\n"
    "v_exp_f32 v111, v111\n"
    "v_exp_f32 v112, v112\n"
    "v_exp_f32 v113, v113\n"
    "s_waitcnt vmcnt(12) lgkmcnt(0)\n"
    "v_smfmac_f32_32x32x32_bf16 v[128:143], v[52:55], v[104:111], v4\n"
    "s_add_u32 m0, s57, 0xc80\n"
    "v_exp_f32 v114, v114\n"
    "v_exp_f32 v115, v115\n"
    "v_exp_f32 v116, v116\n"
    "v_mfma_f32_32x32x16_bf16 v[96:111], v[76:79], v[88:91], v[160:175]\n"
    "buffer_load_dwordx4 v1, s[20:23], s43 offen offset:1024 sc1 lds\n"
    "v_exp_f32 v117, v117\n"
    "v_exp_f32 v118, v118\n"
    "v_exp_f32 v119, v119\n"
    "v_exp_f32 v120, v120\n"
    "v_exp_f32 v121, v121\n"
    "v_exp_f32 v122, v122\n"
    "v_smfmac_f32_32x32x32_bf16 v[144:159], v[48:51], v[112:119], v4\n"
    "ds_read_b128 v[48:51], v84 offset:5280\n"
    "v_exp_f32 v123, v123\n"
    "v_exp_f32 v124, v124\n"
    "v_exp_f32 v125, v125\n"
    "v_exp_f32 v126, v126\n"
    "v_exp_f32 v127, v127\n"
    "buffer_store_dwordx4 v[30:33], v1, s[20:23], s52 offen offset:1024 sc1\n"
    "v_exp_f32 v96, v96\n"
    "v_exp_f32 v97, v97\n"
    "v_smfmac_f32_32x32x32_bf16 v[144:159], v[52:55], v[120:127], v4\n"
    "ds_read_b128 v[52:55], v85 offset:5280\n"
    "v_exp_f32 v98, v98\n"
    "v_exp_f32 v99, v99\n"
    "v_exp_f32 v100, v100\n"
    "v_mfma_f32_32x32x16_bf16 v[112:127], v[76:79], v[92:95], v[176:191]\n"
    "s_waitcnt lgkmcnt(1)\n"
    "v_exp_f32 v101, v101\n"
    "v_exp_f32 v102, v102\n"
    "v_exp_f32 v103, v103\n"
    "v_exp_f32 v104, v104\n"
    "v_exp_f32 v105, v105\n"
    "v_exp_f32 v106, v106\n"
    "v_smfmac_f32_32x32x32_bf16 v[128:143], v[48:51], v[96:103], v4\n"
    "buffer_load_dwordx4 v[76:79], v1, s[20:23], s43 offen offset:2048 sc1\n"
    "v_exp_f32 v107, v107\n"
    "v_exp_f32 v108, v108\n"
    "v_exp_f32 v109, v109\n"
    "v_exp_f32 v110, v110\n"
    "v_exp_f32 v111, v111\n"
    "v_exp_f32 v112, v112\n"
    "v_exp_f32 v113, v113\n"
    "s_waitcnt vmcnt(13) lgkmcnt(0)\n"
    "v_smfmac_f32_32x32x32_bf16 v[128:143], v[52:55], v[104:111], v4\n"
    "s_add_u32 m0, s57, 0x8a0\n"
    "v_exp_f32 v114, v114\n"
    "v_exp_f32 v115, v115\n"
    "v_exp_f32 v116, v116\n"
    "v_mfma_f32_32x32x16_bf16 v[96:111], v[56:59], v[88:91], v[160:175]\n"
    "buffer_load_dwordx4 v1, s[20:23], s43 offen offset:3072 sc1 lds\n"
    "v_exp_f32 v117, v117\n"
    "v_exp_f32 v118, v118\n"
    "v_exp_f32 v119, v119\n"
    "v_exp_f32 v120, v120\n"
    "v_exp_f32 v121, v121\n"
    "v_exp_f32 v122, v122\n"
    "v_smfmac_f32_32x32x32_bf16 v[144:159], v[48:51], v[112:119], v4\n"
    "ds_read_b128 v[48:51], v84\n"
    "v_exp_f32 v123, v123\n"
    "v_exp_f32 v124, v124\n"
    "v_exp_f32 v125, v125\n"
    "v_exp_f32 v126, v126\n"
    "v_exp_f32 v127, v127\n"
    "s_lshl_b32 s52, s56, 6\n"
    "s_add_u32 s52, s52, s33\n"
    "s_lshl_b32 s52, s52, 2\n"
    "s_add_u32 s52, s52, s39\n"
    "s_lshl_b32 s52, s52, 10\n"
    "s_add_u32 s52, s52, 8388608\n"
    "buffer_store_dwordx4 v[30:33], v1, s[20:23], s52 offen sc1\n"
    "v_exp_f32 v96, v96\n"
    "v_exp_f32 v97, v97\n"
    "v_smfmac_f32_32x32x32_bf16 v[144:159], v[52:55], v[120:127], v4\n"
    "ds_read_b128 v[52:55], v85\n"
    "v_exp_f32 v98, v98\n"
    "v_exp_f32 v99, v99\n"
    "v_exp_f32 v100, v100\n"
    "v_mfma_f32_32x32x16_bf16 v[112:127], v[56:59], v[92:95], v[176:191]\n"
    "s_waitcnt lgkmcnt(1)\n"
    "v_exp_f32 v101, v101\n"
    "v_exp_f32 v102, v102\n"
    "v_exp_f32 v103, v103\n"
    "v_exp_f32 v104, v104\n"
    "v_exp_f32 v105, v105\n"
    "v_exp_f32 v106, v106\n"
    "v_smfmac_f32_32x32x32_bf16 v[128:143], v[48:51], v[96:103], v4\n"
    "s_add_u32 s43, s43, 0x1000\n"
    "buffer_load_dwordx4 v[56:59], v1, s[20:23], s43 offen sc1\n"
    "v_exp_f32 v107, v107\n"
    "v_exp_f32 v108, v108\n"
    "v_exp_f32 v109, v109\n"
    "v_exp_f32 v110, v110\n"
    "v_exp_f32 v111, v111\n"
    "v_exp_f32 v112, v112\n"
    "v_exp_f32 v113, v113\n"
    "s_waitcnt vmcnt(13) lgkmcnt(0)\n"
    "v_smfmac_f32_32x32x32_bf16 v[128:143], v[52:55], v[104:111], v4\n"
    "s_add_u32 m0, s57, 0xfffffc00\n"
    "v_exp_f32 v114, v114\n"
    "v_exp_f32 v115, v115\n"
    "v_exp_f32 v116, v116\n"
    "v_mfma_f32_32x32x16_bf16 v[96:111], v[60:63], v[88:91], v[160:175]\n"
    "buffer_load_dwordx4 v1, s[20:23], s43 offen offset:1024 sc1 lds\n"
    "v_exp_f32 v117, v117\n"
    "v_exp_f32 v118, v118\n"
    "v_exp_f32 v119, v119\n"
    "v_exp_f32 v120, v120\n"
    "v_exp_f32 v121, v121\n"
    "v_exp_f32 v122, v122\n"
    "v_smfmac_f32_32x32x32_bf16 v[144:159], v[48:51], v[112:119], v4\n"
    "ds_read_b128 v[48:51], v84 offset:1056\n"
    "v_exp_f32 v123, v123\n"
    "v_exp_f32 v124, v124\n"
    "v_exp_f32 v125, v125\n"
    "v_exp_f32 v126, v126\n"
    "v_exp_f32 v127, v127\n"
    "s_add_u32 s52, s52, 131072\n"
    "buffer_store_dwordx4 v[30:33], v1, s[20:23], s52 offen sc1\n"
    "v_exp_f32 v96, v96\n"
    "v_exp_f32 v97, v97\n"
    "v_smfmac_f32_32x32x32_bf16 v[144:159], v[52:55], v[120:127], v4\n"
    "ds_read_b128 v[52:55], v85 offset:1056\n"
    "v_exp_f32 v98, v98\n"
    "v_exp_f32 v99, v99\n"
    "v_exp_f32 v100, v100\n"
    "v_mfma_f32_32x32x16_bf16 v[112:127], v[60:63], v[92:95], v[176:191]\n"
    "s_waitcnt lgkmcnt(1)\n"
    "v_exp_f32 v101, v101\n"
    "v_exp_f32 v102, v102\n"
    "v_exp_f32 v103, v103\n"
    "v_exp_f32 v104, v104\n"
    "v_exp_f32 v105, v105\n"
    "v_exp_f32 v106, v106\n"
    "v_smfmac_f32_32x32x32_bf16 v[128:143], v[48:51], v[96:103], v4\n"
    "buffer_load_dwordx4 v[60:63], v1, s[20:23], s43 offen offset:2048 sc1\n"
    "v_exp_f32 v107, v107\n"
    "v_exp_f32 v108, v108\n"
    "v_exp_f32 v109, v109\n"
    "v_exp_f32 v110, v110\n"
    "v_exp_f32 v111, v111\n"
    "v_exp_f32 v112, v112\n"
    "v_exp_f32 v113, v113\n"
    "s_waitcnt vmcnt(13) lgkmcnt(0)\n"
    "v_smfmac_f32_32x32x32_bf16 v[128:143], v[52:55], v[104:111], v4\n"
    "s_add_u32 m0, s57, 0xfffff820\n"
    "v_exp_f32 v114, v114\n"
    "v_exp_f32 v115, v115\n"
    "v_exp_f32 v116, v116\n"
    "v_mfma_f32_32x32x16_bf16 v[96:111], v[64:67], v[88:91], v[160:175]\n"
    "buffer_load_dwordx4 v1, s[20:23], s43 offen offset:3072 sc1 lds\n"
    "v_exp_f32 v117, v117\n"
    "v_exp_f32 v118, v118\n"
    "v_exp_f32 v119, v119\n"
    "v_exp_f32 v120, v120\n"
    "v_exp_f32 v121, v121\n"
    "v_exp_f32 v122, v122\n"
    "v_smfmac_f32_32x32x32_bf16 v[144:159], v[48:51], v[112:119], v4\n"
    "ds_read_b128 v[48:51], v84 offset:2112\n"
    "v_exp_f32 v123, v123\n"
    "v_exp_f32 v124, v124\n"
    "v_exp_f32 v125, v125\n"
    "v_exp_f32 v126, v126\n"
    "v_exp_f32 v127, v127\n"
    "v_exp_f32 v96, v96\n"
    "v_exp_f32 v97, v97\n"
    "v_smfmac_f32_32x32x32_bf16 v[144:159], v[52:55], v[120:127], v4\n"
    "ds_read_b128 v[52:55], v85 offset:2112\n"
    "v_exp_f32 v98, v98\n"
    "v_exp_f32 v99, v99\n"
    "v_exp_f32 v100, v100\n"
    "v_mfma_f32_32x32x16_bf16 v[112:127], v[64:67], v[92:95], v[176:191]\n"
    "s_waitcnt lgkmcnt(1)\n"
    "v_exp_f32 v101, v101\n"
    "v_exp_f32 v102, v102\n"
    "v_exp_f32 v103, v103\n"
    "v_exp_f32 v104, v104\n"
    "v_exp_f32 v105, v105\n"
    "v_exp_f32 v106, v106\n"
    "v_smfmac_f32_32x32x32_bf16 v[128:143], v[48:51], v[96:103], v4\n"
    "s_add_u32 s43, s43, 0x1000\n"
    "buffer_load_dwordx4 v[64:67], v1, s[20:23], s43 offen sc1\n"
    "v_exp_f32 v107, v107\n"
    "v_exp_f32 v108, v108\n"
    "v_exp_f32 v109, v109\n"
    "v_exp_f32 v110, v110\n"
    "v_exp_f32 v111, v111\n"
    "v_exp_f32 v112, v112\n"
    "v_exp_f32 v113, v113\n"
    "s_waitcnt vmcnt(13) lgkmcnt(0)\n"
    "v_smfmac_f32_32x32x32_bf16 v[128:143], v[52:55], v[104:111], v4\n"
    "s_add_u32 m0, s57, 0x440\n"
    "v_exp_f32 v114, v114\n"
    "v_exp_f32 v115, v115\n"
    "v_exp_f32 v116, v116\n"
    "v_mfma_f32_32x32x16_bf16 v[96:111], v[68:71], v[88:91], v[160:175]\n"
    "buffer_load_dwordx4 v1, s[20:23], s43 offen offset:1024 sc1 lds\n"
    "v_exp_f32 v117, v117\n"
    "v_exp_f32 v118, v118\n"
    "v_exp_f32 v119, v119\n"
    "v_exp_f32 v120, v120\n"
    "v_exp_f32 v121, v121\n"
    "v_exp_f32 v122, v122\n"
    "v_smfmac_f32_32x32x32_bf16 v[144:159], v[48:51], v[112:119], v4\n"
    "ds_read_b128 v[48:51], v84 offset:3168\n"
    "v_exp_f32 v123, v123\n"
    "v_exp_f32 v124, v124\n"
    "v_exp_f32 v125, v125\n"
    "v_exp_f32 v126, v126\n"
    "v_exp_f32 v127, v127\n"
    "v_exp_f32 v96, v96\n"
    "v_exp_f32 v97, v97\n"
    "v_smfmac_f32_32x32x32_bf16 v[144:159], v[52:55], v[120:127], v4\n"
    "ds_read_b128 v[52:55], v85 offset:3168\n"
    "v_exp_f32 v98, v98\n"
    "v_exp_f32 v99, v99\n"
    "v_exp_f32 v100, v100\n"
    "v_mfma_f32_32x32x16_bf16 v[112:127], v[68:71], v[92:95], v[176:191]\n"
    "s_waitcnt lgkmcnt(1)\n"
    "v_exp_f32 v101, v101\n"
    "v_exp_f32 v102, v102\n"
    "v_exp_f32 v103, v103\n"
    "v_exp_f32 v104, v104\n"
    "v_exp_f32 v105, v105\n"
    "v_exp_f32 v106, v106\n"
    "v_smfmac_f32_32x32x32_bf16 v[128:143], v[48:51], v[96:103], v4\n"
    "buffer_load_dwordx4 v[68:71], v1, s[20:23], s43 offen offset:2048 sc1\n"
    "v_exp_f32 v107, v107\n"
    "v_exp_f32 v108, v108\n"
    "v_exp_f32 v109, v109\n"
    "v_exp_f32 v110, v110\n"
    "v_exp_f32 v111, v111\n"
    "v_exp_f32 v112, v112\n"
    "v_exp_f32 v113, v113\n"
    "s_waitcnt vmcnt(12) lgkmcnt(0)\n"
    "v_smfmac_f32_32x32x32_bf16 v[128:143], v[52:55], v[104:111], v4\n"
    "s_add_u32 m0, s57, 0x60\n"
    "v_exp_f32 v114, v114\n"
    "v_exp_f32 v115, v115\n"
    "v_exp_f32 v116, v116\n"
    "v_mfma_f32_32x32x16_bf16 v[96:111], v[72:75], v[88:91], v[160:175]\n"
    "buffer_load_dwordx4 v1, s[20:23], s43 offen offset:3072 sc1 lds\n"
    "v_exp_f32 v117, v117\n"
    "v_exp_f32 v118, v118\n"
    "v_exp_f32 v119, v119\n"
    "v_exp_f32 v120, v120\n"
    "v_exp_f32 v121, v121\n"
    "v_exp_f32 v122, v122\n"
    "v_smfmac_f32_32x32x32_bf16 v[144:159], v[48:51], v[112:119], v4\n"
    "ds_read_b128 v[48:51], v84 offset:4224\n"
    "v_exp_f32 v123, v123\n"
    "v_exp_f32 v124, v124\n"
    "v_exp_f32 v125, v125\n"
    "v_exp_f32 v126, v126\n"
    "v_exp_f32 v127, v127\n"
    "v_exp_f32 v96, v96\n"
    "v_exp_f32 v97, v97\n"
    "v_smfmac_f32_32x32x32_bf16 v[144:159], v[52:55], v[120:127], v4\n"
    "ds_read_b128 v[52:55], v85 offset:4224\n"
    "v_exp_f32 v98, v98\n"
    "v_exp_f32 v99, v99\n"
    "v_exp_f32 v100, v100\n"
    "v_mfma_f32_32x32x16_bf16 v[112:127], v[72:75], v[92:95], v[176:191]\n"
    "s_waitcnt lgkmcnt(1)\n"
    "v_exp_f32 v101, v101\n"
    "v_exp_f32 v102, v102\n"
    "v_exp_f32 v103, v103\n"
    "v_exp_f32 v104, v104\n"
    "v_exp_f32 v105, v105\n"
    "v_exp_f32 v106, v106\n"
    "v_smfmac_f32_32x32x32_bf16 v[128:143], v[48:51], v[96:103], v4\n"
    "s_add_u32 s43, s43, 0x1000\n"
    "buffer_load_dwordx4 v[72:75], v1, s[20:23], s43 offen sc1\n"
    "v_exp_f32 v107, v107\n"
    "v_exp_f32 v108, v108\n"
    "v_exp_f32 v109, v109\n"
    "v_exp_f32 v110, v110\n"
    "v_exp_f32 v111, v111\n"
    "v_exp_f32 v112, v112\n"
    "v_exp_f32 v113, v113\n"
    "s_waitcnt vmcnt(11) lgkmcnt(0)\n"
    "v_smfmac_f32_32x32x32_bf16 v[128:143], v[52:55], v[104:111], v4\n"
    "s_add_u32 m0, s57, 0xc80\n"
    "v_exp_f32 v114, v114\n"
    "v_exp_f32 v115, v115\n"
    "v_exp_f32 v116, v116\n"
    "v_mfma_f32_32x32x16_bf16 v[96:111], v[76:79], v[88:91], v[160:175]\n"
    "buffer_load_dwordx4 v1, s[20:23], s43 offen offset:1024 sc1 lds\n"
    "v_exp_f32 v117, v117\n"
    "v_exp_f32 v118, v118\n"
    "v_exp_f32 v119, v119\n"
    "v_exp_f32 v120, v120\n"
    "v_exp_f32 v121, v121\n"
    "v_exp_f32 v122, v122\n"
    "v_smfmac_f32_32x32x32_bf16 v[144:159], v[48:51], v[112:119], v4\n"
    "ds_read_b128 v[48:51], v84 offset:5280\n"
    "v_exp_f32 v123, v123\n"
    "v_exp_f32 v124, v124\n"
    "v_exp_f32 v125, v125\n"
    "v_exp_f32 v126, v126\n"
    "v_exp_f32 v127, v127\n"
    "v_exp_f32 v96, v96\n"
    "v_exp_f32 v97, v97\n"
    "v_smfmac_f32_32x32x32_bf16 v[144:159], v[52:55], v[120:127], v4\n"
    "ds_read_b128 v[52:55], v85 offset:5280\n"
    "v_exp_f32 v98, v98\n"
    "v_exp_f32 v99, v99\n"
    "v_exp_f32 v100, v100\n"
    "v_mfma_f32_32x32x16_bf16 v[112:127], v[76:79], v[92:95], v[176:191]\n"
    "s_waitcnt lgkmcnt(1)\n"
    "v_exp_f32 v101, v101\n"
    "v_exp_f32 v102, v102\n"
    "v_exp_f32 v103, v103\n"
    "v_exp_f32 v104, v104\n"
    "v_exp_f32 v105, v105\n"
    "v_exp_f32 v106, v106\n"
    "v_smfmac_f32_32x32x32_bf16 v[128:143], v[48:51], v[96:103], v4\n"
    "buffer_load_dwordx4 v[76:79], v1, s[20:23], s43 offen offset:2048 sc1\n"
    "v_exp_f32 v107, v107\n"
    "v_exp_f32 v108, v108\n"
    "v_exp_f32 v109, v109\n"
    "v_exp_f32 v110, v110\n"
    "v_exp_f32 v111, v111\n"
    "v_exp_f32 v112, v112\n"
    "v_exp_f32 v113, v113\n"
    "s_waitcnt vmcnt(10) lgkmcnt(0)\n"
    "v_smfmac_f32_32x32x32_bf16 v[128:143], v[52:55], v[104:111], v4\n"
    "s_add_u32 m0, s57, 0x8a0\n"
    "ds_read_b32 v41, v43 offset:4\n"
    "v_exp_f32 v114, v114\n"
    "v_exp_f32 v115, v115\n"
    "v_exp_f32 v116, v116\n"
    "v_mfma_f32_32x32x16_bf16 v[96:111], v[56:59], v[88:91], v[160:175]\n"
    "buffer_load_dwordx4 v1, s[20:23], s43 offen offset:3072 sc1 lds\n"
    "v_exp_f32 v117, v117\n"
    "v_exp_f32 v118, v118\n"
    "v_exp_f32 v119, v119\n"
    "v_exp_f32 v120, v120\n"
    "v_exp_f32 v121, v121\n"
    "v_exp_f32 v122, v122\n"
    "v_smfmac_f32_32x32x32_bf16 v[144:159], v[48:51], v[112:119], v4\n"
    "ds_read_b128 v[48:51], v84\n"
    "v_exp_f32 v123, v123\n"
    "v_exp_f32 v124, v124\n"
    "v_exp_f32 v125, v125\n"
    "v_exp_f32 v126, v126\n"
    "v_exp_f32 v127, v127\n"
    "v_exp_f32 v96, v96\n"
    "v_exp_f32 v97, v97\n"
    "v_smfmac_f32_32x32x32_bf16 v[144:159], v[52:55], v[120:127], v4\n"
    "ds_read_b128 v[52:55], v85\n"
    "s_mov_b32 s55, 0x40000\n"
    ".Lk_acl0:\n"
    "s_waitcnt lgkmcnt(2)\n"
    "v_readfirstlane_b32 s50, v41\n"
    "s_cmp_ge_u32 s50, 4\n"
    "s_cbranch_scc1 .Lk_acd0\n"
    "s_sub_u32 s55, s55, 1\n"
    "s_cbranch_scc1 .Lk_acd0\n"
    "s_sleep 2\n"
    "ds_read_b32 v41, v43 offset:4\n"
    "s_waitcnt lgkmcnt(0)\n"
    "s_branch .Lk_acl0\n"
    ".Lk_acd0:\n"
    "v_exp_f32 v98, v98\n"
    "v_exp_f32 v99, v99\n"
    "v_exp_f32 v100, v100\n"
    "v_mfma_f32_32x32x16_bf16 v[112:127], v[56:59], v[92:95], v[176:191]\n"
    "s_add_u32 m0, s59, 16384\n"
    "s_add_u32 s45, s68, 16384\n"
    "buffer_load_dwordx4 v1, s[20:23], s45 offen sc1 lds\n"
    "buffer_load_dwordx4 v1, s[20:23], s45 offen offset:1024 sc1 lds\n"
    "s_waitcnt lgkmcnt(1)\n"
    "v_exp_f32 v101, v101\n"
    "v_exp_f32 v102, v102\n"
    "v_exp_f32 v103, v103\n"
    "v_exp_f32 v104, v104\n"
    "v_exp_f32 v105, v105\n"
    "v_exp_f32 v106, v106\n"
    "v_smfmac_f32_32x32x32_bf16 v[128:143], v[48:51], v[96:103], v4\n"
    "s_add_u32 s43, s43, 0x1000\n"
    "buffer_load_dwordx4 v[56:59], v1, s[20:23], s43 offen sc1\n"
    "v_exp_f32 v107, v107\n"
    "v_exp_f32 v108, v108\n"
    "v_exp_f32 v109, v109\n"
    "v_exp_f32 v110, v110\n"
    "v_exp_f32 v111, v111\n"
    "v_exp_f32 v112, v112\n"
    "v_exp_f32 v113, v113\n"
    "s_waitcnt vmcnt(11) lgkmcnt(0)\n"
    "v_smfmac_f32_32x32x32_bf16 v[128:143], v[52:55], v[104:111], v4\n"
    "s_add_u32 m0, s57, 0xfffffc00\n"
    "buffer_load_dwordx4 v[192:195], v1, s[20:23], s44 offen sc1\n"
    "v_exp_f32 v114, v114\n"
    "v_exp_f32 v115, v115\n"
    "v_exp_f32 v116, v116\n"
    "v_mfma_f32_32x32x16_bf16 v[96:111], v[60:63], v[88:91], v[160:175]\n"
    "buffer_load_dwordx4 v1, s[20:23], s43 offen offset:1024 sc1 lds\n"
    "v_exp_f32 v117, v117\n"
    "v_exp_f32 v118, v118\n"
    "v_exp_f32 v119, v119\n"
    "v_exp_f32 v120, v120\n"
    "v_exp_f32 v121, v121\n"
    "v_exp_f32 v122, v122\n"
    "v_smfmac_f32_32x32x32_bf16 v[144:159], v[48:51], v[112:119], v4\n"
    "ds_read_b128 v[48:51], v84 offset:1056\n"
    "v_exp_f32 v123, v123\n"
    "v_exp_f32 v124, v124\n"
    "v_exp_f32 v125, v125\n"
    "v_exp_f32 v126, v126\n"
    "v_exp_f32 v127, v127\n"
    "v_exp_f32 v96, v96\n"
    "v_exp_f32 v97, v97\n"
    "v_smfmac_f32_32x32x32_bf16 v[144:159], v[52:55], v[120:127], v4\n"
    "ds_read_b128 v[52:55], v85 offset:1056\n"
    "v_exp_f32 v98, v98\n"
    "v_exp_f32 v99, v99\n"
    "v_exp_f32 v100, v100\n"
    "v_mfma_f32_32x32x16_bf16 v[112:127], v[60:63], v[92:95], v[176:191]\n"
    "s_waitcnt lgkmcnt(1)\n"
    "v_exp_f32 v101, v101\n"
    "v_exp_f32 v102, v102\n"
    "v_exp_f32 v103, v103\n"
    "v_exp_f32 v104, v104\n"
    "v_exp_f32 v105, v105\n"
    "v_exp_f32 v106, v106\n"
    "v_smfmac_f32_32x32x32_bf16 v[128:143], v[48:51], v[96:103], v4\n"
    "buffer_load_dwordx4 v[60:63], v1, s[20:23], s43 offen offset:2048 sc1\n"
    "v_exp_f32 v107, v107\n"
    "v_exp_f32 v108, v108\n"
    "v_exp_f32 v109, v109\n"
    "v_exp_f32 v110, v110\n"
    "v_exp_f32 v111, v111\n"
    "v_exp_f32 v112, v112\n"
    "v_exp_f32 v113, v113\n"
    "s_waitcnt vmcnt(12) lgkmcnt(0)\n"
    "v_smfmac_f32_32x32x32_bf16 v[128:143], v[52:55], v[104:111], v4\n"
    "s_add_u32 m0, s57, 0xfffff820\n"
    "buffer_load_dwordx4 v[196:199], v1, s[20:23], s44 offen offset:1024 sc1\n"
    "v_exp_f32 v114, v114\n"
    "v_exp_f32 v115, v115\n"
    "v_exp_f32 v116, v116\n"
    "v_mfma_f32_32x32x16_bf16 v[96:111], v[64:67], v[88:91], v[160:175]\n"
    "buffer_load_dwordx4 v1, s[20:23], s43 offen offset:3072 sc1 lds\n"
    "v_exp_f32 v117, v117\n"
    "v_exp_f32 v118, v118\n"
    "v_exp_f32 v119, v119\n"
    "v_exp_f32 v120, v120\n"
    "v_exp_f32 v121, v121\n"
    "v_exp_f32 v122, v122\n"
    "v_smfmac_f32_32x32x32_bf16 v[144:159], v[48:51], v[112:119], v4\n"
    "ds_read_b128 v[48:51], v84 offset:2112\n"
    "v_exp_f32 v123, v123\n"
    "v_exp_f32 v124, v124\n"
    "v_exp_f32 v125, v125\n"
    "v_exp_f32 v126, v126\n"
    "v_exp_f32 v127, v127\n"
    "v_exp_f32 v96, v96\n"
    "v_exp_f32 v97, v97\n"
    "v_smfmac_f32_32x32x32_bf16 v[144:159], v[52:55], v[120:127], v4\n"
    "ds_read_b128 v[52:55], v85 offset:2112\n"
    "v_exp_f32 v98, v98\n"
    "v_exp_f32 v99, v99\n"
    "v_exp_f32 v100, v100\n"
    "v_mfma_f32_32x32x16_bf16 v[112:127], v[64:67], v[92:95], v[176:191]\n"
    "s_waitcnt lgkmcnt(1)\n"
    "v_exp_f32 v101, v101\n"
    "v_exp_f32 v102, v102\n"
    "v_exp_f32 v103, v103\n"
    "v_exp_f32 v104, v104\n"
    "v_exp_f32 v105, v105\n"
    "v_exp_f32 v106, v106\n"
    "v_smfmac_f32_32x32x32_bf16 v[128:143], v[48:51], v[96:103], v4\n"
    "s_add_u32 s43, s43, 0x1000\n"
    "buffer_load_dwordx4 v[64:67], v1, s[20:23], s43 offen sc1\n"
    "v_exp_f32 v107, v107\n"
    "v_exp_f32 v108, v108\n"
    "v_exp_f32 v109, v109\n"
    "v_exp_f32 v110, v110\n"
    "v_exp_f32 v111, v111\n"
    "v_exp_f32 v112, v112\n"
    "v_exp_f32 v113, v113\n"
    "s_waitcnt vmcnt(13) lgkmcnt(0)\n"
    "v_smfmac_f32_32x32x32_bf16 v[128:143], v[52:55], v[104:111], v4\n"
    "s_add_u32 m0, s57, 0x440\n"
    "buffer_load_dwordx4 v[200:203], v1, s[20:23], s44 offen offset:2048 sc1\n"
    "v_exp_f32 v114, v114\n"
    "v_exp_f32 v115, v115\n"
    "v_exp_f32 v116, v116\n"
    "v_mfma_f32_32x32x16_bf16 v[96:111], v[68:71], v[88:91], v[160:175]\n"
    "buffer_load_dwordx4 v1, s[20:23], s43 offen offset:1024 sc1 lds\n"
    "v_exp_f32 v117, v117\n"
    "v_exp_f32 v118, v118\n"
    "v_exp_f32 v119, v119\n"
    "v_exp_f32 v120, v120\n"
    "v_exp_f32 v121, v121\n"
    "v_exp_f32 v122, v122\n"
    "v_smfmac_f32_32x32x32_bf16 v[144:159], v[48:51], v[112:119], v4\n"
    "ds_read_b128 v[48:51], v84 offset:3168\n"
    "v_exp_f32 v123, v123\n"
    "v_exp_f32 v124, v124\n"
    "v_exp_f32 v125, v125\n"
    "v_exp_f32 v126, v126\n"
    "v_exp_f32 v127, v127\n"
    "v_exp_f32 v96, v96\n"
    "v_exp_f32 v97, v97\n"
    "v_smfmac_f32_32x32x32_bf16 v[144:159], v[52:55], v[120:127], v4\n"
    "ds_read_b128 v[52:55], v85 offset:3168\n"
    "v_exp_f32 v98, v98\n"
    "v_exp_f32 v99, v99\n"
    "v_exp_f32 v100, v100\n"
    "v_mfma_f32_32x32x16_bf16 v[112:127], v[68:71], v[92:95], v[176:191]\n"
    "s_waitcnt lgkmcnt(1)\n"
    "v_exp_f32 v101, v101\n"
    "v_exp_f32 v102, v102\n"
    "v_exp_f32 v103, v103\n"
    "v_exp_f32 v104, v104\n"
    "v_exp_f32 v105, v105\n"
    "v_exp_f32 v106, v106\n"
    "v_smfmac_f32_32x32x32_bf16 v[128:143], v[48:51], v[96:103], v4\n"
    "buffer_load_dwordx4 v[68:71], v1, s[20:23], s43 offen offset:2048 sc1\n"
    "v_exp_f32 v107, v107\n"
    "v_exp_f32 v108, v108\n"
    "v_exp_f32 v109, v109\n"
    "v_exp_f32 v110, v110\n"
    "v_exp_f32 v111, v111\n"
    "v_exp_f32 v112, v112\n"
    "v_exp_f32 v113, v113\n"
    "s_waitcnt vmcnt(14) lgkmcnt(0)\n"
    "v_smfmac_f32_32x32x32_bf16 v[128:143], v[52:55], v[104:111], v4\n"
    "s_add_u32 m0, s57, 0x60\n"
    "buffer_load_dwordx4 v[204:207], v1, s[20:23], s44 offen offset:3072 sc1\n"
    "v_exp_f32 v114, v114\n"
    "v_exp_f32 v115, v115\n"
    "v_exp_f32 v116, v116\n"
    "v_mfma_f32_32x32x16_bf16 v[96:111], v[72:75], v[88:91], v[160:175]\n"
    "buffer_load_dwordx4 v1, s[20:23], s43 offen offset:3072 sc1 lds\n"
    "v_exp_f32 v117, v117\n"
    "v_exp_f32 v118, v118\n"
    "v_exp_f32 v119, v119\n"
    "v_exp_f32 v120, v120\n"
    "v_exp_f32 v121, v121\n"
    "v_exp_f32 v122, v122\n"
    "v_smfmac_f32_32x32x32_bf16 v[144:159], v[48:51], v[112:119], v4\n"
    "ds_read_b128 v[48:51], v84 offset:4224\n"
    "v_exp_f32 v123, v123\n"
    "v_exp_f32 v124, v124\n"
    "v_exp_f32 v125, v125\n"
    "v_exp_f32 v126, v126\n"
    "v_exp_f32 v127, v127\n"
    "v_exp_f32 v96, v96\n"
    "v_exp_f32 v97, v97\n"
    "v_smfmac_f32_32x32x32_bf16 v[144:159], v[52:55], v[120:127], v4\n"
    "ds_read_b128 v[52:55], v85 offset:4224\n"
    "v_exp_f32 v98, v98\n"
    "v_exp_f32 v99, v99\n"
    "v_exp_f32 v100, v100\n"
    "v_mfma_f32_32x32x16_bf16 v[112:127], v[72:75], v[92:95], v[176:191]\n"
    "s_add_u32 m0, s59, 16384\n"
    "s_add_u32 s45, s68, 16384\n"
    "buffer_load_dwordx4 v1, s[20:23], s45 offen offset:2048 sc1 lds\n"
    "buffer_load_dwordx4 v1, s[20:23], s45 offen offset:3072 sc1 lds\n"
    "s_waitcnt lgkmcnt(1)\n"
    "v_exp_f32 v101, v101\n"
    "v_exp_f32 v102, v102\n"
    "v_exp_f32 v103, v103\n"
    "v_exp_f32 v104, v104\n"
    "v_exp_f32 v105, v105\n"
    "v_exp_f32 v106, v106\n"
    "v_smfmac_f32_32x32x32_bf16 v[128:143], v[48:51], v[96:103], v4\n"
    "s_add_u32 s43, s43, 0x1000\n"
    "buffer_load_dwordx4 v[72:75], v1, s[20:23], s43 offen sc1\n"
    "v_exp_f32 v107, v107\n"
    "v_exp_f32 v108, v108\n"
    "v_exp_f32 v109, v109\n"
    "v_exp_f32 v110, v110\n"
    "v_exp_f32 v111, v111\n"
    "v_exp_f32 v112, v112\n"
    "v_exp_f32 v113, v113\n"
    "s_waitcnt vmcnt(17) lgkmcnt(0)\n"
    "v_smfmac_f32_32x32x32_bf16 v[128:143], v[52:55], v[104:111], v4\n"
    "s_add_u32 m0, s57, 0xc80\n"
    "s_add_u32 s44, s44, 0x1000\n"
    "buffer_load_dwordx4 v[208:211], v1, s[20:23], s44 offen sc1\n"
    "v_exp_f32 v114, v114\n"
    "v_exp_f32 v115, v115\n"
    "v_exp_f32 v116, v116\n"
    "v_mfma_f32_32x32x16_bf16 v[96:111], v[76:79], v[88:91], v[160:175]\n"
    "buffer_load_dwordx4 v1, s[20:23], s43 offen offset:1024 sc1 lds\n"
    "v_exp_f32 v117, v117\n"
    "v_exp_f32 v118, v118\n"
    "v_exp_f32 v119, v119\n"
    "v_exp_f32 v120, v120\n"
    "v_exp_f32 v121, v121\n"
    "v_exp_f32 v122, v122\n"
    "v_smfmac_f32_32x32x32_bf16 v[144:159], v[48:51], v[112:119], v4\n"
    "ds_read_b128 v[48:51], v84 offset:5280\n"
    "v_exp_f32 v123, v123\n"
    "v_exp_f32 v124, v124\n"
    "v_exp_f32 v125, v125\n"
    "v_exp_f32 v126, v126\n"
    "v_exp_f32 v127, v127\n"
    "v_exp_f32 v96, v96\n"
    "v_exp_f32 v97, v97\n"
    "v_smfmac_f32_32x32x32_bf16 v[144:159], v[52:55], v[120:127], v4\n"
    "ds_read_b128 v[52:55], v85 offset:5280\n"
    "v_exp_f32 v98, v98\n"
    "v_exp_f32 v99, v99\n"
    "v_exp_f32 v100, v100\n"
    "v_mfma_f32_32x32x16_bf16 v[112:127], v[76:79], v[92:95], v[176:191]\n"
    "s_waitcnt lgkmcnt(1)\n"
    "v_exp_f32 v101, v101\n"
    "v_exp_f32 v102, v102\n"
    "v_exp_f32 v103, v103\n"
    "v_exp_f32 v104, v104\n"
    "v_exp_f32 v105, v105\n"
    "v_exp_f32 v106, v106\n"
    "v_smfmac_f32_32x32x32_bf16 v[128:143], v[48:51], v[96:103], v4\n"
    "buffer_load_dwordx4 v[76:79], v1, s[20:23], s43 offen offset:2048 sc1\n"
    "v_exp_f32 v107, v107\n"
    "v_exp_f32 v108, v108\n"
    "v_exp_f32 v109, v109\n"
    "v_exp_f32 v110, v110\n"
    "v_exp_f32 v111, v111\n"
    "v_exp_f32 v112, v112\n"
    "v_exp_f32 v113, v113\n"
    "s_waitcnt vmcnt(15) lgkmcnt(0)\n"
    "v_smfmac_f32_32x32x32_bf16 v[128:143], v[52:55], v[104:111], v4\n"
    "s_add_u32 m0, s57, 0x8a0\n"
    "buffer_load_dwordx4 v[212:215], v1, s[20:23], s44 offen offset:1024 sc1\n"
    "v_exp_f32 v114, v114\n"
    "v_exp_f32 v115, v115\n"
    "v_exp_f32 v116, v116\n"
    "v_mfma_f32_32x32x16_bf16 v[96:111], v[56:59], v[88:91], v[160:175]\n"
    "buffer_load_dwordx4 v1, s[20:23], s43 offen offset:3072 sc1 lds\n"
    "v_exp_f32 v117, v117\n"
    "v_exp_f32 v118, v118\n"
    "v_exp_f32 v119, v119\n"
    "v_exp_f32 v120, v120\n"
    "v_exp_f32 v121, v121\n"
    "v_exp_f32 v122, v122\n"
    "v_smfmac_f32_32x32x32_bf16 v[144:159], v[48:51], v[112:119], v4\n"
    "ds_read_b128 v[48:51], v84\n"
    "v_exp_f32 v123, v123\n"
    "v_exp_f32 v124, v124\n"
    "v_exp_f32 v125, v125\n"
    "v_exp_f32 v126, v126\n"
    "v_exp_f32 v127, v127\n"
    "v_exp_f32 v96, v96\n"
    "v_exp_f32 v97, v97\n"
    "v_smfmac_f32_32x32x32_bf16 v[144:159], v[52:55], v[120:127], v4\n"
    "ds_read_b128 v[52:55], v85\n"
    "v_exp_f32 v98, v98\n"
    "v_exp_f32 v99, v99\n"
    "v_exp_f32 v100, v100\n"
    "v_mfma_f32_32x32x16_bf16 v[112:127], v[56:59], v[92:95], v[176:191]\n"
    "s_waitcnt lgkmcnt(1)\n"
    "v_exp_f32 v101, v101\n"
    "v_exp_f32 v102, v102\n"
    "v_exp_f32 v103, v103\n"
    "v_exp_f32 v104, v104\n"
    "v_exp_f32 v105, v105\n"
    "v_exp_f32 v106, v106\n"
    "v_smfmac_f32_32x32x32_bf16 v[128:143], v[48:51], v[96:103], v4\n"
    "s_add_u32 s43, s43, 0x1000\n"
    "buffer_load_dwordx4 v[56:59], v1, s[20:23], s43 offen sc1\n"
    "v_exp_f32 v107, v107\n"
    "v_exp_f32 v108, v108\n"
    "v_exp_f32 v109, v109\n"
    "v_exp_f32 v110, v110\n"
    "v_exp_f32 v111, v111\n"
    "v_exp_f32 v112, v112\n"
    "v_exp_f32 v113, v113\n"
    "s_waitcnt vmcnt(15) lgkmcnt(0)\n"
    "v_smfmac_f32_32x32x32_bf16 v[128:143], v[52:55], v[104:111], v4\n"
    "s_add_u32 m0, s57, 0xfffffc00\n"
    "buffer_load_dwordx4 v[216:219], v1, s[20:23], s44 offen offset:2048 sc1\n"
    "v_exp_f32 v114, v114\n"
    "v_exp_f32 v115, v115\n"
    "v_exp_f32 v116, v116\n"
    "v_mfma_f32_32x32x16_bf16 v[96:111], v[60:63], v[88:91], v[160:175]\n"
    "buffer_load_dwordx4 v1, s[20:23], s43 offen offset:1024 sc1 lds\n"
    "v_exp_f32 v117, v117\n"
    "v_exp_f32 v118, v118\n"
    "v_exp_f32 v119, v119\n"
    "v_exp_f32 v120, v120\n"
    "v_exp_f32 v121, v121\n"
    "v_exp_f32 v122, v122\n"
    "v_smfmac_f32_32x32x32_bf16 v[144:159], v[48:51], v[112:119], v4\n"
    "ds_read_b128 v[48:51], v84 offset:1056\n"
    "v_exp_f32 v123, v123\n"
    "v_exp_f32 v124, v124\n"
    "v_exp_f32 v125, v125\n"
    "v_exp_f32 v126, v126\n"
    "v_exp_f32 v127, v127\n"
    "v_exp_f32 v96, v96\n"
    "v_exp_f32 v97, v97\n"
    "v_smfmac_f32_32x32x32_bf16 v[144:159], v[52:55], v[120:127], v4\n"
    "ds_read_b128 v[52:55], v85 offset:1056\n"
    "v_exp_f32 v98, v98\n"
    "v_exp_f32 v99, v99\n"
    "v_exp_f32 v100, v100\n"
    "v_mfma_f32_32x32x16_bf16 v[112:127], v[60:63], v[92:95], v[176:191]\n"
    "s_waitcnt lgkmcnt(1)\n"
    "v_exp_f32 v101, v101\n"
    "v_exp_f32 v102, v102\n"
    "v_exp_f32 v103, v103\n"
    "v_exp_f32 v104, v104\n"
    "v_exp_f32 v105, v105\n"
    "v_exp_f32 v106, v106\n"
    "v_smfmac_f32_32x32x32_bf16 v[128:143], v[48:51], v[96:103], v4\n"
    "buffer_load_dwordx4 v[60:63], v1, s[20:23], s43 offen offset:2048 sc1\n"
    "v_exp_f32 v107, v107\n"
    "v_exp_f32 v108, v108\n"
    "v_exp_f32 v109, v109\n"
    "v_exp_f32 v110, v110\n"
    "v_exp_f32 v111, v111\n"
    "v_exp_f32 v112, v112\n"
    "v_exp_f32 v113, v113\n"
    "s_waitcnt vmcnt(15) lgkmcnt(0)\n"
    "v_smfmac_f32_32x32x32_bf16 v[128:143], v[52:55], v[104:111], v4\n"
    "s_add_u32 m0, s57, 0xfffff820\n"
    "buffer_load_dwordx4 v[220:223], v1, s[20:23], s44 offen offset:3072 sc1\n"
    "v_exp_f32 v114, v114\n"
    "v_exp_f32 v115, v115\n"
    "v_exp_f32 v116, v116\n"
    "v_mfma_f32_32x32x16_bf16 v[96:111], v[64:67], v[88:91], v[160:175]\n"
    "buffer_load_dwordx4 v1, s[20:23], s43 offen offset:3072 sc1 lds\n"
    "v_exp_f32 v117, v117\n"
    "v_exp_f32 v118, v118\n"
    "v_exp_f32 v119, v119\n"
    "v_exp_f32 v120, v120\n"
    "v_exp_f32 v121, v121\n"
    "v_exp_f32 v122, v122\n"
    "v_smfmac_f32_32x32x32_bf16 v[144:159], v[48:51], v[112:119], v4\n"
    "ds_read_b128 v[48:51], v84 offset:2112\n"
    "v_exp_f32 v123, v123\n"
    "v_exp_f32 v124, v124\n"
    "v_exp_f32 v125, v125\n"
    "v_exp_f32 v126, v126\n"
    "v_exp_f32 v127, v127\n"
    "v_exp_f32 v96, v96\n"
    "v_exp_f32 v97, v97\n"
    "v_smfmac_f32_32x32x32_bf16 v[144:159], v[52:55], v[120:127], v4\n"
    "ds_read_b128 v[52:55], v85 offset:2112\n"
    "v_exp_f32 v98, v98\n"
    "v_exp_f32 v99, v99\n"
    "v_exp_f32 v100, v100\n"
    "v_mfma_f32_32x32x16_bf16 v[112:127], v[64:67], v[92:95], v[176:191]\n"
    "s_add_u32 m0, s59, 20480\n"
    "s_add_u32 s45, s68, 20480\n"
    "buffer_load_dwordx4 v1, s[20:23], s45 offen sc1 lds\n"
    "buffer_load_dwordx4 v1, s[20:23], s45 offen offset:1024 sc1 lds\n"
    "s_waitcnt lgkmcnt(1)\n"
    "v_exp_f32 v101, v101\n"
    "v_exp_f32 v102, v102\n"
    "v_exp_f32 v103, v103\n"
    "v_exp_f32 v104, v104\n"
    "v_exp_f32 v105, v105\n"
    "v_exp_f32 v106, v106\n"
    "v_smfmac_f32_32x32x32_bf16 v[128:143], v[48:51], v[96:103], v4\n"
    "s_add_u32 s43, s43, 0x1000\n"
    "buffer_load_dwordx4 v[64:67], v1, s[20:23], s43 offen sc1\n"
    "v_exp_f32 v107, v107\n"
    "v_exp_f32 v108, v108\n"
    "v_exp_f32 v109, v109\n"
    "v_exp_f32 v110, v110\n"
    "v_exp_f32 v111, v111\n"
    "v_exp_f32 v112, v112\n"
    "v_exp_f32 v113, v113\n"
    "s_waitcnt vmcnt(17) lgkmcnt(0)\n"
    "v_smfmac_f32_32x32x32_bf16 v[128:143], v[52:55], v[104:111], v4\n"
    "s_add_u32 m0, s57, 0x440\n"
    "s_add_u32 s44, s44, 0x1000\n"
    "buffer_load_dwordx4 v[224:227], v1, s[20:23], s44 offen sc1\n"
    "v_exp_f32 v114, v114\n"
    "v_exp_f32 v115, v115\n"
    "v_exp_f32 v116, v116\n"
    "v_mfma_f32_32x32x16_bf16 v[96:111], v[68:71], v[88:91], v[160:175]\n"
    "buffer_load_dwordx4 v1, s[20:23], s43 offen offset:1024 sc1 lds\n"
    "v_exp_f32 v117, v117\n"
    "v_exp_f32 v118, v118\n"
    "v_exp_f32 v119, v119\n"
    "v_exp_f32 v120, v120\n"
    "v_exp_f32 v121, v121\n"
    "v_exp_f32 v122, v122\n"
    "v_smfmac_f32_32x32x32_bf16 v[144:159], v[48:51], v[112:119], v4\n"
    "ds_read_b128 v[48:51], v84 offset:3168\n"
    "v_exp_f32 v123, v123\n"
    "v_exp_f32 v124, v124\n"
    "v_exp_f32 v125, v125\n"
    "v_exp_f32 v126, v126\n"
    "v_exp_f32 v127, v127\n"
    "v_exp_f32 v96, v96\n"
    "v_exp_f32 v97, v97\n"
    "v_smfmac_f32_32x32x32_bf16 v[144:159], v[52:55], v[120:127], v4\n"
    "ds_read_b128 v[52:55], v85 offset:3168\n"
    "v_exp_f32 v98, v98\n"
    "v_exp_f32 v99, v99\n"
    "v_exp_f32 v100, v100\n"
    "v_mfma_f32_32x32x16_bf16 v[112:127], v[68:71], v[92:95], v[176:191]\n"
    "s_waitcnt lgkmcnt(1)\n"
    "v_exp_f32 v101, v101\n"
    "v_exp_f32 v102, v102\n"
    "v_exp_f32 v103, v103\n"
    "v_exp_f32 v104, v104\n"
    "v_exp_f32 v105, v105\n"
    "v_exp_f32 v106, v106\n"
    "v_smfmac_f32_32x32x32_bf16 v[128:143], v[48:51], v[96:103], v4\n"
    "buffer_load_dwordx4 v[68:71], v1, s[20:23], s43 offen offset:2048 sc1\n"
    "v_exp_f32 v107, v107\n"
    "v_exp_f32 v108, v108\n"
    "v_exp_f32 v109, v109\n"
    "v_exp_f32 v110, v110\n"
    "v_exp_f32 v111, v111\n"
    "v_exp_f32 v112, v112\n"
    "v_exp_f32 v113, v113\n"
    "s_waitcnt vmcnt(15) lgkmcnt(0)\n"
    "v_smfmac_f32_32x32x32_bf16 v[128:143], v[52:55], v[104:111], v4\n"
    "s_add_u32 m0, s57, 0x60\n"
    "buffer_load_dwordx4 v[228:231], v1, s[20:23], s44 offen offset:1024 sc1\n"
    "v_exp_f32 v114, v114\n"
    "v_exp_f32 v115, v115\n"
    "v_exp_f32 v116, v116\n"
    "v_mfma_f32_32x32x16_bf16 v[96:111], v[72:75], v[88:91], v[160:175]\n"
    "buffer_load_dwordx4 v1, s[20:23], s43 offen offset:3072 sc1 lds\n"
    "v_exp_f32 v117, v117\n"
    "v_exp_f32 v118, v118\n"
    "v_exp_f32 v119, v119\n"
    "v_exp_f32 v120, v120\n"
    "v_exp_f32 v121, v121\n"
    "v_exp_f32 v122, v122\n"
    "v_smfmac_f32_32x32x32_bf16 v[144:159], v[48:51], v[112:119], v4\n"
    "ds_read_b128 v[48:51], v84 offset:4224\n"
    "v_exp_f32 v123, v123\n"
    "v_exp_f32 v124, v124\n"
    "v_exp_f32 v125, v125\n"
    "v_exp_f32 v126, v126\n"
    "v_exp_f32 v127, v127\n"
    "v_exp_f32 v96, v96\n"
    "v_exp_f32 v97, v97\n"
    "v_smfmac_f32_32x32x32_bf16 v[144:159], v[52:55], v[120:127], v4\n"
    "ds_read_b128 v[52:55], v85 offset:4224\n"
    "v_exp_f32 v98, v98\n"
    "v_exp_f32 v99, v99\n"
    "v_exp_f32 v100, v100\n"
    "v_mfma_f32_32x32x16_bf16 v[112:127], v[72:75], v[92:95], v[176:191]\n"
    "s_waitcnt lgkmcnt(1)\n"
    "v_exp_f32 v101, v101\n"
    "v_exp_f32 v102, v102\n"
    "v_exp_f32 v103, v103\n"
    "v_exp_f32 v104, v104\n"
    "v_exp_f32 v105, v105\n"
    "v_exp_f32 v106, v106\n"
    "v_smfmac_f32_32x32x32_bf16 v[128:143], v[48:51], v[96:103], v4\n"
    "s_add_u32 s43, s43, 0x1000\n"
    "buffer_load_dwordx4 v[72:75], v1, s[20:23], s43 offen sc1\n"
    "v_exp_f32 v107, v107\n"
    "v_exp_f32 v108, v108\n"
    "v_exp_f32 v109, v109\n"
    "v_exp_f32 v110, v110\n"
    "v_exp_f32 v111, v111\n"
    "v_exp_f32 v112, v112\n"
    "v_exp_f32 v113, v113\n"
    "s_waitcnt vmcnt(15) lgkmcnt(0)\n"
    "v_smfmac_f32_32x32x32_bf16 v[128:143], v[52:55], v[104:111], v4\n"
    "s_add_u32 m0, s57, 0xc80\n"
    "buffer_load_dwordx4 v[232:235], v1, s[20:23], s44 offen offset:2048 sc1\n"
    "v_exp_f32 v114, v114\n"
    "v_exp_f32 v115, v115\n"
    "v_exp_f32 v116, v116\n"
    "v_mfma_f32_32x32x16_bf16 v[96:111], v[76:79], v[88:91], v[160:175]\n"
    "buffer_load_dwordx4 v1, s[20:23], s43 offen offset:1024 sc1 lds\n"
    "v_exp_f32 v117, v117\n"
    "v_exp_f32 v118, v118\n"
    "v_exp_f32 v119, v119\n"
    "v_exp_f32 v120, v120\n"
    "v_exp_f32 v121, v121\n"
    "v_exp_f32 v122, v122\n"
    "v_smfmac_f32_32x32x32_bf16 v[144:159], v[48:51], v[112:119], v4\n"
    "ds_read_b128 v[48:51], v84 offset:5280\n"
    "v_exp_f32 v123, v123\n"
    "v_exp_f32 v124, v124\n"
    "v_exp_f32 v125, v125\n"
    "v_exp_f32 v126, v126\n"
    "v_exp_f32 v127, v127\n"
    "v_exp_f32 v96, v96\n"
    "v_exp_f32 v97, v97\n"
    "v_smfmac_f32_32x32x32_bf16 v[144:159], v[52:55], v[120:127], v4\n"
    "ds_read_b128 v[52:55], v85 offset:5280\n"
    "v_exp_f32 v98, v98\n"
    "v_exp_f32 v99, v99\n"
    "v_exp_f32 v100, v100\n"
    "v_mfma_f32_32x32x16_bf16 v[112:127], v[76:79], v[92:95], v[176:191]\n"
    "s_waitcnt lgkmcnt(1)\n"
    "v_exp_f32 v101, v101\n"
    "v_exp_f32 v102, v102\n"
    "v_exp_f32 v103, v103\n"
    "v_exp_f32 v104, v104\n"
    "v_exp_f32 v105, v105\n"
    "v_exp_f32 v106, v106\n"
    "v_smfmac_f32_32x32x32_bf16 v[128:143], v[48:51], v[96:103], v4\n"
    "buffer_load_dwordx4 v[76:79], v1, s[20:23], s43 offen offset:2048 sc1\n"
    "v_exp_f32 v107, v107\n"
    "v_exp_f32 v108, v108\n"
    "v_exp_f32 v109, v109\n"
    "v_exp_f32 v110, v110\n"
    "v_exp_f32 v111, v111\n"
    "v_exp_f32 v112, v112\n"
    "v_exp_f32 v113, v113\n"
    "s_waitcnt vmcnt(15) lgkmcnt(0)\n"
    "v_smfmac_f32_32x32x32_bf16 v[128:143], v[52:55], v[104:111], v4\n"
    "s_add_u32 m0, s57, 0x8a0\n"
    "buffer_load_dwordx4 v[236:239], v1, s[20:23], s44 offen offset:3072 sc1\n"
    "v_exp_f32 v114, v114\n"
    "v_exp_f32 v115, v115\n"
    "v_exp_f32 v116, v116\n"
    "v_mfma_f32_32x32x16_bf16 v[96:111], v[56:59], v[88:91], v[160:175]\n"
    "buffer_load_dwordx4 v1, s[20:23], s43 offen offset:3072 sc1 lds\n"
    "v_exp_f32 v117, v117\n"
    "v_exp_f32 v118, v118\n"
    "v_exp_f32 v119, v119\n"
    "v_exp_f32 v120, v120\n"
    "v_exp_f32 v121, v121\n"
    "v_exp_f32 v122, v122\n"
    "v_smfmac_f32_32x32x32_bf16 v[144:159], v[48:51], v[112:119], v4\n"
    "ds_read_b128 v[48:51], v84\n"
    "v_exp_f32 v123, v123\n"
    "v_exp_f32 v124, v124\n"
    "v_exp_f32 v125, v125\n"
    "v_exp_f32 v126, v126\n"
    "v_exp_f32 v127, v127\n"
    "v_exp_f32 v96, v96\n"
    "v_exp_f32 v97, v97\n"
    "v_smfmac_f32_32x32x32_bf16 v[144:159], v[52:55], v[120:127], v4\n"
    "ds_read_b128 v[52:55], v85\n"
    "v_exp_f32 v98, v98\n"
    "v_exp_f32 v99, v99\n"
    "v_exp_f32 v100, v100\n"
    "v_mfma_f32_32x32x16_bf16 v[112:127], v[56:59], v[92:95], v[176:191]\n"
    "s_add_u32 m0, s59, 20480\n"
    "s_add_u32 s45, s68, 20480\n"
    "buffer_load_dwordx4 v1, s[20:23], s45 offen offset:2048 sc1 lds\n"
    "buffer_load_dwordx4 v1, s[20:23], s45 offen offset:3072 sc1 lds\n"
    "s_waitcnt lgkmcnt(1)\n"
    "v_exp_f32 v101, v101\n"
    "v_exp_f32 v102, v102\n"
    "v_exp_f32 v103, v103\n"
    "v_exp_f32 v104, v104\n"
    "v_exp_f32 v105, v105\n"
    "v_exp_f32 v106, v106\n"
    "v_smfmac_f32_32x32x32_bf16 v[128:143], v[48:51], v[96:103], v4\n"
    "s_add_u32 s43, s43, 0x1000\n"
    "buffer_load_dwordx4 v[56:59], v1, s[20:23], s43 offen sc1\n"
    "v_exp_f32 v107, v107\n"
    "v_exp_f32 v108, v108\n"
    "v_exp_f32 v109, v109\n"
    "v_exp_f32 v110, v110\n"
    "v_exp_f32 v111, v111\n"
    "v_exp_f32 v112, v112\n"
    "v_exp_f32 v113, v113\n"
    "s_waitcnt vmcnt(17) lgkmcnt(0)\n"
    "v_smfmac_f32_32x32x32_bf16 v[128:143], v[52:55], v[104:111], v4\n"
    "s_add_u32 m0, s57, 0xfffffc00\n"
    "s_add_u32 s44, s44, 0x1000\n"
    "buffer_load_dwordx4 v[240:243], v1, s[20:23], s44 offen sc1\n"
    "v_exp_f32 v114, v114\n"
    "v_exp_f32 v115, v115\n"
    "v_exp_f32 v116, v116\n"
    "v_mfma_f32_32x32x16_bf16 v[96:111], v[60:63], v[88:91], v[160:175]\n"
    "buffer_load_dwordx4 v1, s[20:23], s43 offen offset:1024 sc1 lds\n"
    "v_exp_f32 v117, v117\n"
    "v_exp_f32 v118, v118\n"
    "v_exp_f32 v119, v119\n"
    "v_exp_f32 v120, v120\n"
    "v_exp_f32 v121, v121\n"
    "v_exp_f32 v122, v122\n"
    "v_smfmac_f32_32x32x32_bf16 v[144:159], v[48:51], v[112:119], v4\n"
    "ds_read_b128 v[48:51], v84 offset:1056\n"
    "v_exp_f32 v123, v123\n"
    "v_exp_f32 v124, v124\n"
    "v_exp_f32 v125, v125\n"
    "v_exp_f32 v126, v126\n"
    "v_exp_f32 v127, v127\n"
    "v_exp_f32 v96, v96\n"
    "v_exp_f32 v97, v97\n"
    "v_smfmac_f32_32x32x32_bf16 v[144:159], v[52:55], v[120:127], v4\n"
    "ds_read_b128 v[52:55], v85 offset:1056\n"
    "v_exp_f32 v98, v98\n"
    "v_exp_f32 v99, v99\n"
    "v_exp_f32 v100, v100\n"
    "v_mfma_f32_32x32x16_bf16 v[112:127], v[60:63], v[92:95], v[176:191]\n"
    "s_waitcnt lgkmcnt(1)\n"
    "v_exp_f32 v101, v101\n"
    "v_exp_f32 v102, v102\n"
    "v_exp_f32 v103, v103\n"
    "v_exp_f32 v104, v104\n"
    "v_exp_f32 v105, v105\n"
    "v_exp_f32 v106, v106\n"
    "v_smfmac_f32_32x32x32_bf16 v[128:143], v[48:51], v[96:103], v4\n"
    "buffer_load_dwordx4 v[60:63], v1, s[20:23], s43 offen offset:2048 sc1\n"
    "v_exp_f32 v107, v107\n"
    "v_exp_f32 v108, v108\n"
    "v_exp_f32 v109, v109\n"
    "v_exp_f32 v110, v110\n"
    "v_exp_f32 v111, v111\n"
    "v_exp_f32 v112, v112\n"
    "v_exp_f32 v113, v113\n"
    "s_waitcnt vmcnt(15) lgkmcnt(0)\n"
    "v_smfmac_f32_32x32x32_bf16 v[128:143], v[52:55], v[104:111], v4\n"
    "s_add_u32 m0, s57, 0xfffff820\n"
    "buffer_load_dwordx4 v[244:247], v1, s[20:23], s44 offen offset:1024 sc1\n"
    "v_exp_f32 v114, v114\n"
    "v_exp_f32 v115, v115\n"
    "v_exp_f32 v116, v116\n"
    "v_mfma_f32_32x32x16_bf16 v[96:111], v[64:67], v[88:91], v[160:175]\n"
    "buffer_load_dwordx4 v1, s[20:23], s43 offen offset:3072 sc1 lds\n"
    "v_exp_f32 v117, v117\n"
    "v_exp_f32 v118, v118\n"
    "v_exp_f32 v119, v119\n"
    "v_exp_f32 v120, v120\n"
    "v_exp_f32 v121, v121\n"
    "v_exp_f32 v122, v122\n"
    "v_smfmac_f32_32x32x32_bf16 v[144:159], v[48:51], v[112:119], v4\n"
    "ds_read_b128 v[48:51], v84 offset:2112\n"
    "v_exp_f32 v123, v123\n"
    "v_exp_f32 v124, v124\n"
    "v_exp_f32 v125, v125\n"
    "v_exp_f32 v126, v126\n"
    "v_exp_f32 v127, v127\n"
    "v_exp_f32 v96, v96\n"
    "v_exp_f32 v97, v97\n"
    "v_smfmac_f32_32x32x32_bf16 v[144:159], v[52:55], v[120:127], v4\n"
    "ds_read_b128 v[52:55], v85 offset:2112\n"
    "v_exp_f32 v98, v98\n"
    "v_exp_f32 v99, v99\n"
    "v_exp_f32 v100, v100\n"
    "v_mfma_f32_32x32x16_bf16 v[112:127], v[64:67], v[92:95], v[176:191]\n"
    "s_waitcnt lgkmcnt(1)\n"
    "v_exp_f32 v101, v101\n"
    "v_exp_f32 v102, v102\n"
    "v_exp_f32 v103, v103\n"
    "v_exp_f32 v104, v104\n"
    "v_exp_f32 v105, v105\n"
    "v_exp_f32 v106, v106\n"
    "v_smfmac_f32_32x32x32_bf16 v[128:143], v[48:51], v[96:103], v4\n"
    "s_lshl_b32 s69, s35, 1\n"
    "s_add_u32 s69, s69, s40\n"
    "v_exp_f32 v107, v107\n"
    "v_exp_f32 v108, v108\n"
    "v_exp_f32 v109, v109\n"
    "v_exp_f32 v110, v110\n"
    "v_exp_f32 v111, v111\n"
    "v_exp_f32 v112, v112\n"
    "v_exp_f32 v113, v113\n"
    "s_waitcnt vmcnt(14) lgkmcnt(0)\n"
    "v_smfmac_f32_32x32x32_bf16 v[128:143], v[52:55], v[104:111], v4\n"
    "s_lshl_b32 s69, s69, 4\n"
    "s_add_u32 s69, s69, s34\n"
    "buffer_load_dwordx4 v[248:251], v1, s[20:23], s44 offen offset:2048 sc1\n"
    "v_exp_f32 v114, v114\n"
    "v_exp_f32 v115, v115\n"
    "v_exp_f32 v116, v116\n"
    "v_mfma_f32_32x32x16_bf16 v[96:111], v[68:71], v[88:91], v[160:175]\n"
    "s_lshl_b32 s69, s69, 2\n"
    "v_exp_f32 v117, v117\n"
    "v_exp_f32 v118, v118\n"
    "v_exp_f32 v119, v119\n"
    "v_exp_f32 v120, v120\n"
    "v_exp_f32 v121, v121\n"
    "v_exp_f32 v122, v122\n"
    "v_smfmac_f32_32x32x32_bf16 v[144:159], v[48:51], v[112:119], v4\n"
    "ds_read_b128 v[48:51], v84 offset:3168\n"
    "v_exp_f32 v123, v123\n"
    "v_exp_f32 v124, v124\n"
    "v_exp_f32 v125, v125\n"
    "v_exp_f32 v126, v126\n"
    "v_exp_f32 v127, v127\n"
    "v_exp_f32 v96, v96\n"
    "v_exp_f32 v97, v97\n"
    "v_smfmac_f32_32x32x32_bf16 v[144:159], v[52:55], v[120:127], v4\n"
    "ds_read_b128 v[52:55], v85 offset:3168\n"
    "v_exp_f32 v98, v98\n"
    "v_exp_f32 v99, v99\n"
    "v_exp_f32 v100, v100\n"
    "v_mfma_f32_32x32x16_bf16 v[112:127], v[68:71], v[92:95], v[176:191]\n"
    "s_waitcnt lgkmcnt(1)\n"
    "v_exp_f32 v101, v101\n"
    "v_exp_f32 v102, v102\n"
    "v_exp_f32 v103, v103\n"
    "v_exp_f32 v104, v104\n"
    "v_exp_f32 v105, v105\n"
    "v_exp_f32 v106, v106\n"
    "v_smfmac_f32_32x32x32_bf16 v[128:143], v[48:51], v[96:103], v4\n"
    "s_add_u32 s69, s69, s39\n"
    "s_lshl_b32 s69, s69, 9\n"
    "v_exp_f32 v107, v107\n"
    "v_exp_f32 v108, v108\n"
    "v_exp_f32 v109, v109\n"
    "v_exp_f32 v110, v110\n"
    "v_exp_f32 v111, v111\n"
    "v_exp_f32 v112, v112\n"
    "v_exp_f32 v113, v113\n"
    "s_waitcnt vmcnt(12) lgkmcnt(0)\n"
    "v_smfmac_f32_32x32x32_bf16 v[128:143], v[52:55], v[104:111], v4\n"
    "s_add_u32 s69, s69, 6291456\n"
    "s_lshr_b32 s48, s35, 2\n"
    "buffer_load_dwordx4 v[252:255], v1, s[20:23], s44 offen offset:3072 sc1\n"
    "v_exp_f32 v114, v114\n"
    "v_exp_f32 v115, v115\n"
    "v_exp_f32 v116, v116\n"
    "v_mfma_f32_32x32x16_bf16 v[96:111], v[72:75], v[88:91], v[160:175]\n"
    "s_lshl_b32 s48, s48, 1\n"
    "v_exp_f32 v117, v117\n"
    "v_exp_f32 v118, v118\n"
    "v_exp_f32 v119, v119\n"
    "v_exp_f32 v120, v120\n"
    "v_exp_f32 v121, v121\n"
    "v_exp_f32 v122, v122\n"
    "v_smfmac_f32_32x32x32_bf16 v[144:159], v[48:51], v[112:119], v4\n"
    "ds_read_b128 v[48:51], v84 offset:4224\n"
    "v_exp_f32 v123, v123\n"
    "v_exp_f32 v124, v124\n"
    "v_exp_f32 v125, v125\n"
    "v_exp_f32 v126, v126\n"
    "v_exp_f32 v127, v127\n"
    "v_exp_f32 v96, v96\n"
    "v_exp_f32 v97, v97\n"
    "v_smfmac_f32_32x32x32_bf16 v[144:159], v[52:55], v[120:127], v4\n"
    "ds_read_b128 v[52:55], v85 offset:4224\n"
    "v_exp_f32 v98, v98\n"
    "v_exp_f32 v99, v99\n"
    "v_exp_f32 v100, v100\n"
    "v_mfma_f32_32x32x16_bf16 v[112:127], v[72:75], v[92:95], v[176:191]\n"
    "s_waitcnt lgkmcnt(1)\n"
    "v_exp_f32 v101, v101\n"
    "v_exp_f32 v102, v102\n"
    "v_exp_f32 v103, v103\n"
    "v_exp_f32 v104, v104\n"
    "v_exp_f32 v105, v105\n"
    "v_exp_f32 v106, v106\n"
    "v_smfmac_f32_32x32x32_bf16 v[128:143], v[48:51], v[96:103], v4\n"
    "s_and_b32 s54, s35, 1\n"
    "s_add_u32 s48, s48, s54\n"
    "v_exp_f32 v107, v107\n"
    "v_exp_f32 v108, v108\n"
    "v_exp_f32 v109, v109\n"
    "v_exp_f32 v110, v110\n"
    "v_exp_f32 v111, v111\n"
    "v_exp_f32 v112, v112\n"
    "v_exp_f32 v113, v113\n"
    "s_waitcnt vmcnt(10) lgkmcnt(0)\n"
    "v_smfmac_f32_32x32x32_bf16 v[128:143], v[52:55], v[104:111], v4\n"
    "s_lshl_b32 s48, s48, 9\n"
    "s_and_b32 s54, s35, 2\n"
    "v_exp_f32 v114, v114\n"
    "v_exp_f32 v115, v115\n"
    "v_exp_f32 v116, v116\n"
    "v_mfma_f32_32x32x16_bf16 v[96:111], v[76:79], v[88:91], v[160:175]\n"
    "s_lshl_b32 s54, s54, 2\n"
    "v_exp_f32 v117, v117\n"
    "v_exp_f32 v118, v118\n"
    "v_exp_f32 v119, v119\n"
    "v_exp_f32 v120, v120\n"
    "v_exp_f32 v121, v121\n"
    "v_exp_f32 v122, v122\n"
    "v_smfmac_f32_32x32x32_bf16 v[144:159], v[48:51], v[112:119], v4\n"
    "ds_read_b128 v[48:51], v84 offset:5280\n"
    "v_exp_f32 v123, v123\n"
    "v_exp_f32 v124, v124\n"
    "v_exp_f32 v125, v125\n"
    "v_exp_f32 v126, v126\n"
    "v_exp_f32 v127, v127\n"
    "v_exp_f32 v96, v96\n"
    "v_exp_f32 v97, v97\n"
    "v_smfmac_f32_32x32x32_bf16 v[144:159], v[52:55], v[120:127], v4\n"
    "ds_read_b128 v[52:55], v85 offset:5280\n"
    "v_exp_f32 v98, v98\n"
    "v_exp_f32 v99, v99\n"
    "v_exp_f32 v100, v100\n"
    "v_mfma_f32_32x32x16_bf16 v[112:127], v[76:79], v[92:95], v[176:191]\n"
    "s_waitcnt lgkmcnt(1)\n"
    "v_exp_f32 v101, v101\n"
    "v_exp_f32 v102, v102\n"
    "v_exp_f32 v103, v103\n"
    "v_exp_f32 v104, v104\n"
    "v_exp_f32 v105, v105\n"
    "v_exp_f32 v106, v106\n"
    "v_smfmac_f32_32x32x32_bf16 v[128:143], v[48:51], v[96:103], v4\n"
    "s_add_u32 s48, s48, s54\n"
    "s_add_u32 s48, s48, s34\n"
    "v_exp_f32 v107, v107\n"
    "v_exp_f32 v108, v108\n"
    "v_exp_f32 v109, v109\n"
    "v_exp_f32 v110, v110\n"
    "v_exp_f32 v111, v111\n"
    "v_exp_f32 v112, v112\n"
    "v_exp_f32 v113, v113\n"
    "s_waitcnt vmcnt(5) lgkmcnt(0)\n"
    "v_smfmac_f32_32x32x32_bf16 v[128:143], v[52:55], v[104:111], v4\n"
    "s_lshl_b32 s48, s48, 2\n"
    "s_add_u32 s48, s48, 1441792\n"
    "v_exp_f32 v114, v114\n"
    "v_exp_f32 v115, v115\n"
    "v_exp_f32 v116, v116\n"
    "v_mfma_f32_32x32x16_bf16 v[96:111], v[56:59], v[88:91], v[160:175]\n"
    "s_lshl_b32 s49, s56, 5\n"
    "v_exp_f32 v117, v117\n"
    "v_exp_f32 v118, v118\n"
    "v_exp_f32 v119, v119\n"
    "v_exp_f32 v120, v120\n"
    "v_exp_f32 v121, v121\n"
    "v_exp_f32 v122, v122\n"
    "v_smfmac_f32_32x32x32_bf16 v[144:159], v[48:51], v[112:119], v4\n"
    "ds_read_b128 v[48:51], v84\n"
    "v_exp_f32 v123, v123\n"
    "v_exp_f32 v124, v124\n"
    "v_exp_f32 v125, v125\n"
    "v_exp_f32 v126, v126\n"
    "v_exp_f32 v127, v127\n"
    "v_exp_f32 v96, v96\n"
    "v_exp_f32 v97, v97\n"
    "v_smfmac_f32_32x32x32_bf16 v[144:159], v[52:55], v[120:127], v4\n"
    "ds_read_b128 v[52:55], v85\n"
    "v_exp_f32 v98, v98\n"
    "v_exp_f32 v99, v99\n"
    "v_exp_f32 v100, v100\n"
    "v_mfma_f32_32x32x16_bf16 v[112:127], v[56:59], v[92:95], v[176:191]\n"
    "s_waitcnt lgkmcnt(1)\n"
    "v_exp_f32 v101, v101\n"
    "v_exp_f32 v102, v102\n"
    "v_exp_f32 v103, v103\n"
    "v_exp_f32 v104, v104\n"
    "v_exp_f32 v105, v105\n"
    "v_exp_f32 v106, v106\n"
    "v_smfmac_f32_32x32x32_bf16 v[128:143], v[48:51], v[96:103], v4\n"
    "s_add_u32 s49, s49, s33\n"
    "s_andn2_b32 s49, s49, 1\n"
    "v_exp_f32 v107, v107\n"
    "v_exp_f32 v108, v108\n"
    "v_exp_f32 v109, v109\n"
    "v_exp_f32 v110, v110\n"
    "v_exp_f32 v111, v111\n"
    "v_exp_f32 v112, v112\n"
    "v_exp_f32 v113, v113\n"
    "s_waitcnt vmcnt(2) lgkmcnt(0)\n"
    "v_smfmac_f32_32x32x32_bf16 v[128:143], v[52:55], v[104:111], v4\n"
    "s_lshl_b32 s49, s49, 5\n"
    "s_add_u32 s49, s49, s3\n"
    "v_exp_f32 v114, v114\n"
    "v_exp_f32 v115, v115\n"
    "v_exp_f32 v116, v116\n"
    "v_mfma_f32_32x32x16_bf16 v[96:111], v[60:63], v[88:91], v[160:175]\n"
    "s_lshl_b32 s49, s49, 2\n"
    "v_exp_f32 v117, v117\n"
    "v_exp_f32 v118, v118\n"
    "v_exp_f32 v119, v119\n"
    "v_exp_f32 v120, v120\n"
    "v_exp_f32 v121, v121\n"
    "v_exp_f32 v122, v122\n"
    "v_smfmac_f32_32x32x32_bf16 v[144:159], v[48:51], v[112:119], v4\n"
    "ds_read_b128 v[48:51], v84 offset:1056\n"
    "v_exp_f32 v123, v123\n"
    "v_exp_f32 v124, v124\n"
    "v_exp_f32 v125, v125\n"
    "v_exp_f32 v126, v126\n"
    "v_exp_f32 v127, v127\n"
    "v_exp_f32 v96, v96\n"
    "v_exp_f32 v97, v97\n"
    "v_smfmac_f32_32x32x32_bf16 v[144:159], v[52:55], v[120:127], v4\n"
    "ds_read_b128 v[52:55], v85 offset:1056\n"
    "v_exp_f32 v98, v98\n"
    "v_exp_f32 v99, v99\n"
    "v_exp_f32 v100, v100\n"
    "v_mfma_f32_32x32x16_bf16 v[112:127], v[60:63], v[92:95], v[176:191]\n"
    "s_waitcnt lgkmcnt(1)\n"
    "v_exp_f32 v101, v101\n"
    "v_exp_f32 v102, v102\n"
    "v_exp_f32 v103, v103\n"
    "v_exp_f32 v104, v104\n"
    "v_exp_f32 v105, v105\n"
    "v_exp_f32 v106, v106\n"
    "v_smfmac_f32_32x32x32_bf16 v[128:143], v[48:51], v[96:103], v4\n"
    "s_add_u32 s49, s49, 1441792\n"
    "s_xor_b32 s53, s3, 4\n"
    "v_exp_f32 v107, v107\n"
    "v_exp_f32 v108, v108\n"
    "v_exp_f32 v109, v109\n"
    "v_exp_f32 v110, v110\n"
    "v_exp_f32 v111, v111\n"
    "v_exp_f32 v112, v112\n"
    "v_exp_f32 v113, v113\n"
    "s_waitcnt lgkmcnt(0)\n"
    "v_smfmac_f32_32x32x32_bf16 v[128:143], v[52:55], v[104:111], v4\n"
    "s_mul_i32 s53, s53, 6336\n"
    "s_add_u32 s53, s53, 98304\n"
    "v_exp_f32 v114, v114\n"
    "v_exp_f32 v115, v115\n"
    "v_exp_f32 v116, v116\n"
    "v_exp_f32 v117, v117\n"
    "v_exp_f32 v118, v118\n"
    "v_exp_f32 v119, v119\n"
    "v_exp_f32 v120, v120\n"
    "v_exp_f32 v121, v121\n"
    "v_exp_f32 v122, v122\n"
    "v_smfmac_f32_32x32x32_bf16 v[144:159], v[48:51], v[112:119], v4\n"
    "v_exp_f32 v123, v123\n"
    "v_exp_f32 v124, v124\n"
    "v_exp_f32 v125, v125\n"
    "v_exp_f32 v126, v126\n"
    "v_exp_f32 v127, v127\n"
    "s_branch .Lk_b_end\n"
    ".Lk_b_g1:\n"
    "s_waitcnt vmcnt(10)\n"
    "ds_read_b128 v[48:51], v84\n"
    "v_mfma_f32_32x32x16_bf16 v[96:111], v[56:59], v[88:91], v[160:175]\n"
    "s_setprio 2\n"
    "s_nop 10\n"
    "v_exp_f32 v96, v96\n"
    "v_exp_f32 v97, v97\n"
    "ds_read_b128 v[52:55], v85\n"
    "v_exp_f32 v98, v98\n"
    "v_exp_f32 v99, v99\n"
    "v_exp_f32 v100, v100\n"
    "v_mfma_f32_32x32x16_bf16 v[112:127], v[56:59], v[92:95], v[176:191]\n"
    "s_waitcnt lgkmcnt(1)\n"
    "v_exp_f32 v101, v101\n"
    "v_exp_f32 v102, v102\n"
    "v_exp_f32 v103, v103\n"
    "v_exp_f32 v104, v104\n"
    "v_exp_f32 v105, v105\n"
    "v_exp_f32 v106, v106\n"
    "v_smfmac_f32_32x32x32_bf16 v[128:143], v[48:51], v[96:103], v4\n"
    "s_add_u32 s43, s43, 0x1000\n"
    "buffer_load_dwordx4 v[56:59], v1, s[20:23], s43 offen sc1\n"
    "v_exp_f32 v107, v107\n"
    "v_exp_f32 v108, v108\n"
    "v_exp_f32 v109, v109\n"
    "v_exp_f32 v110, v110\n"
    "v_exp_f32 v111, v111\n"
    "v_exp_f32 v112, v112\n"
    "v_exp_f32 v113, v113\n"
    "s_waitcnt vmcnt(9) lgkmcnt(0)\n"
    "v_smfmac_f32_32x32x32_bf16 v[128:143], v[52:55], v[104:111], v4\n"
    "s_add_u32 m0, s57, 0xfffffc00\n"
    "v_exp_f32 v114, v114\n"
    "v_exp_f32 v115, v115\n"
    "v_exp_f32 v116, v116\n"
    "v_mfma_f32_32x32x16_bf16 v[96:111], v[60:63], v[88:91], v[160:175]\n"
    "buffer_load_dwordx4 v1, s[20:23], s43 offen offset:1024 sc1 lds\n"
    "v_exp_f32 v117, v117\n"
    "v_exp_f32 v118, v118\n"
    "v_exp_f32 v119, v119\n"
    "v_exp_f32 v120, v120\n"
    "v_exp_f32 v121, v121\n"
    "v_exp_f32 v122, v122\n"
    "v_smfmac_f32_32x32x32_bf16 v[144:159], v[48:51], v[112:119], v4\n"
    "ds_read_b128 v[48:51], v84 offset:1056\n"
    "v_exp_f32 v123, v123\n"
    "v_exp_f32 v124, v124\n"
    "v_exp_f32 v125, v125\n"
    "v_exp_f32 v126, v126\n"
    "v_exp_f32 v127, v127\n"
    "v_exp_f32 v96, v96\n"
    "v_exp_f32 v97, v97\n"
    "v_smfmac_f32_32x32x32_bf16 v[144:159], v[52:55], v[120:127], v4\n"
    "ds_read_b128 v[52:55], v85 offset:1056\n"
    "ds_write_b128 v26, v[192:195]\n"
    "ds_write_b128 v26, v[196:199] offset:1024\n"
    "ds_write_b128 v26, v[200:203] offset:2048\n"
    "ds_write_b128 v26, v[204:207] offset:3072\n"
    "v_exp_f32 v98, v98\n"
    "v_exp_f32 v99, v99\n"
    "v_exp_f32 v100, v100\n"
    "v_mfma_f32_32x32x16_bf16 v[112:127], v[60:63], v[92:95], v[176:191]\n"
    "s_waitcnt lgkmcnt(5)\n"
    "v_exp_f32 v101, v101\n"
    "v_exp_f32 v102, v102\n"
    "v_exp_f32 v103, v103\n"
    "v_exp_f32 v104, v104\n"
    "v_exp_f32 v105, v105\n"
    "v_exp_f32 v106, v106\n"
    "v_smfmac_f32_32x32x32_bf16 v[128:143], v[48:51], v[96:103], v4\n"
    "buffer_load_dwordx4 v[60:63], v1, s[20:23], s43 offen offset:2048 sc1\n"
    "v_exp_f32 v107, v107\n"
    "v_exp_f32 v108, v108\n"
    "v_exp_f32 v109, v109\n"
    "v_exp_f32 v110, v110\n"
    "v_exp_f32 v111, v111\n"
    "v_exp_f32 v112, v112\n"
    "v_exp_f32 v113, v113\n"
    "s_waitcnt vmcnt(9) lgkmcnt(0)\n"
    "v_smfmac_f32_32x32x32_bf16 v[128:143], v[52:55], v[104:111], v4\n"
    "s_add_u32 m0, s57, 0xfffff820\n"
    "ds_read_b64_tr_b16 v[192:193], v22\n"
    "ds_read_b64_tr_b16 v[194:195], v23\n"
    "ds_read_b64_tr_b16 v[196:197], v22 offset:32\n"
    "ds_read_b64_tr_b16 v[198:199], v23 offset:32\n"
    "ds_read_b64_tr_b16 v[200:201], v22 offset:64\n"
    "ds_read_b64_tr_b16 v[202:203], v23 offset:64\n"
    "ds_read_b64_tr_b16 v[204:205], v22 offset:96\n"
    "ds_read_b64_tr_b16 v[206:207], v23 offset:96\n"
    "v_exp_f32 v114, v114\n"
    "v_exp_f32 v115, v115\n"
    "v_exp_f32 v116, v116\n"
    "v_mfma_f32_32x32x16_bf16 v[96:111], v[64:67], v[88:91], v[160:175]\n"
    "buffer_load_dwordx4 v1, s[20:23], s43 offen offset:3072 sc1 lds\n"
    "v_exp_f32 v117, v117\n"
    "v_exp_f32 v118, v118\n"
    "v_exp_f32 v119, v119\n"
    "v_exp_f32 v120, v120\n"
    "v_exp_f32 v121, v121\n"
    "v_exp_f32 v122, v122\n"
    "v_smfmac_f32_32x32x32_bf16 v[144:159], v[48:51], v[112:119], v4\n"
    "ds_read_b128 v[48:51], v84 offset:2112\n"
    "v_exp_f32 v123, v123\n"
    "v_exp_f32 v124, v124\n"
    "v_exp_f32 v125, v125\n"
    "v_exp_f32 v126, v126\n"
    "v_exp_f32 v127, v127\n"
    "v_exp_f32 v96, v96\n"
    "v_exp_f32 v97, v97\n"
    "v_smfmac_f32_32x32x32_bf16 v[144:159], v[52:55], v[120:127], v4\n"
    "ds_read_b128 v[52:55], v85 offset:2112\n"
    "ds_write_b128 v26, v[208:211]\n"
    "ds_write_b128 v26, v[212:215] offset:1024\n"
    "ds_write_b128 v26, v[216:219] offset:2048\n"
    "ds_write_b128 v26, v[220:223] offset:3072\n"
    "v_exp_f32 v98, v98\n"
    "v_exp_f32 v99, v99\n"
    "v_exp_f32 v100, v100\n"
    "v_mfma_f32_32x32x16_bf16 v[112:127], v[64:67], v[92:95], v[176:191]\n"
    "s_waitcnt lgkmcnt(12)\n"
    "buffer_store_dwordx4 v[192:195], v29, s[20:23], s72 offen sc1\n"
    "s_add_u32 s72, s72, s73\n"
    "s_mov_b32 s24, s4\n"
    "s_and_b32 s25, s5, 0xffff\n"
    "s_mov_b32 s26, 0x200000\n"
    "s_mov_b32 s27, 0x20000\n"
    "buffer_load_dwordx2 v[8:9], v86, s[24:27], s66 offen\n"
    "s_waitcnt lgkmcnt(5)\n"
    "v_exp_f32 v101, v101\n"
    "v_exp_f32 v102, v102\n"
    "v_exp_f32 v103, v103\n"
    "v_exp_f32 v104, v104\n"
    "v_exp_f32 v105, v105\n"
    "v_exp_f32 v106, v106\n"
    "v_smfmac_f32_32x32x32_bf16 v[128:143], v[48:51], v[96:103], v4\n"
    "s_add_u32 s43, s43, 0x1000\n"
    "buffer_load_dwordx4 v[64:67], v1, s[20:23], s43 offen sc1\n"
    "v_exp_f32 v107, v107\n"
    "v_exp_f32 v108, v108\n"
    "v_exp_f32 v109, v109\n"
    "v_exp_f32 v110, v110\n"
    "v_exp_f32 v111, v111\n"
    "v_exp_f32 v112, v112\n"
    "v_exp_f32 v113, v113\n"
    "s_waitcnt vmcnt(11) lgkmcnt(0)\n"
    "v_smfmac_f32_32x32x32_bf16 v[128:143], v[52:55], v[104:111], v4\n"
    "s_add_u32 m0, s57, 0x440\n"
    "ds_read_b64_tr_b16 v[208:209], v24\n"
    "ds_read_b64_tr_b16 v[210:211], v25\n"
    "ds_read_b64_tr_b16 v[212:213], v24 offset:32\n"
    "ds_read_b64_tr_b16 v[214:215], v25 offset:32\n"
    "ds_read_b64_tr_b16 v[216:217], v24 offset:64\n"
    "ds_read_b64_tr_b16 v[218:219], v25 offset:64\n"
    "ds_read_b64_tr_b16 v[220:221], v24 offset:96\n"
    "ds_read_b64_tr_b16 v[222:223], v25 offset:96\n"
    "v_exp_f32 v114, v114\n"
    "v_exp_f32 v115, v115\n"
    "v_exp_f32 v116, v116\n"
    "v_mfma_f32_32x32x16_bf16 v[96:111], v[68:71], v[88:91], v[160:175]\n"
    "buffer_load_dwordx4 v1, s[20:23], s43 offen offset:1024 sc1 lds\n"
    "v_exp_f32 v117, v117\n"
    "v_exp_f32 v118, v118\n"
    "v_exp_f32 v119, v119\n"
    "v_exp_f32 v120, v120\n"
    "v_exp_f32 v121, v121\n"
    "v_exp_f32 v122, v122\n"
    "v_smfmac_f32_32x32x32_bf16 v[144:159], v[48:51], v[112:119], v4\n"
    "ds_read_b128 v[48:51], v84 offset:3168\n"
    "v_exp_f32 v123, v123\n"
    "v_exp_f32 v124, v124\n"
    "v_exp_f32 v125, v125\n"
    "v_exp_f32 v126, v126\n"
    "v_exp_f32 v127, v127\n"
    "v_exp_f32 v96, v96\n"
    "v_exp_f32 v97, v97\n"
    "v_smfmac_f32_32x32x32_bf16 v[144:159], v[52:55], v[120:127], v4\n"
    "ds_read_b128 v[52:55], v85 offset:3168\n"
    "s_waitcnt lgkmcnt(8)\n"
    "buffer_store_dwordx4 v[208:211], v28, s[20:23], s76 offen sc1\n"
    "v_exp_f32 v98, v98\n"
    "v_exp_f32 v99, v99\n"
    "v_exp_f32 v100, v100\n"
    "v_mfma_f32_32x32x16_bf16 v[112:127], v[68:71], v[92:95], v[176:191]\n"
    "buffer_store_dwordx4 v[196:199], v29, s[20:23], s72 offen sc1\n"
    "s_add_u32 s72, s72, s73\n"
    "s_add_u32 s54, s66, 131072\n"
    "buffer_load_dwordx2 v[10:11], v86, s[24:27], s54 offen\n"
    "s_waitcnt lgkmcnt(1)\n"
    "v_exp_f32 v101, v101\n"
    "v_exp_f32 v102, v102\n"
    "v_exp_f32 v103, v103\n"
    "v_exp_f32 v104, v104\n"
    "v_exp_f32 v105, v105\n"
    "v_exp_f32 v106, v106\n"
    "v_smfmac_f32_32x32x32_bf16 v[128:143], v[48:51], v[96:103], v4\n"
    "buffer_load_dwordx4 v[68:71], v1, s[20:23], s43 offen offset:2048 sc1\n"
    "v_exp_f32 v107, v107\n"
    "v_exp_f32 v108, v108\n"
    "v_exp_f32 v109, v109\n"
    "v_exp_f32 v110, v110\n"
    "v_exp_f32 v111, v111\n"
    "v_exp_f32 v112, v112\n"
    "v_exp_f32 v113, v113\n"
    "s_waitcnt vmcnt(14) lgkmcnt(0)\n"
    "v_smfmac_f32_32x32x32_bf16 v[128:143], v[52:55], v[104:111], v4\n"
    "s_add_u32 m0, s57, 0x60\n"
    "v_exp_f32 v114, v114\n"
    "v_exp_f32 v115, v115\n"
    "v_exp_f32 v116, v116\n"
    "v_mfma_f32_32x32x16_bf16 v[96:111], v[72:75], v[88:91], v[160:175]\n"
    "buffer_load_dwordx4 v1, s[20:23], s43 offen offset:3072 sc1 lds\n"
    "v_exp_f32 v117, v117\n"
    "v_exp_f32 v118, v118\n"
    "v_exp_f32 v119, v119\n"
    "v_exp_f32 v120, v120\n"
    "v_exp_f32 v121, v121\n"
    "v_exp_f32 v122, v122\n"
    "v_smfmac_f32_32x32x32_bf16 v[144:159], v[48:51], v[112:119], v4\n"
    "ds_read_b128 v[48:51], v84 offset:4224\n"
    "v_exp_f32 v123, v123\n"
    "v_exp_f32 v124, v124\n"
    "v_exp_f32 v125, v125\n"
    "v_exp_f32 v126, v126\n"
    "v_exp_f32 v127, v127\n"
    "v_exp_f32 v96, v96\n"
    "v_exp_f32 v97, v97\n"
    "v_smfmac_f32_32x32x32_bf16 v[144:159], v[52:55], v[120:127], v4\n"
    "ds_read_b128 v[52:55], v85 offset:4224\n"
    "buffer_store_dwordx4 v[212:215], v28, s[20:23], s76 offen offset:1024 sc1\n"
    "v_exp_f32 v98, v98\n"
    "v_exp_f32 v99, v99\n"
    "v_exp_f32 v100, v100\n"
    "v_mfma_f32_32x32x16_bf16 v[112:127], v[72:75], v[92:95], v[176:191]\n"
    "buffer_store_dwordx4 v[200:203], v27, s[20:23], s72 offen sc1\n"
    "s_add_u32 s72, s72, s73\n"
    "s_waitcnt lgkmcnt(1)\n"
    "v_exp_f32 v101, v101\n"
    "v_exp_f32 v102, v102\n"
    "v_exp_f32 v103, v103\n"
    "v_exp_f32 v104, v104\n"
    "v_exp_f32 v105, v105\n"
    "v_exp_f32 v106, v106\n"
    "v_smfmac_f32_32x32x32_bf16 v[128:143], v[48:51], v[96:103], v4\n"
    "s_add_u32 s43, s43, 0x1000\n"
    "buffer_load_dwordx4 v[72:75], v1, s[20:23], s43 offen sc1\n"
    "v_exp_f32 v107, v107\n"
    "v_exp_f32 v108, v108\n"
    "v_exp_f32 v109, v109\n"
    "v_exp_f32 v110, v110\n"
    "v_exp_f32 v111, v111\n"
    "v_exp_f32 v112, v112\n"
    "v_exp_f32 v113, v113\n"
    "s_waitcnt vmcnt(16) lgkmcnt(0)\n"
    "v_smfmac_f32_32x32x32_bf16 v[128:143], v[52:55], v[104:111], v4\n"
    "s_add_u32 m0, s57, 0xc80\n"
    "v_exp_f32 v114, v114\n"
    "v_exp_f32 v115, v115\n"
    "v_exp_f32 v116, v116\n"
    "v_mfma_f32_32x32x16_bf16 v[96:111], v[76:79], v[88:91], v[160:175]\n"
    "buffer_load_dwordx4 v1, s[20:23], s43 offen offset:1024 sc1 lds\n"
    "v_exp_f32 v117, v117\n"
    "v_exp_f32 v118, v118\n"
    "v_exp_f32 v119, v119\n"
    "v_exp_f32 v120, v120\n"
    "v_exp_f32 v121, v121\n"
    "v_exp_f32 v122, v122\n"
    "v_smfmac_f32_32x32x32_bf16 v[144:159], v[48:51], v[112:119], v4\n"
    "ds_read_b128 v[48:51], v84 offset:5280\n"
    "v_exp_f32 v123, v123\n"
    "v_exp_f32 v124, v124\n"
    "v_exp_f32 v125, v125\n"
    "v_exp_f32 v126, v126\n"
    "v_exp_f32 v127, v127\n"
    "v_exp_f32 v96, v96\n"
    "v_exp_f32 v97, v97\n"
    "v_smfmac_f32_32x32x32_bf16 v[144:159], v[52:55], v[120:127], v4\n"
    "ds_read_b128 v[52:55], v85 offset:5280\n"
    "buffer_store_dwordx4 v[216:219], v28, s[20:23], s76 offen offset:2048 sc1\n"
    "v_exp_f32 v98, v98\n"
    "v_exp_f32 v99, v99\n"
    "v_exp_f32 v100, v100\n"
    "v_mfma_f32_32x32x16_bf16 v[112:127], v[76:79], v[92:95], v[176:191]\n"
    "buffer_store_dwordx4 v[204:207], v27, s[20:23], s72 offen sc1\n"
    "s_add_u32 s72, s72, s73\n"
    "s_waitcnt lgkmcnt(1)\n"
    "v_exp_f32 v101, v101\n"
    "v_exp_f32 v102, v102\n"
    "v_exp_f32 v103, v103\n"
    "v_exp_f32 v104, v104\n"
    "v_exp_f32 v105, v105\n"
    "v_exp_f32 v106, v106\n"
    "v_smfmac_f32_32x32x32_bf16 v[128:143], v[48:51], v[96:103], v4\n"
    "buffer_load_dwordx4 v[76:79], v1, s[20:23], s43 offen offset:2048 sc1\n"
    "v_exp_f32 v107, v107\n"
    "v_exp_f32 v108, v108\n"
    "v_exp_f32 v109, v109\n"
    "v_exp_f32 v110, v110\n"
    "v_exp_f32 v111, v111\n"
    "v_exp_f32 v112, v112\n"
    "v_exp_f32 v113, v113\n"
    "s_waitcnt vmcnt(18) lgkmcnt(0)\n"
    "v_smfmac_f32_32x32x32_bf16 v[128:143], v[52:55], v[104:111], v4\n"
    "s_add_u32 m0, s57, 0x8a0\n"
    "v_exp_f32 v114, v114\n"
    "v_exp_f32 v115, v115\n"
    "v_exp_f32 v116, v116\n"
    "v_mfma_f32_32x32x16_bf16 v[96:111], v[56:59], v[88:91], v[160:175]\n"
    "buffer_load_dwordx4 v1, s[20:23], s43 offen offset:3072 sc1 lds\n"
    "v_exp_f32 v117, v117\n"
    "v_exp_f32 v118, v118\n"
    "v_exp_f32 v119, v119\n"
    "v_exp_f32 v120, v120\n"
    "v_exp_f32 v121, v121\n"
    "v_exp_f32 v122, v122\n"
    "v_smfmac_f32_32x32x32_bf16 v[144:159], v[48:51], v[112:119], v4\n"
    "ds_read_b128 v[48:51], v84\n"
    "v_exp_f32 v123, v123\n"
    "v_exp_f32 v124, v124\n"
    "v_exp_f32 v125, v125\n"
    "v_exp_f32 v126, v126\n"
    "v_exp_f32 v127, v127\n"
    "v_exp_f32 v96, v96\n"
    "v_exp_f32 v97, v97\n"
    "v_smfmac_f32_32x32x32_bf16 v[144:159], v[52:55], v[120:127], v4\n"
    "ds_read_b128 v[52:55], v85\n"
    "buffer_store_dwordx4 v[220:223], v28, s[20:23], s76 offen offset:3072 sc1\n"
    "v_exp_f32 v98, v98\n"
    "v_exp_f32 v99, v99\n"
    "v_exp_f32 v100, v100\n"
    "v_mfma_f32_32x32x16_bf16 v[112:127], v[56:59], v[92:95], v[176:191]\n"
    "s_waitcnt lgkmcnt(1)\n"
    "v_exp_f32 v101, v101\n"
    "v_exp_f32 v102, v102\n"
    "v_exp_f32 v103, v103\n"
    "v_exp_f32 v104, v104\n"
    "v_exp_f32 v105, v105\n"
    "v_exp_f32 v106, v106\n"
    "v_smfmac_f32_32x32x32_bf16 v[128:143], v[48:51], v[96:103], v4\n"
    "s_add_u32 s43, s43, 0x1000\n"
    "buffer_load_dwordx4 v[56:59], v1, s[20:23], s43 offen sc1\n"
    "v_exp_f32 v107, v107\n"
    "v_exp_f32 v108, v108\n"
    "v_exp_f32 v109, v109\n"
    "v_exp_f32 v110, v110\n"
    "v_exp_f32 v111, v111\n"
    "v_exp_f32 v112, v112\n"
    "v_exp_f32 v113, v113\n"
    "s_waitcnt vmcnt(19) lgkmcnt(0)\n"
    "v_smfmac_f32_32x32x32_bf16 v[128:143], v[52:55], v[104:111], v4\n"
    "s_add_u32 m0, s57, 0xfffffc00\n"
    "v_exp_f32 v114, v114\n"
    "v_exp_f32 v115, v115\n"
    "v_exp_f32 v116, v116\n"
    "v_mfma_f32_32x32x16_bf16 v[96:111], v[60:63], v[88:91], v[160:175]\n"
    "buffer_load_dwordx4 v1, s[20:23], s43 offen offset:1024 sc1 lds\n"
    "v_exp_f32 v117, v117\n"
    "v_exp_f32 v118, v118\n"
    "v_exp_f32 v119, v119\n"
    "v_exp_f32 v120, v120\n"
    "v_exp_f32 v121, v121\n"
    "v_exp_f32 v122, v122\n"
    "v_smfmac_f32_32x32x32_bf16 v[144:159], v[48:51], v[112:119], v4\n"
    "ds_read_b128 v[48:51], v84 offset:1056\n"
    "v_exp_f32 v123, v123\n"
    "v_exp_f32 v124, v124\n"
    "v_exp_f32 v125, v125\n"
    "v_exp_f32 v126, v126\n"
    "v_exp_f32 v127, v127\n"
    "v_exp_f32 v96, v96\n"
    "v_exp_f32 v97, v97\n"
    "v_smfmac_f32_32x32x32_bf16 v[144:159], v[52:55], v[120:127], v4\n"
    "ds_read_b128 v[52:55], v85 offset:1056\n"
    "v_exp_f32 v98, v98\n"
    "v_exp_f32 v99, v99\n"
    "v_exp_f32 v100, v100\n"
    "v_mfma_f32_32x32x16_bf16 v[112:127], v[60:63], v[92:95], v[176:191]\n"
    "s_waitcnt lgkmcnt(1)\n"
    "v_exp_f32 v101, v101\n"
    "v_exp_f32 v102, v102\n"
    "v_exp_f32 v103, v103\n"
    "v_exp_f32 v104, v104\n"
    "v_exp_f32 v105, v105\n"
    "v_exp_f32 v106, v106\n"
    "v_smfmac_f32_32x32x32_bf16 v[128:143], v[48:51], v[96:103], v4\n"
    "buffer_load_dwordx4 v[60:63], v1, s[20:23], s43 offen offset:2048 sc1\n"
    "v_exp_f32 v107, v107\n"
    "v_exp_f32 v108, v108\n"
    "v_exp_f32 v109, v109\n"
    "v_exp_f32 v110, v110\n"
    "v_exp_f32 v111, v111\n"
    "v_exp_f32 v112, v112\n"
    "v_exp_f32 v113, v113\n"
    "s_waitcnt vmcnt(17) lgkmcnt(0)\n"
    "v_smfmac_f32_32x32x32_bf16 v[128:143], v[52:55], v[104:111], v4\n"
    "s_add_u32 m0, s57, 0xfffff820\n"
    "v_exp_f32 v114, v114\n"
    "v_exp_f32 v115, v115\n"
    "v_exp_f32 v116, v116\n"
    "v_mfma_f32_32x32x16_bf16 v[96:111], v[64:67], v[88:91], v[160:175]\n"
    "buffer_load_dwordx4 v1, s[20:23], s43 offen offset:3072 sc1 lds\n"
    "v_exp_f32 v117, v117\n"
    "v_exp_f32 v118, v118\n"
    "v_exp_f32 v119, v119\n"
    "v_exp_f32 v120, v120\n"
    "v_exp_f32 v121, v121\n"
    "v_exp_f32 v122, v122\n"
    "v_smfmac_f32_32x32x32_bf16 v[144:159], v[48:51], v[112:119], v4\n"
    "ds_read_b128 v[48:51], v84 offset:2112\n"
    "v_exp_f32 v123, v123\n"
    "v_exp_f32 v124, v124\n"
    "v_exp_f32 v125, v125\n"
    "v_exp_f32 v126, v126\n"
    "v_exp_f32 v127, v127\n"
    "v_exp_f32 v96, v96\n"
    "v_exp_f32 v97, v97\n"
    "v_smfmac_f32_32x32x32_bf16 v[144:159], v[52:55], v[120:127], v4\n"
    "ds_read_b128 v[52:55], v85 offset:2112\n"
    "v_exp_f32 v98, v98\n"
    "v_exp_f32 v99, v99\n"
    "v_exp_f32 v100, v100\n"
    "v_mfma_f32_32x32x16_bf16 v[112:127], v[64:67], v[92:95], v[176:191]\n"
    "s_waitcnt lgkmcnt(1)\n"
    "v_exp_f32 v101, v101\n"
    "v_exp_f32 v102, v102\n"
    "v_exp_f32 v103, v103\n"
    "v_exp_f32 v104, v104\n"
    "v_exp_f32 v105, v105\n"
    "v_exp_f32 v106, v106\n"
    "v_smfmac_f32_32x32x32_bf16 v[128:143], v[48:51], v[96:103], v4\n"
    "s_add_u32 s43, s43, 0x1000\n"
    "buffer_load_dwordx4 v[64:67], v1, s[20:23], s43 offen sc1\n"
    "v_exp_f32 v107, v107\n"
    "v_exp_f32 v108, v108\n"
    "v_exp_f32 v109, v109\n"
    "v_exp_f32 v110, v110\n"
    "v_exp_f32 v111, v111\n"
    "v_exp_f32 v112, v112\n"
    "v_exp_f32 v113, v113\n"
    "s_waitcnt vmcnt(14) lgkmcnt(0)\n"
    "v_smfmac_f32_32x32x32_bf16 v[128:143], v[52:55], v[104:111], v4\n"
    "s_add_u32 m0, s57, 0x440\n"
    "v_exp_f32 v114, v114\n"
    "v_exp_f32 v115, v115\n"
    "v_exp_f32 v116, v116\n"
    "v_mfma_f32_32x32x16_bf16 v[96:111], v[68:71], v[88:91], v[160:175]\n"
    "buffer_load_dwordx4 v1, s[20:23], s43 offen offset:1024 sc1 lds\n"
    "v_exp_f32 v117, v117\n"
    "v_exp_f32 v118, v118\n"
    "v_exp_f32 v119, v119\n"
    "v_exp_f32 v120, v120\n"
    "v_exp_f32 v121, v121\n"
    "v_exp_f32 v122, v122\n"
    "v_smfmac_f32_32x32x32_bf16 v[144:159], v[48:51], v[112:119], v4\n"
    "ds_read_b128 v[48:51], v84 offset:3168\n"
    "v_exp_f32 v123, v123\n"
    "v_exp_f32 v124, v124\n"
    "v_exp_f32 v125, v125\n"
    "v_exp_f32 v126, v126\n"
    "v_exp_f32 v127, v127\n"
    "s_cmp_eq_u32 s46, 1\n"
    "s_cbranch_scc1 .Lk_jf1\n"
    "s_lshl_b32 s54, s2, 7\n"
    "s_add_u32 s54, s54, 32768\n"
    "s_waitcnt vmcnt(6)\n"
    "s_mov_b64 exec, 1\n"
    "v_mov_b32 v14, 160264\n"
    "v_mov_b32 v15, 1\n"
    "ds_add_rtn_u32 v15, v14, v15\n"
    "s_waitcnt lgkmcnt(0)\n"
    "v_readfirstlane_b32 s55, v15\n"
    "s_mov_b64 exec, -1\n"
    "s_cmp_eq_u32 s55, 3\n"
    "s_cbranch_scc0 .Lk_jf1\n"
    "s_mov_b64 exec, 1\n"
    "v_mov_b32 v14, s36\n"
    "v_mov_b32 v15, s37\n"
    "buffer_atomic_swap_x2 v[14:15], v1, s[20:23], s54 offen sc1\n"
    "s_mov_b64 exec, -1\n"
    ".Lk_jf1:\n"
    "v_exp_f32 v96, v96\n"
    "v_exp_f32 v97, v97\n"
    "v_smfmac_f32_32x32x32_bf16 v[144:159], v[52:55], v[120:127], v4\n"
    "ds_read_b128 v[52:55], v85 offset:3168\n"
    "v_exp_f32 v98, v98\n"
    "v_exp_f32 v99, v99\n"
    "v_exp_f32 v100, v100\n"
    "v_mfma_f32_32x32x16_bf16 v[112:127], v[68:71], v[92:95], v[176:191]\n"
    "s_waitcnt lgkmcnt(1)\n"
    "v_exp_f32 v101, v101\n"
    "v_exp_f32 v102, v102\n"
    "v_exp_f32 v103, v103\n"
    "v_exp_f32 v104, v104\n"
    "v_exp_f32 v105, v105\n"
    "v_exp_f32 v106, v106\n"
    "v_smfmac_f32_32x32x32_bf16 v[128:143], v[48:51], v[96:103], v4\n"
    "buffer_load_dwordx4 v[68:71], v1, s[20:23], s43 offen offset:2048 sc1\n"
    "v_exp_f32 v107, v107\n"
    "v_exp_f32 v108, v108\n"
    "v_exp_f32 v109, v109\n"
    "v_exp_f32 v110, v110\n"
    "v_exp_f32 v111, v111\n"
    "v_exp_f32 v112, v112\n"
    "v_exp_f32 v113, v113\n"
    "s_waitcnt vmcnt(12) lgkmcnt(0)\n"
    "v_smfmac_f32_32x32x32_bf16 v[128:143], v[52:55], v[104:111], v4\n"
    "s_add_u32 m0, s57, 0x60\n"
    "v_exp_f32 v114, v114\n"
    "v_exp_f32 v115, v115\n"
    "v_exp_f32 v116, v116\n"
    "v_mfma_f32_32x32x16_bf16 v[96:111], v[72:75], v[88:91], v[160:175]\n"
    "buffer_load_dwordx4 v1, s[20:23], s43 offen offset:3072 sc1 lds\n"
    "v_exp_f32 v117, v117\n"
    "v_exp_f32 v118, v118\n"
    "v_exp_f32 v119, v119\n"
    "v_exp_f32 v120, v120\n"
    "v_exp_f32 v121, v121\n"
    "v_exp_f32 v122, v122\n"
    "v_smfmac_f32_32x32x32_bf16 v[144:159], v[48:51], v[112:119], v4\n"
    "ds_read_b128 v[48:51], v84 offset:4224\n"
    "v_exp_f32 v123, v123\n"
    "v_exp_f32 v124, v124\n"
    "v_exp_f32 v125, v125\n"
    "v_exp_f32 v126, v126\n"
    "v_exp_f32 v127, v127\n"
    "v_exp_f32 v96, v96\n"
    "v_exp_f32 v97, v97\n"
    "v_smfmac_f32_32x32x32_bf16 v[144:159], v[52:55], v[120:127], v4\n"
    "ds_read_b128 v[52:55], v85 offset:4224\n"
    "v_exp_f32 v98, v98\n"
    "v_exp_f32 v99, v99\n"
    "v_exp_f32 v100, v100\n"
    "v_mfma_f32_32x32x16_bf16 v[112:127], v[72:75], v[92:95], v[176:191]\n"
    "s_waitcnt lgkmcnt(1)\n"
    "v_exp_f32 v101, v101\n"
    "v_exp_f32 v102, v102\n"
    "v_exp_f32 v103, v103\n"
    "v_exp_f32 v104, v104\n"
    "v_exp_f32 v105, v105\n"
    "v_exp_f32 v106, v106\n"
    "v_smfmac_f32_32x32x32_bf16 v[128:143], v[48:51], v[96:103], v4\n"
    "s_add_u32 s43, s43, 0x1000\n"
    "buffer_load_dwordx4 v[72:75], v1, s[20:23], s43 offen sc1\n"
    "v_exp_f32 v107, v107\n"
    "v_exp_f32 v108, v108\n"
    "v_exp_f32 v109, v109\n"
    "v_exp_f32 v110, v110\n"
    "v_exp_f32 v111, v111\n"
    "v_exp_f32 v112, v112\n"
    "v_exp_f32 v113, v113\n"
    "s_waitcnt vmcnt(10) lgkmcnt(0)\n"
    "v_smfmac_f32_32x32x32_bf16 v[128:143], v[52:55], v[104:111], v4\n"
    "s_add_u32 m0, s57, 0xc80\n"
    "v_exp_f32 v114, v114\n"
    "v_exp_f32 v115, v115\n"
    "v_exp_f32 v116, v116\n"
    "v_mfma_f32_32x32x16_bf16 v[96:111], v[76:79], v[88:91], v[160:175]\n"
    "buffer_load_dwordx4 v1, s[20:23], s43 offen offset:1024 sc1 lds\n"
    "v_exp_f32 v117, v117\n"
    "v_exp_f32 v118, v118\n"
    "v_exp_f32 v119, v119\n"
    "v_exp_f32 v120, v120\n"
    "v_exp_f32 v121, v121\n"
    "v_exp_f32 v122, v122\n"
    "v_smfmac_f32_32x32x32_bf16 v[144:159], v[48:51], v[112:119], v4\n"
    "ds_read_b128 v[48:51], v84 offset:5280\n"
    "v_exp_f32 v123, v123\n"
    "v_exp_f32 v124, v124\n"
    "v_exp_f32 v125, v125\n"
    "v_exp_f32 v126, v126\n"
    "v_exp_f32 v127, v127\n"
    "v_exp_f32 v96, v96\n"
    "v_exp_f32 v97, v97\n"
    "v_smfmac_f32_32x32x32_bf16 v[144:159], v[52:55], v[120:127], v4\n"
    "ds_read_b128 v[52:55], v85 offset:5280\n"
    "v_exp_f32 v98, v98\n"
    "v_exp_f32 v99, v99\n"
    "v_exp_f32 v100, v100\n"
    "v_mfma_f32_32x32x16_bf16 v[112:127], v[76:79], v[92:95], v[176:191]\n"
    "s_waitcnt lgkmcnt(1)\n"
    "v_exp_f32 v101, v101\n"
    "v_exp_f32 v102, v102\n"
    "v_exp_f32 v103, v103\n"
    "v_exp_f32 v104, v104\n"
    "v_exp_f32 v105, v105\n"
    "v_exp_f32 v106, v106\n"
    "v_smfmac_f32_32x32x32_bf16 v[128:143], v[48:51], v[96:103], v4\n"
    "buffer_load_dwordx4 v[76:79], v1, s[20:23], s43 offen offset:2048 sc1\n"
    "v_exp_f32 v107, v107\n"
    "v_exp_f32 v108, v108\n"
    "v_exp_f32 v109, v109\n"
    "v_exp_f32 v110, v110\n"
    "v_exp_f32 v111, v111\n"
    "v_exp_f32 v112, v112\n"
    "v_exp_f32 v113, v113\n"
    "s_waitcnt vmcnt(9) lgkmcnt(0)\n"
    "v_smfmac_f32_32x32x32_bf16 v[128:143], v[52:55], v[104:111], v4\n"
    "s_add_u32 m0, s57, 0x8a0\n"
    "v_exp_f32 v114, v114\n"
    "v_exp_f32 v115, v115\n"
    "v_exp_f32 v116, v116\n"
    "v_mfma_f32_32x32x16_bf16 v[96:111], v[56:59], v[88:91], v[160:175]\n"
    "buffer_load_dwordx4 v1, s[20:23], s43 offen offset:3072 sc1 lds\n"
    "v_exp_f32 v117, v117\n"
    "v_exp_f32 v118, v118\n"
    "v_exp_f32 v119, v119\n"
    "v_exp_f32 v120, v120\n"
    "v_exp_f32 v121, v121\n"
    "v_exp_f32 v122, v122\n"
    "v_smfmac_f32_32x32x32_bf16 v[144:159], v[48:51], v[112:119], v4\n"
    "ds_read_b128 v[48:51], v84\n"
    "v_exp_f32 v123, v123\n"
    "v_exp_f32 v124, v124\n"
    "v_exp_f32 v125, v125\n"
    "v_exp_f32 v126, v126\n"
    "v_exp_f32 v127, v127\n"
    "v_exp_f32 v96, v96\n"
    "v_exp_f32 v97, v97\n"
    "v_smfmac_f32_32x32x32_bf16 v[144:159], v[52:55], v[120:127], v4\n"
    "ds_read_b128 v[52:55], v85\n"
    "v_exp_f32 v98, v98\n"
    "v_exp_f32 v99, v99\n"
    "v_exp_f32 v100, v100\n"
    "v_mfma_f32_32x32x16_bf16 v[112:127], v[56:59], v[92:95], v[176:191]\n"
    "s_waitcnt lgkmcnt(1)\n"
    "v_exp_f32 v101, v101\n"
    "v_exp_f32 v102, v102\n"
    "v_exp_f32 v103, v103\n"
    "v_exp_f32 v104, v104\n"
    "v_exp_f32 v105, v105\n"
    "v_exp_f32 v106, v106\n"
    "v_smfmac_f32_32x32x32_bf16 v[128:143], v[48:51], v[96:103], v4\n"
    "s_add_u32 s43, s43, 0x1000\n"
    "buffer_load_dwordx4 v[56:59], v1, s[20:23], s43 offen sc1\n"
    "v_exp_f32 v107, v107\n"
    "v_exp_f32 v108, v108\n"
    "v_exp_f32 v109, v109\n"
    "v_exp_f32 v110, v110\n"
    "v_exp_f32 v111, v111\n"
    "v_exp_f32 v112, v112\n"
    "v_exp_f32 v113, v113\n"
    "s_waitcnt vmcnt(9) lgkmcnt(0)\n"
    "v_smfmac_f32_32x32x32_bf16 v[128:143], v[52:55], v[104:111], v4\n"
    "s_add_u32 m0, s57, 0xfffffc00\n"
    "v_exp_f32 v114, v114\n"
    "v_exp_f32 v115, v115\n"
    "v_exp_f32 v116, v116\n"
    "v_mfma_f32_32x32x16_bf16 v[96:111], v[60:63], v[88:91], v[160:175]\n"
    "buffer_load_dwordx4 v1, s[20:23], s43 offen offset:1024 sc1 lds\n"
    "v_exp_f32 v117, v117\n"
    "v_exp_f32 v118, v118\n"
    "v_exp_f32 v119, v119\n"
    "v_exp_f32 v120, v120\n"
    "v_exp_f32 v121, v121\n"
    "v_exp_f32 v122, v122\n"
    "v_smfmac_f32_32x32x32_bf16 v[144:159], v[48:51], v[112:119], v4\n"
    "ds_read_b128 v[48:51], v84 offset:1056\n"
    "v_exp_f32 v123, v123\n"
    "v_exp_f32 v124, v124\n"
    "v_exp_f32 v125, v125\n"
    "v_exp_f32 v126, v126\n"
    "v_exp_f32 v127, v127\n"
    "v_exp_f32 v96, v96\n"
    "v_exp_f32 v97, v97\n"
    "v_smfmac_f32_32x32x32_bf16 v[144:159], v[52:55], v[120:127], v4\n"
    "ds_read_b128 v[52:55], v85 offset:1056\n"
    "v_exp_f32 v98, v98\n"
    "v_exp_f32 v99, v99\n"
    "v_exp_f32 v100, v100\n"
    "v_mfma_f32_32x32x16_bf16 v[112:127], v[60:63], v[92:95], v[176:191]\n"
    "s_waitcnt lgkmcnt(1)\n"
    "v_exp_f32 v101, v101\n"
    "v_exp_f32 v102, v102\n"
    "v_exp_f32 v103, v103\n"
    "v_exp_f32 v104, v104\n"
    "v_exp_f32 v105, v105\n"
    "v_exp_f32 v106, v106\n"
    "v_smfmac_f32_32x32x32_bf16 v[128:143], v[48:51], v[96:103], v4\n"
    "buffer_load_dwordx4 v[60:63], v1, s[20:23], s43 offen offset:2048 sc1\n"
    "v_exp_f32 v107, v107\n"
    "v_exp_f32 v108, v108\n"
    "v_exp_f32 v109, v109\n"
    "v_exp_f32 v110, v110\n"
    "v_exp_f32 v111, v111\n"
    "v_exp_f32 v112, v112\n"
    "v_exp_f32 v113, v113\n"
    "s_waitcnt vmcnt(9) lgkmcnt(0)\n"
    "v_smfmac_f32_32x32x32_bf16 v[128:143], v[52:55], v[104:111], v4\n"
    "s_add_u32 m0, s57, 0xfffff820\n"
    "v_exp_f32 v114, v114\n"
    "v_exp_f32 v115, v115\n"
    "v_exp_f32 v116, v116\n"
    "v_mfma_f32_32x32x16_bf16 v[96:111], v[64:67], v[88:91], v[160:175]\n"
    "buffer_load_dwordx4 v1, s[20:23], s43 offen offset:3072 sc1 lds\n"
    "v_exp_f32 v117, v117\n"
    "v_exp_f32 v118, v118\n"
    "v_exp_f32 v119, v119\n"
    "v_exp_f32 v120, v120\n"
    "v_exp_f32 v121, v121\n"
    "v_exp_f32 v122, v122\n"
    "v_smfmac_f32_32x32x32_bf16 v[144:159], v[48:51], v[112:119], v4\n"
    "ds_read_b128 v[48:51], v84 offset:2112\n"
    "v_exp_f32 v123, v123\n"
    "v_exp_f32 v124, v124\n"
    "v_exp_f32 v125, v125\n"
    "v_exp_f32 v126, v126\n"
    "v_exp_f32 v127, v127\n"
    "v_exp_f32 v96, v96\n"
    "v_exp_f32 v97, v97\n"
    "v_smfmac_f32_32x32x32_bf16 v[144:159], v[52:55], v[120:127], v4\n"
    "ds_read_b128 v[52:55], v85 offset:2112\n"
    "v_exp_f32 v98, v98\n"
    "v_exp_f32 v99, v99\n"
    "v_exp_f32 v100, v100\n"
    "v_mfma_f32_32x32x16_bf16 v[112:127], v[64:67], v[92:95], v[176:191]\n"
    "s_waitcnt lgkmcnt(1)\n"
    "v_exp_f32 v101, v101\n"
    "v_exp_f32 v102, v102\n"
    "v_exp_f32 v103, v103\n"
    "v_exp_f32 v104, v104\n"
    "v_exp_f32 v105, v105\n"
    "v_exp_f32 v106, v106\n"
    "v_smfmac_f32_32x32x32_bf16 v[128:143], v[48:51], v[96:103], v4\n"
    "s_add_u32 s43, s43, 0x1000\n"
    "buffer_load_dwordx4 v[64:67], v1, s[20:23], s43 offen sc1\n"
    "v_exp_f32 v107, v107\n"
    "v_exp_f32 v108, v108\n"
    "v_exp_f32 v109, v109\n"
    "v_exp_f32 v110, v110\n"
    "v_exp_f32 v111, v111\n"
    "v_exp_f32 v112, v112\n"
    "v_exp_f32 v113, v113\n"
    "s_waitcnt vmcnt(9) lgkmcnt(0)\n"
    "v_smfmac_f32_32x32x32_bf16 v[128:143], v[52:55], v[104:111], v4\n"
    "s_add_u32 m0, s57, 0x440\n"
    "v_exp_f32 v114, v114\n"
    "v_exp_f32 v115, v115\n"
    "v_exp_f32 v116, v116\n"
    "v_mfma_f32_32x32x16_bf16 v[96:111], v[68:71], v[88:91], v[160:175]\n"
    "buffer_load_dwordx4 v1, s[20:23], s43 offen offset:1024 sc1 lds\n"
    "v_exp_f32 v117, v117\n"
    "v_exp_f32 v118, v118\n"
    "v_exp_f32 v119, v119\n"
    "v_exp_f32 v120, v120\n"
    "v_exp_f32 v121, v121\n"
    "v_exp_f32 v122, v122\n"
    "v_smfmac_f32_32x32x32_bf16 v[144:159], v[48:51], v[112:119], v4\n"
    "ds_read_b128 v[48:51], v84 offset:3168\n"
    "v_exp_f32 v123, v123\n"
    "v_exp_f32 v124, v124\n"
    "v_exp_f32 v125, v125\n"
    "v_exp_f32 v126, v126\n"
    "v_exp_f32 v127, v127\n"
    "v_exp_f32 v96, v96\n"
    "v_exp_f32 v97, v97\n"
    "v_smfmac_f32_32x32x32_bf16 v[144:159], v[52:55], v[120:127], v4\n"
    "ds_read_b128 v[52:55], v85 offset:3168\n"
    "v_exp_f32 v98, v98\n"
    "v_exp_f32 v99, v99\n"
    "v_exp_f32 v100, v100\n"
    "v_mfma_f32_32x32x16_bf16 v[112:127], v[68:71], v[92:95], v[176:191]\n"
    "s_waitcnt lgkmcnt(1)\n"
    "v_exp_f32 v101, v101\n"
    "v_exp_f32 v102, v102\n"
    "v_exp_f32 v103, v103\n"
    "v_exp_f32 v104, v104\n"
    "v_exp_f32 v105, v105\n"
    "v_exp_f32 v106, v106\n"
    "v_smfmac_f32_32x32x32_bf16 v[128:143], v[48:51], v[96:103], v4\n"
    "buffer_load_dwordx4 v[68:71], v1, s[20:23], s43 offen offset:2048 sc1\n"
    "v_exp_f32 v107, v107\n"
    "v_exp_f32 v108, v108\n"
    "v_exp_f32 v109, v109\n"
    "v_exp_f32 v110, v110\n"
    "v_exp_f32 v111, v111\n"
    "v_exp_f32 v112, v112\n"
    "v_exp_f32 v113, v113\n"
    "s_waitcnt vmcnt(9) lgkmcnt(0)\n"
    "v_smfmac_f32_32x32x32_bf16 v[128:143], v[52:55], v[104:111], v4\n"
    "s_add_u32 m0, s57, 0x60\n"
    "v_exp_f32 v114, v114\n"
    "v_exp_f32 v115, v115\n"
    "v_exp_f32 v116, v116\n"
    "v_mfma_f32_32x32x16_bf16 v[96:111], v[72:75], v[88:91], v[160:175]\n"
    "buffer_load_dwordx4 v1, s[20:23], s43 offen offset:3072 sc1 lds\n"
    "v_exp_f32 v117, v117\n"
    "v_exp_f32 v118, v118\n"
    "v_exp_f32 v119, v119\n"
    "v_exp_f32 v120, v120\n"
    "v_exp_f32 v121, v121\n"
    "v_exp_f32 v122, v122\n"
    "v_smfmac_f32_32x32x32_bf16 v[144:159], v[48:51], v[112:119], v4\n"
    "ds_read_b128 v[48:51], v84 offset:4224\n"
    "v_exp_f32 v123, v123\n"
    "v_exp_f32 v124, v124\n"
    "v_exp_f32 v125, v125\n"
    "v_exp_f32 v126, v126\n"
    "v_exp_f32 v127, v127\n"
    "v_exp_f32 v96, v96\n"
    "v_exp_f32 v97, v97\n"
    "v_smfmac_f32_32x32x32_bf16 v[144:159], v[52:55], v[120:127], v4\n"
    "ds_read_b128 v[52:55], v85 offset:4224\n"
    "v_exp_f32 v98, v98\n"
    "v_exp_f32 v99, v99\n"
    "v_exp_f32 v100, v100\n"
    "v_mfma_f32_32x32x16_bf16 v[112:127], v[72:75], v[92:95], v[176:191]\n"
    "s_waitcnt lgkmcnt(1)\n"
    "v_exp_f32 v101, v101\n"
    "v_exp_f32 v102, v102\n"
    "v_exp_f32 v103, v103\n"
    "v_exp_f32 v104, v104\n"
    "v_exp_f32 v105, v105\n"
    "v_exp_f32 v106, v106\n"
    "v_smfmac_f32_32x32x32_bf16 v[128:143], v[48:51], v[96:103], v4\n"
    "s_add_u32 s43, s43, 0x1000\n"
    "buffer_load_dwordx4 v[72:75], v1, s[20:23], s43 offen sc1\n"
    "v_exp_f32 v107, v107\n"
    "v_exp_f32 v108, v108\n"
    "v_exp_f32 v109, v109\n"
    "v_exp_f32 v110, v110\n"
    "v_exp_f32 v111, v111\n"
    "v_exp_f32 v112, v112\n"
    "v_exp_f32 v113, v113\n"
    "s_waitcnt vmcnt(9) lgkmcnt(0)\n"
    "v_smfmac_f32_32x32x32_bf16 v[128:143], v[52:55], v[104:111], v4\n"
    "s_add_u32 m0, s57, 0xc80\n"
    "v_exp_f32 v114, v114\n"
    "v_exp_f32 v115, v115\n"
    "v_exp_f32 v116, v116\n"
    "v_mfma_f32_32x32x16_bf16 v[96:111], v[76:79], v[88:91], v[160:175]\n"
    "buffer_load_dwordx4 v1, s[20:23], s43 offen offset:1024 sc1 lds\n"
    "v_exp_f32 v117, v117\n"
    "v_exp_f32 v118, v118\n"
    "v_exp_f32 v119, v119\n"
    "v_exp_f32 v120, v120\n"
    "v_exp_f32 v121, v121\n"
    "v_exp_f32 v122, v122\n"
    "v_smfmac_f32_32x32x32_bf16 v[144:159], v[48:51], v[112:119], v4\n"
    "ds_read_b128 v[48:51], v84 offset:5280\n"
    "v_exp_f32 v123, v123\n"
    "v_exp_f32 v124, v124\n"
    "v_exp_f32 v125, v125\n"
    "v_exp_f32 v126, v126\n"
    "v_exp_f32 v127, v127\n"
    "v_exp_f32 v96, v96\n"
    "v_exp_f32 v97, v97\n"
    "v_smfmac_f32_32x32x32_bf16 v[144:159], v[52:55], v[120:127], v4\n"
    "ds_read_b128 v[52:55], v85 offset:5280\n"
    "v_exp_f32 v98, v98\n"
    "v_exp_f32 v99, v99\n"
    "v_exp_f32 v100, v100\n"
    "v_mfma_f32_32x32x16_bf16 v[112:127], v[76:79], v[92:95], v[176:191]\n"
    "buffer_load_dwordx2 v[40:41], v42, s[20:23], s47 offen sc1\n"
    "s_waitcnt lgkmcnt(1)\n"
    "v_exp_f32 v101, v101\n"
    "v_exp_f32 v102, v102\n"
    "v_exp_f32 v103, v103\n"
    "v_exp_f32 v104, v104\n"
    "v_exp_f32 v105, v105\n"
    "v_exp_f32 v106, v106\n"
    "v_smfmac_f32_32x32x32_bf16 v[128:143], v[48:51], v[96:103], v4\n"
    "buffer_load_dwordx4 v[76:79], v1, s[20:23], s43 offen offset:2048 sc1\n"
    "v_exp_f32 v107, v107\n"
    "v_exp_f32 v108, v108\n"
    "v_exp_f32 v109, v109\n"
    "v_exp_f32 v110, v110\n"
    "v_exp_f32 v111, v111\n"
    "v_exp_f32 v112, v112\n"
    "v_exp_f32 v113, v113\n"
    "s_waitcnt vmcnt(10) lgkmcnt(0)\n"
    "v_smfmac_f32_32x32x32_bf16 v[128:143], v[52:55], v[104:111], v4\n"
    "s_add_u32 m0, s57, 0x8a0\n"
    "v_exp_f32 v114, v114\n"
    "v_exp_f32 v115, v115\n"
    "v_exp_f32 v116, v116\n"
    "v_mfma_f32_32x32x16_bf16 v[96:111], v[56:59], v[88:91], v[160:175]\n"
    "buffer_load_dwordx4 v1, s[20:23], s43 offen offset:3072 sc1 lds\n"
    "v_exp_f32 v117, v117\n"
    "v_exp_f32 v118, v118\n"
    "v_exp_f32 v119, v119\n"
    "v_exp_f32 v120, v120\n"
    "v_exp_f32 v121, v121\n"
    "v_exp_f32 v122, v122\n"
    "v_smfmac_f32_32x32x32_bf16 v[144:159], v[48:51], v[112:119], v4\n"
    "ds_read_b128 v[48:51], v84\n"
    "v_exp_f32 v123, v123\n"
    "v_exp_f32 v124, v124\n"
    "v_exp_f32 v125, v125\n"
    "v_exp_f32 v126, v126\n"
    "v_exp_f32 v127, v127\n"
    "v_exp_f32 v96, v96\n"
    "v_exp_f32 v97, v97\n"
    "v_smfmac_f32_32x32x32_bf16 v[144:159], v[52:55], v[120:127], v4\n"
    "ds_read_b128 v[52:55], v85\n"
    "v_exp_f32 v98, v98\n"
    "v_exp_f32 v99, v99\n"
    "v_exp_f32 v100, v100\n"
    "v_mfma_f32_32x32x16_bf16 v[112:127], v[56:59], v[92:95], v[176:191]\n"
    "s_waitcnt lgkmcnt(1)\n"
    "v_exp_f32 v101, v101\n"
    "v_exp_f32 v102, v102\n"
    "v_exp_f32 v103, v103\n"
    "v_exp_f32 v104, v104\n"
    "v_exp_f32 v105, v105\n"
    "v_exp_f32 v106, v106\n"
    "v_smfmac_f32_32x32x32_bf16 v[128:143], v[48:51], v[96:103], v4\n"
    "s_add_u32 s43, s43, 0x1000\n"
    "buffer_load_dwordx4 v[56:59], v1, s[20:23], s43 offen sc1\n"
    "v_exp_f32 v107, v107\n"
    "v_exp_f32 v108, v108\n"
    "v_exp_f32 v109, v109\n"
    "v_exp_f32 v110, v110\n"
    "v_exp_f32 v111, v111\n"
    "v_exp_f32 v112, v112\n"
    "v_exp_f32 v113, v113\n"
    "s_waitcnt vmcnt(10) lgkmcnt(0)\n"
    "v_smfmac_f32_32x32x32_bf16 v[128:143], v[52:55], v[104:111], v4\n"
    "s_add_u32 m0, s57, 0xfffffc00\n"
    "v_exp_f32 v114, v114\n"
    "v_exp_f32 v115, v115\n"
    "v_exp_f32 v116, v116\n"
    "v_mfma_f32_32x32x16_bf16 v[96:111], v[60:63], v[88:91], v[160:175]\n"
    "buffer_load_dwordx4 v1, s[20:23], s43 offen offset:1024 sc1 lds\n"
    "v_exp_f32 v117, v117\n"
    "v_exp_f32 v118, v118\n"
    "v_exp_f32 v119, v119\n"
    "v_exp_f32 v120, v120\n"
    "v_exp_f32 v121, v121\n"
    "v_exp_f32 v122, v122\n"
    "v_smfmac_f32_32x32x32_bf16 v[144:159], v[48:51], v[112:119], v4\n"
    "ds_read_b128 v[48:51], v84 offset:1056\n"
    "v_exp_f32 v123, v123\n"
    "v_exp_f32 v124, v124\n"
    "v_exp_f32 v125, v125\n"
    "v_exp_f32 v126, v126\n"
    "v_exp_f32 v127, v127\n"
    "v_exp_f32 v96, v96\n"
    "v_exp_f32 v97, v97\n"
    "v_smfmac_f32_32x32x32_bf16 v[144:159], v[52:55], v[120:127], v4\n"
    "ds_read_b128 v[52:55], v85 offset:1056\n"
    "v_exp_f32 v98, v98\n"
    "v_exp_f32 v99, v99\n"
    "v_exp_f32 v100, v100\n"
    "v_mfma_f32_32x32x16_bf16 v[112:127], v[60:63], v[92:95], v[176:191]\n"
    "s_waitcnt lgkmcnt(1)\n"
    "v_exp_f32 v101, v101\n"
    "v_exp_f32 v102, v102\n"
    "v_exp_f32 v103, v103\n"
    "v_exp_f32 v104, v104\n"
    "v_exp_f32 v105, v105\n"
    "v_exp_f32 v106, v106\n"
    "v_smfmac_f32_32x32x32_bf16 v[128:143], v[48:51], v[96:103], v4\n"
    "buffer_load_dwordx4 v[60:63], v1, s[20:23], s43 offen offset:2048 sc1\n"
    "v_exp_f32 v107, v107\n"
    "v_exp_f32 v108, v108\n"
    "v_exp_f32 v109, v109\n"
    "v_exp_f32 v110, v110\n"
    "v_exp_f32 v111, v111\n"
    "v_exp_f32 v112, v112\n"
    "v_exp_f32 v113, v113\n"
    "s_waitcnt vmcnt(10) lgkmcnt(0)\n"
    "v_smfmac_f32_32x32x32_bf16 v[128:143], v[52:55], v[104:111], v4\n"
    "s_add_u32 m0, s57, 0xfffff820\n"
    "v_exp_f32 v114, v114\n"
    "v_exp_f32 v115, v115\n"
    "v_exp_f32 v116, v116\n"
    "v_mfma_f32_32x32x16_bf16 v[96:111], v[64:67], v[88:91], v[160:175]\n"
    "buffer_load_dwordx4 v1, s[20:23], s43 offen offset:3072 sc1 lds\n"
    "v_exp_f32 v117, v117\n"
    "v_exp_f32 v118, v118\n"
    "v_exp_f32 v119, v119\n"
    "v_exp_f32 v120, v120\n"
    "v_exp_f32 v121, v121\n"
    "v_exp_f32 v122, v122\n"
    "v_smfmac_f32_32x32x32_bf16 v[144:159], v[48:51], v[112:119], v4\n"
    "ds_read_b128 v[48:51], v84 offset:2112\n"
    "v_exp_f32 v123, v123\n"
    "v_exp_f32 v124, v124\n"
    "v_exp_f32 v125, v125\n"
    "v_exp_f32 v126, v126\n"
    "v_exp_f32 v127, v127\n"
    "s_mov_b32 s55, 0x2000\n"
    ".Lk_frl:\n"
    "s_waitcnt vmcnt(6)\n"
    "v_cmp_ne_u64 vcc, s[36:37], v[40:41]\n"
    "s_cbranch_vccz .Lk_frd\n"
    "s_sub_u32 s55, s55, 1\n"
    "s_cbranch_scc1 .Lk_frd\n"
    "s_sleep 4\n"
    "buffer_load_dwordx2 v[40:41], v42, s[20:23], s47 offen sc1\n"
    "s_waitcnt vmcnt(0)\n"
    "s_branch .Lk_frl\n"
    ".Lk_frd:\n"
    "s_mov_b64 exec, 1\n"
    "v_mov_b32 v40, 1\n"
    "ds_add_u32 v43, v40 offset:4\n"
    "s_mov_b64 exec, -1\n"
    "v_exp_f32 v96, v96\n"
    "v_exp_f32 v97, v97\n"
    "v_smfmac_f32_32x32x32_bf16 v[144:159], v[52:55], v[120:127], v4\n"
    "ds_read_b128 v[52:55], v85 offset:2112\n"
    "v_exp_f32 v98, v98\n"
    "v_exp_f32 v99, v99\n"
    "v_exp_f32 v100, v100\n"
    "v_mfma_f32_32x32x16_bf16 v[112:127], v[64:67], v[92:95], v[176:191]\n"
    "s_waitcnt lgkmcnt(2)\n"
    "v_exp_f32 v101, v101\n"
    "v_exp_f32 v102, v102\n"
    "v_exp_f32 v103, v103\n"
    "v_exp_f32 v104, v104\n"
    "v_exp_f32 v105, v105\n"
    "v_exp_f32 v106, v106\n"
    "v_smfmac_f32_32x32x32_bf16 v[128:143], v[48:51], v[96:103], v4\n"
    "s_add_u32 s43, s43, 0x1000\n"
    "buffer_load_dwordx4 v[64:67], v1, s[20:23], s43 offen sc1\n"
    "v_exp_f32 v107, v107\n"
    "v_exp_f32 v108, v108\n"
    "v_exp_f32 v109, v109\n"
    "v_exp_f32 v110, v110\n"
    "v_exp_f32 v111, v111\n"
    "v_exp_f32 v112, v112\n"
    "v_exp_f32 v113, v113\n"
    "s_waitcnt lgkmcnt(0)\n"
    "v_smfmac_f32_32x32x32_bf16 v[128:143], v[52:55], v[104:111], v4\n"
    "s_add_u32 m0, s57, 0x440\n"
    "ds_read_b32 v41, v43 offset:4\n"
    "v_exp_f32 v114, v114\n"
    "v_exp_f32 v115, v115\n"
    "v_exp_f32 v116, v116\n"
    "v_mfma_f32_32x32x16_bf16 v[96:111], v[68:71], v[88:91], v[160:175]\n"
    "buffer_load_dwordx4 v1, s[20:23], s43 offen offset:1024 sc1 lds\n"
    "v_exp_f32 v117, v117\n"
    "v_exp_f32 v118, v118\n"
    "v_exp_f32 v119, v119\n"
    "v_exp_f32 v120, v120\n"
    "v_exp_f32 v121, v121\n"
    "v_exp_f32 v122, v122\n"
    "v_smfmac_f32_32x32x32_bf16 v[144:159], v[48:51], v[112:119], v4\n"
    "ds_read_b128 v[48:51], v84 offset:3168\n"
    "v_exp_f32 v123, v123\n"
    "v_exp_f32 v124, v124\n"
    "v_exp_f32 v125, v125\n"
    "v_exp_f32 v126, v126\n"
    "v_exp_f32 v127, v127\n"
    "v_exp_f32 v96, v96\n"
    "v_exp_f32 v97, v97\n"
    "v_smfmac_f32_32x32x32_bf16 v[144:159], v[52:55], v[120:127], v4\n"
    "ds_read_b128 v[52:55], v85 offset:3168\n"
    "s_mov_b32 s55, 0x40000\n"
    ".Lk_acl1:\n"
    "s_waitcnt lgkmcnt(2)\n"
    "v_readfirstlane_b32 s50, v41\n"
    "s_cmp_ge_u32 s50, 4\n"
    "s_cbranch_scc1 .Lk_acd1\n"
    "s_sub_u32 s55, s55, 1\n"
    "s_cbranch_scc1 .Lk_acd1\n"
    "s_sleep 2\n"
    "ds_read_b32 v41, v43 offset:4\n"
    "s_waitcnt lgkmcnt(0)\n"
    "s_branch .Lk_acl1\n"
    ".Lk_acd1:\n"
    "s_add_u32 m0, s59, 0\n"
    "s_add_u32 s45, s68, 0\n"
    "buffer_load_dwordx4 v1, s[20:23], s45 offen sc1 lds\n"
    "buffer_load_dwordx4 v1, s[20:23], s45 offen offset:1024 sc1 lds\n"
    "v_exp_f32 v98, v98\n"
    "v_exp_f32 v99, v99\n"
    "v_exp_f32 v100, v100\n"
    "v_mfma_f32_32x32x16_bf16 v[112:127], v[68:71], v[92:95], v[176:191]\n"
    "s_waitcnt lgkmcnt(1)\n"
    "v_exp_f32 v101, v101\n"
    "v_exp_f32 v102, v102\n"
    "v_exp_f32 v103, v103\n"
    "v_exp_f32 v104, v104\n"
    "v_exp_f32 v105, v105\n"
    "v_exp_f32 v106, v106\n"
    "v_smfmac_f32_32x32x32_bf16 v[128:143], v[48:51], v[96:103], v4\n"
    "buffer_load_dwordx4 v[68:71], v1, s[20:23], s43 offen offset:2048 sc1\n"
    "v_exp_f32 v107, v107\n"
    "v_exp_f32 v108, v108\n"
    "v_exp_f32 v109, v109\n"
    "v_exp_f32 v110, v110\n"
    "v_exp_f32 v111, v111\n"
    "v_exp_f32 v112, v112\n"
    "v_exp_f32 v113, v113\n"
    "s_waitcnt lgkmcnt(0)\n"
    "v_smfmac_f32_32x32x32_bf16 v[128:143], v[52:55], v[104:111], v4\n"
    "s_add_u32 m0, s57, 0x60\n"
    "buffer_load_dwordx4 v[192:195], v1, s[20:23], s44 offen sc1\n"
    "buffer_load_dwordx4 v[196:199], v1, s[20:23], s44 offen offset:1024 sc1\n"
    "v_exp_f32 v114, v114\n"
    "v_exp_f32 v115, v115\n"
    "v_exp_f32 v116, v116\n"
    "v_mfma_f32_32x32x16_bf16 v[96:111], v[72:75], v[88:91], v[160:175]\n"
    "buffer_load_dwordx4 v1, s[20:23], s43 offen offset:3072 sc1 lds\n"
    "v_exp_f32 v117, v117\n"
    "v_exp_f32 v118, v118\n"
    "v_exp_f32 v119, v119\n"
    "v_exp_f32 v120, v120\n"
    "v_exp_f32 v121, v121\n"
    "v_exp_f32 v122, v122\n"
    "v_smfmac_f32_32x32x32_bf16 v[144:159], v[48:51], v[112:119], v4\n"
    "ds_read_b128 v[48:51], v84 offset:4224\n"
    "v_exp_f32 v123, v123\n"
    "v_exp_f32 v124, v124\n"
    "v_exp_f32 v125, v125\n"
    "v_exp_f32 v126, v126\n"
    "v_exp_f32 v127, v127\n"
    "v_exp_f32 v96, v96\n"
    "v_exp_f32 v97, v97\n"
    "v_smfmac_f32_32x32x32_bf16 v[144:159], v[52:55], v[120:127], v4\n"
    "ds_read_b128 v[52:55], v85 offset:4224\n"
    "v_exp_f32 v98, v98\n"
    "v_exp_f32 v99, v99\n"
    "v_exp_f32 v100, v100\n"
    "v_mfma_f32_32x32x16_bf16 v[112:127], v[72:75], v[92:95], v[176:191]\n"
    "s_add_u32 m0, s59, 0\n"
    "s_add_u32 s45, s68, 0\n"
    "buffer_load_dwordx4 v1, s[20:23], s45 offen offset:2048 sc1 lds\n"
    "buffer_load_dwordx4 v1, s[20:23], s45 offen offset:3072 sc1 lds\n"
    "s_waitcnt lgkmcnt(1)\n"
    "v_exp_f32 v101, v101\n"
    "v_exp_f32 v102, v102\n"
    "v_exp_f32 v103, v103\n"
    "v_exp_f32 v104, v104\n"
    "v_exp_f32 v105, v105\n"
    "v_exp_f32 v106, v106\n"
    "v_smfmac_f32_32x32x32_bf16 v[128:143], v[48:51], v[96:103], v4\n"
    "s_add_u32 s43, s43, 0x1000\n"
    "buffer_load_dwordx4 v[72:75], v1, s[20:23], s43 offen sc1\n"
    "v_exp_f32 v107, v107\n"
    "v_exp_f32 v108, v108\n"
    "v_exp_f32 v109, v109\n"
    "v_exp_f32 v110, v110\n"
    "v_exp_f32 v111, v111\n"
    "v_exp_f32 v112, v112\n"
    "v_exp_f32 v113, v113\n"
    "s_waitcnt vmcnt(15) lgkmcnt(0)\n"
    "v_smfmac_f32_32x32x32_bf16 v[128:143], v[52:55], v[104:111], v4\n"
    "s_add_u32 m0, s57, 0xc80\n"
    "buffer_load_dwordx4 v[200:203], v1, s[20:23], s44 offen offset:2048 sc1\n"
    "buffer_load_dwordx4 v[204:207], v1, s[20:23], s44 offen offset:3072 sc1\n"
    "v_exp_f32 v114, v114\n"
    "v_exp_f32 v115, v115\n"
    "v_exp_f32 v116, v116\n"
    "v_mfma_f32_32x32x16_bf16 v[96:111], v[76:79], v[88:91], v[160:175]\n"
    "buffer_load_dwordx4 v1, s[20:23], s43 offen offset:1024 sc1 lds\n"
    "v_exp_f32 v117, v117\n"
    "v_exp_f32 v118, v118\n"
    "v_exp_f32 v119, v119\n"
    "v_exp_f32 v120, v120\n"
    "v_exp_f32 v121, v121\n"
    "v_exp_f32 v122, v122\n"
    "v_smfmac_f32_32x32x32_bf16 v[144:159], v[48:51], v[112:119], v4\n"
    "ds_read_b128 v[48:51], v84 offset:5280\n"
    "v_exp_f32 v123, v123\n"
    "v_exp_f32 v124, v124\n"
    "v_exp_f32 v125, v125\n"
    "v_exp_f32 v126, v126\n"
    "v_exp_f32 v127, v127\n"
    "v_exp_f32 v96, v96\n"
    "v_exp_f32 v97, v97\n"
    "v_smfmac_f32_32x32x32_bf16 v[144:159], v[52:55], v[120:127], v4\n"
    "ds_read_b128 v[52:55], v85 offset:5280\n"
    "s_add_u32 m0, s59, 4096\n"
    "s_add_u32 s45, s68, 4096\n"
    "buffer_load_dwordx4 v1, s[20:23], s45 offen sc1 lds\n"
    "buffer_load_dwordx4 v1, s[20:23], s45 offen offset:1024 sc1 lds\n"
    "v_exp_f32 v98, v98\n"
    "v_exp_f32 v99, v99\n"
    "v_exp_f32 v100, v100\n"
    "v_mfma_f32_32x32x16_bf16 v[112:127], v[76:79], v[92:95], v[176:191]\n"
    "s_waitcnt lgkmcnt(1)\n"
    "v_exp_f32 v101, v101\n"
    "v_exp_f32 v102, v102\n"
    "v_exp_f32 v103, v103\n"
    "v_exp_f32 v104, v104\n"
    "v_exp_f32 v105, v105\n"
    "v_exp_f32 v106, v106\n"
    "v_smfmac_f32_32x32x32_bf16 v[128:143], v[48:51], v[96:103], v4\n"
    "buffer_load_dwordx4 v[76:79], v1, s[20:23], s43 offen offset:2048 sc1\n"
    "v_exp_f32 v107, v107\n"
    "v_exp_f32 v108, v108\n"
    "v_exp_f32 v109, v109\n"
    "v_exp_f32 v110, v110\n"
    "v_exp_f32 v111, v111\n"
    "v_exp_f32 v112, v112\n"
    "v_exp_f32 v113, v113\n"
    "s_waitcnt vmcnt(19) lgkmcnt(0)\n"
    "v_smfmac_f32_32x32x32_bf16 v[128:143], v[52:55], v[104:111], v4\n"
    "s_add_u32 m0, s57, 0x8a0\n"
    "s_add_u32 s44, s44, 0x1000\n"
    "buffer_load_dwordx4 v[208:211], v1, s[20:23], s44 offen sc1\n"
    "buffer_load_dwordx4 v[212:215], v1, s[20:23], s44 offen offset:1024 sc1\n"
    "v_exp_f32 v114, v114\n"
    "v_exp_f32 v115, v115\n"
    "v_exp_f32 v116, v116\n"
    "v_mfma_f32_32x32x16_bf16 v[96:111], v[56:59], v[88:91], v[160:175]\n"
    "buffer_load_dwordx4 v1, s[20:23], s43 offen offset:3072 sc1 lds\n"
    "v_exp_f32 v117, v117\n"
    "v_exp_f32 v118, v118\n"
    "v_exp_f32 v119, v119\n"
    "v_exp_f32 v120, v120\n"
    "v_exp_f32 v121, v121\n"
    "v_exp_f32 v122, v122\n"
    "v_smfmac_f32_32x32x32_bf16 v[144:159], v[48:51], v[112:119], v4\n"
    "ds_read_b128 v[48:51], v84\n"
    "v_exp_f32 v123, v123\n"
    "v_exp_f32 v124, v124\n"
    "v_exp_f32 v125, v125\n"
    "v_exp_f32 v126, v126\n"
    "v_exp_f32 v127, v127\n"
    "v_exp_f32 v96, v96\n"
    "v_exp_f32 v97, v97\n"
    "v_smfmac_f32_32x32x32_bf16 v[144:159], v[52:55], v[120:127], v4\n"
    "ds_read_b128 v[52:55], v85\n"
    "v_exp_f32 v98, v98\n"
    "v_exp_f32 v99, v99\n"
    "v_exp_f32 v100, v100\n"
    "v_mfma_f32_32x32x16_bf16 v[112:127], v[56:59], v[92:95], v[176:191]\n"
    "s_add_u32 m0, s59, 4096\n"
    "s_add_u32 s45, s68, 4096\n"
    "buffer_load_dwordx4 v1, s[20:23], s45 offen offset:2048 sc1 lds\n"
    "buffer_load_dwordx4 v1, s[20:23], s45 offen offset:3072 sc1 lds\n"
    "s_waitcnt lgkmcnt(1)\n"
    "v_exp_f32 v101, v101\n"
    "v_exp_f32 v102, v102\n"
    "v_exp_f32 v103, v103\n"
    "v_exp_f32 v104, v104\n"
    "v_exp_f32 v105, v105\n"
    "v_exp_f32 v106, v106\n"
    "v_smfmac_f32_32x32x32_bf16 v[128:143], v[48:51], v[96:103], v4\n"
    "s_add_u32 s43, s43, 0x1000\n"
    "buffer_load_dwordx4 v[56:59], v1, s[20:23], s43 offen sc1\n"
    "v_exp_f32 v107, v107\n"
    "v_exp_f32 v108, v108\n"
    "v_exp_f32 v109, v109\n"
    "v_exp_f32 v110, v110\n"
    "v_exp_f32 v111, v111\n"
    "v_exp_f32 v112, v112\n"
    "v_exp_f32 v113, v113\n"
    "s_waitcnt vmcnt(23) lgkmcnt(0)\n"
    "v_smfmac_f32_32x32x32_bf16 v[128:143], v[52:55], v[104:111], v4\n"
    "s_add_u32 m0, s57, 0xfffffc00\n"
    "buffer_load_dwordx4 v[216:219], v1, s[20:23], s44 offen offset:2048 sc1\n"
    "buffer_load_dwordx4 v[220:223], v1, s[20:23], s44 offen offset:3072 sc1\n"
    "v_exp_f32 v114, v114\n"
    "v_exp_f32 v115, v115\n"
    "v_exp_f32 v116, v116\n"
    "v_mfma_f32_32x32x16_bf16 v[96:111], v[60:63], v[88:91], v[160:175]\n"
    "buffer_load_dwordx4 v1, s[20:23], s43 offen offset:1024 sc1 lds\n"
    "v_exp_f32 v117, v117\n"
    "v_exp_f32 v118, v118\n"
    "v_exp_f32 v119, v119\n"
    "v_exp_f32 v120, v120\n"
    "v_exp_f32 v121, v121\n"
    "v_exp_f32 v122, v122\n"
    "v_smfmac_f32_32x32x32_bf16 v[144:159], v[48:51], v[112:119], v4\n"
    "ds_read_b128 v[48:51], v84 offset:1056\n"
    "v_exp_f32 v123, v123\n"
    "v_exp_f32 v124, v124\n"
    "v_exp_f32 v125, v125\n"
    "v_exp_f32 v126, v126\n"
    "v_exp_f32 v127, v127\n"
    "v_exp_f32 v96, v96\n"
    "v_exp_f32 v97, v97\n"
    "v_smfmac_f32_32x32x32_bf16 v[144:159], v[52:55], v[120:127], v4\n"
    "ds_read_b128 v[52:55], v85 offset:1056\n"
    "s_add_u32 m0, s59, 8192\n"
    "s_add_u32 s45, s68, 8192\n"
    "buffer_load_dwordx4 v1, s[20:23], s45 offen sc1 lds\n"
    "buffer_load_dwordx4 v1, s[20:23], s45 offen offset:1024 sc1 lds\n"
    "v_exp_f32 v98, v98\n"
    "v_exp_f32 v99, v99\n"
    "v_exp_f32 v100, v100\n"
    "v_mfma_f32_32x32x16_bf16 v[112:127], v[60:63], v[92:95], v[176:191]\n"
    "s_waitcnt lgkmcnt(1)\n"
    "v_exp_f32 v101, v101\n"
    "v_exp_f32 v102, v102\n"
    "v_exp_f32 v103, v103\n"
    "v_exp_f32 v104, v104\n"
    "v_exp_f32 v105, v105\n"
    "v_exp_f32 v106, v106\n"
    "v_smfmac_f32_32x32x32_bf16 v[128:143], v[48:51], v[96:103], v4\n"
    "buffer_load_dwordx4 v[60:63], v1, s[20:23], s43 offen offset:2048 sc1\n"
    "v_exp_f32 v107, v107\n"
    "v_exp_f32 v108, v108\n"
    "v_exp_f32 v109, v109\n"
    "v_exp_f32 v110, v110\n"
    "v_exp_f32 v111, v111\n"
    "v_exp_f32 v112, v112\n"
    "v_exp_f32 v113, v113\n"
    "s_waitcnt vmcnt(27) lgkmcnt(0)\n"
    "v_smfmac_f32_32x32x32_bf16 v[128:143], v[52:55], v[104:111], v4\n"
    "s_add_u32 m0, s57, 0xfffff820\n"
    "s_add_u32 s44, s44, 0x1000\n"
    "buffer_load_dwordx4 v[224:227], v1, s[20:23], s44 offen sc1\n"
    "v_exp_f32 v114, v114\n"
    "v_exp_f32 v115, v115\n"
    "v_exp_f32 v116, v116\n"
    "v_mfma_f32_32x32x16_bf16 v[96:111], v[64:67], v[88:91], v[160:175]\n"
    "buffer_load_dwordx4 v1, s[20:23], s43 offen offset:3072 sc1 lds\n"
    "v_exp_f32 v117, v117\n"
    "v_exp_f32 v118, v118\n"
    "v_exp_f32 v119, v119\n"
    "v_exp_f32 v120, v120\n"
    "v_exp_f32 v121, v121\n"
    "v_exp_f32 v122, v122\n"
    "v_smfmac_f32_32x32x32_bf16 v[144:159], v[48:51], v[112:119], v4\n"
    "ds_read_b128 v[48:51], v84 offset:2112\n"
    "v_exp_f32 v123, v123\n"
    "v_exp_f32 v124, v124\n"
    "v_exp_f32 v125, v125\n"
    "v_exp_f32 v126, v126\n"
    "v_exp_f32 v127, v127\n"
    "v_exp_f32 v96, v96\n"
    "v_exp_f32 v97, v97\n"
    "v_smfmac_f32_32x32x32_bf16 v[144:159], v[52:55], v[120:127], v4\n"
    "ds_read_b128 v[52:55], v85 offset:2112\n"
    "v_exp_f32 v98, v98\n"
    "v_exp_f32 v99, v99\n"
    "v_exp_f32 v100, v100\n"
    "v_mfma_f32_32x32x16_bf16 v[112:127], v[64:67], v[92:95], v[176:191]\n"
    "s_add_u32 m0, s59, 8192\n"
    "s_add_u32 s45, s68, 8192\n"
    "buffer_load_dwordx4 v1, s[20:23], s45 offen offset:2048 sc1 lds\n"
    "buffer_load_dwordx4 v1, s[20:23], s45 offen offset:3072 sc1 lds\n"
    "s_waitcnt lgkmcnt(1)\n"
    "v_exp_f32 v101, v101\n"
    "v_exp_f32 v102, v102\n"
    "v_exp_f32 v103, v103\n"
    "v_exp_f32 v104, v104\n"
    "v_exp_f32 v105, v105\n"
    "v_exp_f32 v106, v106\n"
    "v_smfmac_f32_32x32x32_bf16 v[128:143], v[48:51], v[96:103], v4\n"
    "s_lshl_b32 s69, s35, 1\n"
    "s_add_u32 s69, s69, s40\n"
    "v_exp_f32 v107, v107\n"
    "v_exp_f32 v108, v108\n"
    "v_exp_f32 v109, v109\n"
    "v_exp_f32 v110, v110\n"
    "v_exp_f32 v111, v111\n"
    "v_exp_f32 v112, v112\n"
    "v_exp_f32 v113, v113\n"
    "s_waitcnt vmcnt(25) lgkmcnt(0)\n"
    "v_smfmac_f32_32x32x32_bf16 v[128:143], v[52:55], v[104:111], v4\n"
    "s_lshl_b32 s69, s69, 4\n"
    "s_add_u32 s69, s69, s34\n"
    "buffer_load_dwordx4 v[228:231], v1, s[20:23], s44 offen offset:1024 sc1\n"
    "buffer_load_dwordx4 v[232:235], v1, s[20:23], s44 offen offset:2048 sc1\n"
    "s_setprio 0\n"
    "v_exp_f32 v114, v114\n"
    "v_exp_f32 v115, v115\n"
    "v_exp_f32 v116, v116\n"
    "v_mfma_f32_32x32x16_bf16 v[96:111], v[68:71], v[88:91], v[160:175]\n"
    "s_lshl_b32 s69, s69, 2\n"
    "v_exp_f32 v117, v117\n"
    "v_exp_f32 v118, v118\n"
    "v_exp_f32 v119, v119\n"
    "v_exp_f32 v120, v120\n"
    "v_exp_f32 v121, v121\n"
    "v_exp_f32 v122, v122\n"
    "v_smfmac_f32_32x32x32_bf16 v[144:159], v[48:51], v[112:119], v4\n"
    "ds_read_b128 v[48:51], v84 offset:3168\n"
    "v_exp_f32 v123, v123\n"
    "v_exp_f32 v124, v124\n"
    "v_exp_f32 v125, v125\n"
    "v_exp_f32 v126, v126\n"
    "v_exp_f32 v127, v127\n"
    "v_exp_f32 v96, v96\n"
    "v_exp_f32 v97, v97\n"
    "v_smfmac_f32_32x32x32_bf16 v[144:159], v[52:55], v[120:127], v4\n"
    "ds_read_b128 v[52:55], v85 offset:3168\n"
    "s_add_u32 m0, s59, 12288\n"
    "s_add_u32 s45, s68, 12288\n"
    "buffer_load_dwordx4 v1, s[20:23], s45 offen sc1 lds\n"
    "buffer_load_dwordx4 v1, s[20:23], s45 offen offset:1024 sc1 lds\n"
    "v_exp_f32 v98, v98\n"
    "v_exp_f32 v99, v99\n"
    "v_exp_f32 v100, v100\n"
    "v_mfma_f32_32x32x16_bf16 v[112:127], v[68:71], v[92:95], v[176:191]\n"
    "s_waitcnt lgkmcnt(1)\n"
    "v_exp_f32 v101, v101\n"
    "v_exp_f32 v102, v102\n"
    "v_exp_f32 v103, v103\n"
    "v_exp_f32 v104, v104\n"
    "v_exp_f32 v105, v105\n"
    "v_exp_f32 v106, v106\n"
    "v_smfmac_f32_32x32x32_bf16 v[128:143], v[48:51], v[96:103], v4\n"
    "s_add_u32 s69, s69, s39\n"
    "s_lshl_b32 s69, s69, 9\n"
    "v_exp_f32 v107, v107\n"
    "v_exp_f32 v108, v108\n"
    "v_exp_f32 v109, v109\n"
    "v_exp_f32 v110, v110\n"
    "v_exp_f32 v111, v111\n"
    "v_exp_f32 v112, v112\n"
    "v_exp_f32 v113, v113\n"
    "s_waitcnt vmcnt(23) lgkmcnt(0)\n"
    "v_smfmac_f32_32x32x32_bf16 v[128:143], v[52:55], v[104:111], v4\n"
    "s_add_u32 s69, s69, 6291456\n"
    "s_lshr_b32 s48, s35, 2\n"
    "buffer_load_dwordx4 v[236:239], v1, s[20:23], s44 offen offset:3072 sc1\n"
    "s_add_u32 s44, s44, 0x1000\n"
    "buffer_load_dwordx4 v[240:243], v1, s[20:23], s44 offen sc1\n"
    "v_exp_f32 v114, v114\n"
    "v_exp_f32 v115, v115\n"
    "v_exp_f32 v116, v116\n"
    "v_mfma_f32_32x32x16_bf16 v[96:111], v[72:75], v[88:91], v[160:175]\n"
    "s_lshl_b32 s48, s48, 1\n"
    "v_exp_f32 v117, v117\n"
    "v_exp_f32 v118, v118\n"
    "v_exp_f32 v119, v119\n"
    "v_exp_f32 v120, v120\n"
    "v_exp_f32 v121, v121\n"
    "v_exp_f32 v122, v122\n"
    "v_smfmac_f32_32x32x32_bf16 v[144:159], v[48:51], v[112:119], v4\n"
    "ds_read_b128 v[48:51], v84 offset:4224\n"
    "v_exp_f32 v123, v123\n"
    "v_exp_f32 v124, v124\n"
    "v_exp_f32 v125, v125\n"
    "v_exp_f32 v126, v126\n"
    "v_exp_f32 v127, v127\n"
    "v_exp_f32 v96, v96\n"
    "v_exp_f32 v97, v97\n"
    "v_smfmac_f32_32x32x32_bf16 v[144:159], v[52:55], v[120:127], v4\n"
    "ds_read_b128 v[52:55], v85 offset:4224\n"
    "v_exp_f32 v98, v98\n"
    "v_exp_f32 v99, v99\n"
    "v_exp_f32 v100, v100\n"
    "v_mfma_f32_32x32x16_bf16 v[112:127], v[72:75], v[92:95], v[176:191]\n"
    "s_add_u32 m0, s59, 12288\n"
    "s_add_u32 s45, s68, 12288\n"
    "buffer_load_dwordx4 v1, s[20:23], s45 offen offset:2048 sc1 lds\n"
    "buffer_load_dwordx4 v1, s[20:23], s45 offen offset:3072 sc1 lds\n"
    "s_waitcnt lgkmcnt(1)\n"
    "v_exp_f32 v101, v101\n"
    "v_exp_f32 v102, v102\n"
    "v_exp_f32 v103, v103\n"
    "v_exp_f32 v104, v104\n"
    "v_exp_f32 v105, v105\n"
    "v_exp_f32 v106, v106\n"
    "v_smfmac_f32_32x32x32_bf16 v[128:143], v[48:51], v[96:103], v4\n"
    "s_and_b32 s54, s35, 1\n"
    "s_add_u32 s48, s48, s54\n"
    "v_exp_f32 v107, v107\n"
    "v_exp_f32 v108, v108\n"
    "v_exp_f32 v109, v109\n"
    "v_exp_f32 v110, v110\n"
    "v_exp_f32 v111, v111\n"
    "v_exp_f32 v112, v112\n"
    "v_exp_f32 v113, v113\n"
    "s_waitcnt vmcnt(21) lgkmcnt(0)\n"
    "v_smfmac_f32_32x32x32_bf16 v[128:143], v[52:55], v[104:111], v4\n"
    "s_lshl_b32 s48, s48, 9\n"
    "s_and_b32 s54, s35, 2\n"
    "buffer_load_dwordx4 v[244:247], v1, s[20:23], s44 offen offset:1024 sc1\n"
    "buffer_load_dwordx4 v[248:251], v1, s[20:23], s44 offen offset:2048 sc1\n"
    "v_exp_f32 v114, v114\n"
    "v_exp_f32 v115, v115\n"
    "v_exp_f32 v116, v116\n"
    "v_mfma_f32_32x32x16_bf16 v[96:111], v[76:79], v[88:91], v[160:175]\n"
    "s_lshl_b32 s54, s54, 2\n"
    "v_exp_f32 v117, v117\n"
    "v_exp_f32 v118, v118\n"
    "v_exp_f32 v119, v119\n"
    "v_exp_f32 v120, v120\n"
    "v_exp_f32 v121, v121\n"
    "v_exp_f32 v122, v122\n"
    "v_smfmac_f32_32x32x32_bf16 v[144:159], v[48:51], v[112:119], v4\n"
    "ds_read_b128 v[48:51], v84 offset:5280\n"
    "v_exp_f32 v123, v123\n"
    "v_exp_f32 v124, v124\n"
    "v_exp_f32 v125, v125\n"
    "v_exp_f32 v126, v126\n"
    "v_exp_f32 v127, v127\n"
    "v_exp_f32 v96, v96\n"
    "v_exp_f32 v97, v97\n"
    "v_smfmac_f32_32x32x32_bf16 v[144:159], v[52:55], v[120:127], v4\n"
    "ds_read_b128 v[52:55], v85 offset:5280\n"
    "v_exp_f32 v98, v98\n"
    "v_exp_f32 v99, v99\n"
    "v_exp_f32 v100, v100\n"
    "v_mfma_f32_32x32x16_bf16 v[112:127], v[76:79], v[92:95], v[176:191]\n"
    "s_waitcnt lgkmcnt(1)\n"
    "v_exp_f32 v101, v101\n"
    "v_exp_f32 v102, v102\n"
    "v_exp_f32 v103, v103\n"
    "v_exp_f32 v104, v104\n"
    "v_exp_f32 v105, v105\n"
    "v_exp_f32 v106, v106\n"
    "v_smfmac_f32_32x32x32_bf16 v[128:143], v[48:51], v[96:103], v4\n"
    "s_add_u32 s48, s48, s54\n"
    "s_add_u32 s48, s48, s34\n"
    "v_exp_f32 v107, v107\n"
    "v_exp_f32 v108, v108\n"
    "v_exp_f32 v109, v109\n"
    "v_exp_f32 v110, v110\n"
    "v_exp_f32 v111, v111\n"
    "v_exp_f32 v112, v112\n"
    "v_exp_f32 v113, v113\n"
    "s_waitcnt vmcnt(17) lgkmcnt(0)\n"
    "v_smfmac_f32_32x32x32_bf16 v[128:143], v[52:55], v[104:111], v4\n"
    "s_lshl_b32 s48, s48, 2\n"
    "s_add_u32 s48, s48, 1441792\n"
    "buffer_load_dwordx4 v[252:255], v1, s[20:23], s44 offen offset:3072 sc1\n"
    "v_exp_f32 v114, v114\n"
    "v_exp_f32 v115, v115\n"
    "v_exp_f32 v116, v116\n"
    "v_mfma_f32_32x32x16_bf16 v[96:111], v[56:59], v[88:91], v[160:175]\n"
    "s_lshl_b32 s49, s56, 5\n"
    "v_exp_f32 v117, v117\n"
    "v_exp_f32 v118, v118\n"
    "v_exp_f32 v119, v119\n"
    "v_exp_f32 v120, v120\n"
    "v_exp_f32 v121, v121\n"
    "v_exp_f32 v122, v122\n"
    "v_smfmac_f32_32x32x32_bf16 v[144:159], v[48:51], v[112:119], v4\n"
    "ds_read_b128 v[48:51], v84\n"
    "v_exp_f32 v123, v123\n"
    "v_exp_f32 v124, v124\n"
    "v_exp_f32 v125, v125\n"
    "v_exp_f32 v126, v126\n"
    "v_exp_f32 v127, v127\n"
    "v_exp_f32 v96, v96\n"
    "v_exp_f32 v97, v97\n"
    "v_smfmac_f32_32x32x32_bf16 v[144:159], v[52:55], v[120:127], v4\n"
    "ds_read_b128 v[52:55], v85\n"
    "v_exp_f32 v98, v98\n"
    "v_exp_f32 v99, v99\n"
    "v_exp_f32 v100, v100\n"
    "v_mfma_f32_32x32x16_bf16 v[112:127], v[56:59], v[92:95], v[176:191]\n"
    "s_waitcnt lgkmcnt(1)\n"
    "v_exp_f32 v101, v101\n"
    "v_exp_f32 v102, v102\n"
    "v_exp_f32 v103, v103\n"
    "v_exp_f32 v104, v104\n"
    "v_exp_f32 v105, v105\n"
    "v_exp_f32 v106, v106\n"
    "v_smfmac_f32_32x32x32_bf16 v[128:143], v[48:51], v[96:103], v4\n"
    "s_add_u32 s49, s49, s33\n"
    "s_andn2_b32 s49, s49, 1\n"
    "v_exp_f32 v107, v107\n"
    "v_exp_f32 v108, v108\n"
    "v_exp_f32 v109, v109\n"
    "v_exp_f32 v110, v110\n"
    "v_exp_f32 v111, v111\n"
    "v_exp_f32 v112, v112\n"
    "v_exp_f32 v113, v113\n"
    "s_waitcnt vmcnt(13) lgkmcnt(0)\n"
    "v_smfmac_f32_32x32x32_bf16 v[128:143], v[52:55], v[104:111], v4\n"
    "s_lshl_b32 s49, s49, 5\n"
    "s_add_u32 s49, s49, s3\n"
    "v_exp_f32 v114, v114\n"
    "v_exp_f32 v115, v115\n"
    "v_exp_f32 v116, v116\n"
    "v_mfma_f32_32x32x16_bf16 v[96:111], v[60:63], v[88:91], v[160:175]\n"
    "s_lshl_b32 s49, s49, 2\n"
    "v_exp_f32 v117, v117\n"
    "v_exp_f32 v118, v118\n"
    "v_exp_f32 v119, v119\n"
    "v_exp_f32 v120, v120\n"
    "v_exp_f32 v121, v121\n"
    "v_exp_f32 v122, v122\n"
    "v_smfmac_f32_32x32x32_bf16 v[144:159], v[48:51], v[112:119], v4\n"
    "ds_read_b128 v[48:51], v84 offset:1056\n"
    "v_exp_f32 v123, v123\n"
    "v_exp_f32 v124, v124\n"
    "v_exp_f32 v125, v125\n"
    "v_exp_f32 v126, v126\n"
    "v_exp_f32 v127, v127\n"
    "v_exp_f32 v96, v96\n"
    "v_exp_f32 v97, v97\n"
    "v_smfmac_f32_32x32x32_bf16 v[144:159], v[52:55], v[120:127], v4\n"
    "ds_read_b128 v[52:55], v85 offset:1056\n"
    "v_exp_f32 v98, v98\n"
    "v_exp_f32 v99, v99\n"
    "v_exp_f32 v100, v100\n"
    "v_mfma_f32_32x32x16_bf16 v[112:127], v[60:63], v[92:95], v[176:191]\n"
    "s_waitcnt lgkmcnt(1)\n"
    "v_exp_f32 v101, v101\n"
    "v_exp_f32 v102, v102\n"
    "v_exp_f32 v103, v103\n"
    "v_exp_f32 v104, v104\n"
    "v_exp_f32 v105, v105\n"
    "v_exp_f32 v106, v106\n"
    "v_smfmac_f32_32x32x32_bf16 v[128:143], v[48:51], v[96:103], v4\n"
    "s_add_u32 s49, s49, 1441792\n"
    "s_xor_b32 s53, s3, 4\n"
    "v_exp_f32 v107, v107\n"
    "v_exp_f32 v108, v108\n"
    "v_exp_f32 v109, v109\n"
    "v_exp_f32 v110, v110\n"
    "v_exp_f32 v111, v111\n"
    "v_exp_f32 v112, v112\n"
    "v_exp_f32 v113, v113\n"
    "s_waitcnt lgkmcnt(0)\n"
    "v_smfmac_f32_32x32x32_bf16 v[128:143], v[52:55], v[104:111], v4\n"
    "s_mul_i32 s53, s53, 6336\n"
    "s_add_u32 s53, s53, 98304\n"
    "v_exp_f32 v114, v114\n"
    "v_exp_f32 v115, v115\n"
    "v_exp_f32 v116, v116\n"
    "v_exp_f32 v117, v117\n"
    "v_exp_f32 v118, v118\n"
    "v_exp_f32 v119, v119\n"
    "v_exp_f32 v120, v120\n"
    "v_exp_f32 v121, v121\n"
    "v_exp_f32 v122, v122\n"
    "v_smfmac_f32_32x32x32_bf16 v[144:159], v[48:51], v[112:119], v4\n"
    "v_exp_f32 v123, v123\n"
    "v_exp_f32 v124, v124\n"
    "v_exp_f32 v125, v125\n"
    "v_exp_f32 v126, v126\n"
    "v_exp_f32 v127, v127\n"
    ".Lk_b_end:\n"
    "v_mul_f32 v12, 0x5d800000, v136\n"
    "v_add_u32 v14, s57, v1\n"
    "v_smfmac_f32_32x32x32_bf16 v[144:159], v[52:55], v[120:127], v4\n"
    "v_fmac_f32 v12, 0, v128\n"
    "v_fmac_f32 v12, 0, v129\n"
    "v_fmac_f32 v12, 0, v130\n"
    "v_fmac_f32 v12, 0, v131\n"
    "v_fmac_f32 v12, 0, v132\n"
    "v_fmac_f32 v12, 0, v133\n"
    "v_fmac_f32 v12, 0, v134\n"
    "v_fmac_f32 v12, 0, v135\n"
    "v_add_u32 v15, s53, v1\n"
    "v_mov_b32 v17, 0x7f800000\n"
    "s_nop 1\n"
    "v_mul_f32 v13, 0x5d800000, v152\n"
    "v_fmac_f32 v13, 0, v144\n"
    "v_fmac_f32 v13, 0, v145\n"
    "v_fmac_f32 v13, 0, v146\n"
    "v_fmac_f32 v13, 0, v147\n"
    "v_fmac_f32 v13, 0, v148\n"
    "v_fmac_f32 v13, 0, v149\n"
    "v_fmac_f32 v13, 0, v150\n"
    "v_fmac_f32 v13, 0, v151\n"
    "v_mov_b32 v153, v6\n"
    "v_fmac_f32 v12, 0, v13\n"
    "v_cmp_nlt_f32 vcc, v12, v17\n"
    "s_cbranch_vccnz .Lk_b_fb\n"
    ".Lk_b_ok:\n"
    "ds_write_b64 v14, v[152:153] offset:2048\n"
    "ds_write_b128 v14, v[144:147]\n"
    "ds_write_b128 v14, v[148:151] offset:1024\n"
    "v_and_b32 v12, 15, v0\n"
    "v_lshlrev_b32 v12, 4, v12\n"
    "v_lshrrev_b32 v13, 5, v0\n"
    "v_lshl_or_b32 v12, v13, 8, v12\n"
    "v_bfe_u32 v13, v0, 4, 1\n"
    "v_lshl_or_b32 v7, v13, 14, v12\n"
    "v_lshlrev_b32 v20, 8, v0\n"
    "v_mov_b32 v21, s38\n"
    "v_lshlrev_b32 v41, 5, v0\n"
    "v_mov_b32 v40, 0\n"
    "s_setprio 0\n"
    "s_cmp_eq_u32 s3, 0\n"
    "s_waitcnt vmcnt(0) lgkmcnt(0)\n"
    "s_barrier\n"
    "ds_read_b64 v[104:105], v15 offset:2048\n"
    "ds_read_b128 v[96:99], v15\n"
    "ds_read_b128 v[100:103], v15 offset:1024\n"
    "s_waitcnt lgkmcnt(2)\n"
    "v_max_f32 v12, v5, v105\n"
    "v_sub_f32 v13, v5, v12\n"
    "v_sub_f32 v12, v105, v12\n"
    "v_exp_f32 v13, v13\n"
    "v_exp_f32 v12, v12\n"
    "v_mul_f32 v14, v136, v13\n"
    "v_fmac_f32 v14, v104, v12\n"
    "v_rcp_f32 v14, v14\n"
    "s_nop 0\n"
    "v_mul_f32 v22, v13, v14\n"
    "v_mul_f32 v12, v12, v14\n"
    "v_mul_f32 v128, v128, v22\n"
    "v_mul_f32 v129, v129, v22\n"
    "v_mul_f32 v130, v130, v22\n"
    "v_mul_f32 v131, v131, v22\n"
    "v_mul_f32 v132, v132, v22\n"
    "v_mul_f32 v133, v133, v22\n"
    "v_mul_f32 v134, v134, v22\n"
    "v_mul_f32 v135, v135, v22\n"
    "s_waitcnt lgkmcnt(1)\n"
    "v_fmac_f32 v128, v96, v12\n"
    "v_fmac_f32 v129, v97, v12\n"
    "v_fmac_f32 v130, v98, v12\n"
    "v_fmac_f32 v131, v99, v12\n"
    "s_waitcnt lgkmcnt(0)\n"
    "v_fmac_f32 v132, v100, v12\n"
    "v_fmac_f32 v133, v101, v12\n"
    "v_fmac_f32 v134, v102, v12\n"
    "v_fmac_f32 v135, v103, v12\n"
    "v_cvt_pk_bf16_f32 v16, v128, v129\n"
    "v_cvt_pk_bf16_f32 v17, v130, v131\n"
    "v_cvt_pk_bf16_f32 v18, v132, v133\n"
    "v_cvt_pk_bf16_f32 v19, v134, v135\n"
    "v_min_u32 v16, -2, v16\n"
    "v_min_u32 v17, -2, v17\n"
    "v_min_u32 v18, -2, v18\n"
    "v_min_u32 v19, -2, v19\n"
    "buffer_store_dwordx4 v[16:19], v7, s[20:23], s69 offen sc1\n"
    "s_cbranch_scc0 .Lk_b_nosig\n"
    "s_mov_b64 exec, 0xff\n"
    "buffer_atomic_swap v21, v20, s[20:23], s48 offen sc1\n"
    "s_mov_b64 exec, -1\n"
    ".Lk_b_nosig:\n"
    "v_lshlrev_b32 v24, 16, v8\n"
    "v_and_b32 v25, 0xffff0000, v8\n"
    "v_lshlrev_b32 v26, 16, v9\n"
    "v_and_b32 v27, 0xffff0000, v9\n"
    "v_lshlrev_b32 v28, 16, v10\n"
    "v_and_b32 v29, 0xffff0000, v10\n"
    "v_lshlrev_b32 v30, 16, v11\n"
    "v_and_b32 v31, 0xffff0000, v11\n"
    "s_lshl_b32 s41, s56, 4\n"
    "s_add_u32 s41, s41, s60\n"
    "s_lshl_b32 s42, s41, 14\n"
    "s_lshl_b32 s54, s3, 11\n"
    "s_add_u32 s41, s42, s54\n"
    "s_add_u32 s41, s41, 6291456\n"
    "s_add_u32 s46, s54, 98304\n"
    "s_and_b32 s57, s33, 7\n"
    "s_mul_i32 s57, s57, 176128\n"
    "s_add_u32 s57, s57, 19464192\n"
    "s_lshr_b32 s58, s33, 3\n"
    "s_bfe_u32 s59, s3, 0x10001\n"
    "s_mul_i32 s45, s3, 3072\n"
    "s_add_u32 m0, s45, 131072\n"
    "s_add_u32 s45, s45, s57\n"
    "s_mul_i32 s53, s40, 77824\n"
    "s_add_u32 s45, s45, s53\n"
    "s_mov_b32 s47, m0\n"
    "s_mov_b32 m0, s46\n"
    "s_cmp_lt_u32 s3, 6\n"
    "s_cbranch_scc0 .Lk_b_nocs\n"
    "s_lshl_b32 s53, s3, 14\n"
    "v_add_u32 v12, s53, v1\n"
    "v_mov_b32 v32, 0x3f803f80\n"
    "v_mov_b32 v33, 0x3f803f80\n"
    "v_mov_b32 v34, 0x3f803f80\n"
    "v_mov_b32 v35, 0x3f803f80\n"
    "ds_read_b128 v[96:99], v12\n"
    "ds_read_b128 v[100:103], v12 offset:1024\n"
    "ds_read_b128 v[104:107], v12 offset:2048\n"
    "ds_read_b128 v[108:111], v12 offset:3072\n"
    "ds_read_b128 v[112:115], v12 offset:4096\n"
    "ds_read_b128 v[116:119], v12 offset:5120\n"
    "ds_read_b128 v[120:123], v12 offset:6144\n"
    "ds_read_b128 v[124:127], v12 offset:7168\n"
    "s_waitcnt lgkmcnt(7)\n"
    "v_mfma_f32_16x16x32_bf16 v[36:39], v[32:35], v[96:99], 0\n"
    "ds_read_b128 v[128:131], v12 offset:8192\n"
    "s_waitcnt lgkmcnt(7)\n"
    "v_mfma_f32_16x16x32_bf16 v[36:39], v[32:35], v[100:103], v[36:39]\n"
    "ds_read_b128 v[132:135], v12 offset:9216\n"
    "s_waitcnt lgkmcnt(7)\n"
    "v_mfma_f32_16x16x32_bf16 v[36:39], v[32:35], v[104:107], v[36:39]\n"
    "ds_read_b128 v[136:139], v12 offset:10240\n"
    "s_waitcnt lgkmcnt(7)\n"
    "v_mfma_f32_16x16x32_bf16 v[36:39], v[32:35], v[108:111], v[36:39]\n"
    "ds_read_b128 v[140:143], v12 offset:11264\n"
    "s_waitcnt lgkmcnt(7)\n"
    "v_mfma_f32_16x16x32_bf16 v[36:39], v[32:35], v[112:115], v[36:39]\n"
    "ds_read_b128 v[144:147], v12 offset:12288\n"
    "s_waitcnt lgkmcnt(7)\n"
    "v_mfma_f32_16x16x32_bf16 v[36:39], v[32:35], v[116:119], v[36:39]\n"
    "ds_read_b128 v[148:151], v12 offset:13312\n"
    "s_waitcnt lgkmcnt(7)\n"
    "v_mfma_f32_16x16x32_bf16 v[36:39], v[32:35], v[120:123], v[36:39]\n"
    "ds_read_b128 v[152:155], v12 offset:14336\n"
    "s_waitcnt lgkmcnt(7)\n"
    "v_mfma_f32_16x16x32_bf16 v[36:39], v[32:35], v[124:127], v[36:39]\n"
    "ds_read_b128 v[156:159], v12 offset:15360\n"
    "s_waitcnt lgkmcnt(7)\n"
    "v_mfma_f32_16x16x32_bf16 v[36:39], v[32:35], v[128:131], v[36:39]\n"
    "s_waitcnt lgkmcnt(6)\n"
    "v_mfma_f32_16x16x32_bf16 v[36:39], v[32:35], v[132:135], v[36:39]\n"
    "s_waitcnt lgkmcnt(5)\n"
    "v_mfma_f32_16x16x32_bf16 v[36:39], v[32:35], v[136:139], v[36:39]\n"
    "s_waitcnt lgkmcnt(4)\n"
    "v_mfma_f32_16x16x32_bf16 v[36:39], v[32:35], v[140:143], v[36:39]\n"
    "s_waitcnt lgkmcnt(3)\n"
    "v_mfma_f32_16x16x32_bf16 v[36:39], v[32:35], v[144:147], v[36:39]\n"
    "s_waitcnt lgkmcnt(2)\n"
    "v_mfma_f32_16x16x32_bf16 v[36:39], v[32:35], v[148:151], v[36:39]\n"
    "s_waitcnt lgkmcnt(1)\n"
    "v_mfma_f32_16x16x32_bf16 v[36:39], v[32:35], v[152:155], v[36:39]\n"
    "s_waitcnt lgkmcnt(0)\n"
    "v_mfma_f32_16x16x32_bf16 v[36:39], v[32:35], v[156:159], v[36:39]\n"
    "s_lshl_b32 s53, s3, 6\n"
    "s_add_u32 s53, s53, 159744\n"
    "v_and_b32 v12, 15, v0\n"
    "v_lshl_add_u32 v12, v12, 2, s53\n"
    "s_nop 3\n"
    "ds_write_b32 v12, v36\n"
    ".Lk_b_nocs:\n"
    "s_cmp_eq_u32 s3, 7\n"
    "s_cbranch_scc0 .Lk_c_np\n"
    "s_sub_u32 s49, s49, 28\n"
    "v_lshlrev_b32 v41, 2, v0\n"
    "s_mov_b64 exec, 0xffff\n"
    "s_mov_b32 s55, 0x4000\n"
    "v_mov_b32 v52, 0\n"
    "buffer_atomic_or v52, v41, s[20:23], s49 offen sc0 sc1\n"
    "s_sleep 2\n"
    "v_mov_b32 v53, 0\n"
    "buffer_atomic_or v53, v41, s[20:23], s49 offen sc0 sc1\n"
    "s_sleep 2\n"
    "v_mov_b32 v54, 0\n"
    "buffer_atomic_or v54, v41, s[20:23], s49 offen sc0 sc1\n"
    "s_sleep 2\n"
    ".Lk_ql3:\n"
    "v_mov_b32 v55, 0\n"
    "buffer_atomic_or v55, v41, s[20:23], s49 offen sc0 sc1\n"
    "s_waitcnt vmcnt(3)\n"
    "v_cmp_ne_u32 vcc, s38, v52\n"
    "s_cbranch_vccz .Lk_qd4\n"
    "v_mov_b32 v52, 0\n"
    "buffer_atomic_or v52, v41, s[20:23], s49 offen sc0 sc1\n"
    "s_waitcnt vmcnt(3)\n"
    "v_cmp_ne_u32 vcc, s38, v53\n"
    "s_cbranch_vccz .Lk_qd4\n"
    "v_mov_b32 v53, 0\n"
    "buffer_atomic_or v53, v41, s[20:23], s49 offen sc0 sc1\n"
    "s_waitcnt vmcnt(3)\n"
    "v_cmp_ne_u32 vcc, s38, v54\n"
    "s_cbranch_vccz .Lk_qd4\n"
    "v_mov_b32 v54, 0\n"
    "buffer_atomic_or v54, v41, s[20:23], s49 offen sc0 sc1\n"
    "s_waitcnt vmcnt(3)\n"
    "v_cmp_ne_u32 vcc, s38, v55\n"
    "s_cbranch_vccz .Lk_qd4\n"
    "s_sub_u32 s55, s55, 1\n"
    "s_cbranch_scc0 .Lk_ql3\n"
    ".Lk_qd4:\n"
    "s_mov_b64 exec, -1\n"
    ".Lk_c_np:\n"
    "s_barrier\n"
    "buffer_load_dwordx4 v1, s[20:23], s41 offen sc1 lds\n"
    "buffer_load_dwordx4 v1, s[20:23], s41 offen offset:1024 sc1 lds\n"
    "s_lshl_b32 s53, s61, 3\n"
    "s_add_u32 s53, s53, s3\n"
    "s_lshl_b32 s53, s53, 9\n"
    "s_add_u32 s42, s42, s53\n"
    "s_add_u32 s42, s42, 8388608\n"
    "v_and_b32 v12, 0x1ff, v7\n"
    "v_bfe_u32 v13, v0, 4, 1\n"
    "v_lshl_or_b32 v8, v13, 30, v12\n"
    "v_add_u32 v9, 98304, v1\n"
    "s_lshl_b32 s53, s3, 3\n"
    "s_add_u32 s53, s53, 155648\n"
    "v_and_b32 v12, 15, v0\n"
    "v_lshl_add_u32 v10, v12, 6, s53\n"
    "v_lshlrev_b32 v20, 12, v0\n"
    "s_lshl_b32 s43, s56, 8\n"
    "s_lshr_b32 s53, s60, 1\n"
    "s_add_u32 s43, s43, s53\n"
    "s_lshl_b32 s43, s43, 5\n"
    "s_and_b32 s53, s33, 7\n"
    "s_add_u32 s43, s43, s53\n"
    "s_lshl_b32 s43, s43, 2\n"
    "s_add_u32 s43, s43, 1048576\n"
    "s_add_u32 s41, s41, 131072\n"
    "s_add_u32 m0, m0, 0x4000\n"
    "s_nop 0\n"
    "buffer_load_dwordx4 v1, s[20:23], s41 offen sc1 lds\n"
    "buffer_load_dwordx4 v1, s[20:23], s41 offen offset:1024 sc1 lds\n"
    "s_lshl_b32 s69, s62, 5\n"
    "s_add_u32 s69, s69, s63\n"
    "s_andn2_b32 s69, s69, 3\n"
    "s_lshl_b32 s69, s69, 3\n"
    "s_add_u32 s69, s69, s3\n"
    "s_lshl_b32 s69, s69, 7\n"
    "s_add_u32 s69, s69, 1048576\n"
    "v_lshlrev_b32 v3, 2, v0\n"
    "v_add_u32 v2, 0x10000, v1\n"
    "v_mov_b32 v4, 0\n"
    "v_mov_b32 v5, 160256\n"
    "s_lshl_b32 s66, s62, 3\n"
    "s_add_u32 s66, s66, s3\n"
    "s_mov_b32 s71, 0\n"
    "s_lshl_b32 s67, s66, 15\n"
    "s_add_u32 s67, s67, 8388608\n"
    "s_add_u32 s68, s67, 0x4000\n"
    "s_mov_b32 m0, s47\n"
    "s_nop 0\n"
    "buffer_load_dwordx4 v1, s[20:23], s45 offen sc1 lds\n"
    "buffer_load_dwordx4 v1, s[20:23], s45 offen offset:1024 sc1 lds\n"
    "buffer_load_dwordx4 v1, s[20:23], s45 offen offset:2048 sc1 lds\n"
    "s_cmp_lt_u32 s3, 4\n"
    "s_cselect_b32 s70, s65, 0\n"
    "s_waitcnt vmcnt(5)\n"
    "s_barrier\n"
    "ds_read_b128 v[64:67], v9\n"
    "ds_read_b128 v[68:71], v9 offset:1024\n"
    "ds_read_b128 v[72:75], v9 offset:2048\n"
    "ds_read_b128 v[76:79], v9 offset:3072\n"
    "ds_read_b128 v[80:83], v9 offset:4096\n"
    "ds_read_b128 v[84:87], v9 offset:5120\n"
    "ds_read_b128 v[88:91], v9 offset:6144\n"
    "ds_read_b128 v[92:95], v9 offset:7168\n"
    "s_waitcnt lgkmcnt(7)\n"
    "v_mfma_f32_16x16x32_bf16 v[44:47], v[192:195], v[64:67], v[24:27]\n"
    "ds_read_b128 v[96:99], v9 offset:8192\n"
    "s_waitcnt lgkmcnt(7)\n"
    "v_mfma_f32_16x16x32_bf16 v[44:47], v[196:199], v[68:71], v[44:47]\n"
    "ds_read_b128 v[100:103], v9 offset:9216\n"
    "s_waitcnt lgkmcnt(7)\n"
    "v_mfma_f32_16x16x32_bf16 v[44:47], v[200:203], v[72:75], v[44:47]\n"
    "ds_read_b128 v[104:107], v9 offset:10240\n"
    "s_waitcnt lgkmcnt(7)\n"
    "v_mfma_f32_16x16x32_bf16 v[44:47], v[204:207], v[76:79], v[44:47]\n"
    "ds_read_b128 v[108:111], v9 offset:11264\n"
    "s_waitcnt lgkmcnt(7)\n"
    "v_mfma_f32_16x16x32_bf16 v[44:47], v[208:211], v[80:83], v[44:47]\n"
    "ds_read_b128 v[112:115], v9 offset:12288\n"
    "s_waitcnt lgkmcnt(7)\n"
    "v_mfma_f32_16x16x32_bf16 v[44:47], v[212:215], v[84:87], v[44:47]\n"
    "ds_read_b128 v[116:119], v9 offset:13312\n"
    "s_waitcnt lgkmcnt(7)\n"
    "v_mfma_f32_16x16x32_bf16 v[44:47], v[216:219], v[88:91], v[44:47]\n"
    "ds_read_b128 v[120:123], v9 offset:14336\n"
    "s_waitcnt lgkmcnt(7)\n"
    "v_mfma_f32_16x16x32_bf16 v[44:47], v[220:223], v[92:95], v[44:47]\n"
    "ds_read_b128 v[124:127], v9 offset:15360\n"
    "s_waitcnt lgkmcnt(7)\n"
    "v_mfma_f32_16x16x32_bf16 v[44:47], v[224:227], v[96:99], v[44:47]\n"
    "s_waitcnt lgkmcnt(6)\n"
    "v_mfma_f32_16x16x32_bf16 v[44:47], v[228:231], v[100:103], v[44:47]\n"
    "s_waitcnt lgkmcnt(5)\n"
    "v_mfma_f32_16x16x32_bf16 v[44:47], v[232:235], v[104:107], v[44:47]\n"
    "s_waitcnt lgkmcnt(4)\n"
    "v_mfma_f32_16x16x32_bf16 v[44:47], v[236:239], v[108:111], v[44:47]\n"
    "s_waitcnt lgkmcnt(3)\n"
    "v_mfma_f32_16x16x32_bf16 v[44:47], v[240:243], v[112:115], v[44:47]\n"
    "s_waitcnt lgkmcnt(2)\n"
    "v_mfma_f32_16x16x32_bf16 v[44:47], v[244:247], v[116:119], v[44:47]\n"
    "s_waitcnt lgkmcnt(1)\n"
    "v_mfma_f32_16x16x32_bf16 v[44:47], v[248:251], v[120:123], v[44:47]\n"
    "s_waitcnt lgkmcnt(0)\n"
    "v_mfma_f32_16x16x32_bf16 v[44:47], v[252:255], v[124:127], v[44:47]\n"
    "s_waitcnt vmcnt(0)\n"
    "s_nop 6\n"
    "v_cmp_u_f32 vcc, v44, v44\n"
    "s_cbranch_vccnz .Lk_c_slow0\n"
    ".Lk_c_back0:\n"
    "s_nop 6\n"
    "v_cvt_pk_bf16_f32 v16, v44, v45\n"
    "v_cvt_pk_bf16_f32 v17, v46, v47\n"
    "v_min_u32 v18, -2, v16\n"
    "v_min_u32 v19, -2, v17\n"
    "v_min_u32 v16, -2, v16\n"
    "v_min_u32 v17, -2, v17\n"
    "s_nop 0\n"
    "v_permlane16_swap_b32 v16, v18\n"
    "v_permlane16_swap_b32 v17, v19\n"
    "buffer_store_dwordx4 v[16:19], v8, s[20:23], s42 offen sc1\n"
    "s_cmp_eq_u32 s3, 3\n"
    "s_cbranch_scc0 .Lk_c_nosig0\n"
    "s_mov_b64 exec, 0xff\n"
    "buffer_atomic_swap v21, v20, s[20:23], s43 offen sc1\n"
    "s_mov_b64 exec, -1\n"
    ".Lk_c_nosig0:\n"
    "s_barrier\n"
    "ds_read_b128 v[64:67], v9 offset:16384\n"
    "ds_read_b128 v[68:71], v9 offset:17408\n"
    "ds_read_b128 v[72:75], v9 offset:18432\n"
    "ds_read_b128 v[76:79], v9 offset:19456\n"
    "ds_read_b128 v[80:83], v9 offset:20480\n"
    "ds_read_b128 v[84:87], v9 offset:21504\n"
    "ds_read_b128 v[88:91], v9 offset:22528\n"
    "ds_read_b128 v[92:95], v9 offset:23552\n"
    "s_waitcnt lgkmcnt(7)\n"
    "v_mfma_f32_16x16x32_bf16 v[48:51], v[192:195], v[64:67], v[28:31]\n"
    "ds_read_b128 v[96:99], v9 offset:24576\n"
    "s_waitcnt lgkmcnt(7)\n"
    "v_mfma_f32_16x16x32_bf16 v[48:51], v[196:199], v[68:71], v[48:51]\n"
    "ds_read_b128 v[100:103], v9 offset:25600\n"
    "s_waitcnt lgkmcnt(7)\n"
    "v_mfma_f32_16x16x32_bf16 v[48:51], v[200:203], v[72:75], v[48:51]\n"
    "ds_read_b128 v[104:107], v9 offset:26624\n"
    "s_waitcnt lgkmcnt(7)\n"
    "v_mfma_f32_16x16x32_bf16 v[48:51], v[204:207], v[76:79], v[48:51]\n"
    "ds_read_b128 v[108:111], v9 offset:27648\n"
    "s_waitcnt lgkmcnt(7)\n"
    "v_mfma_f32_16x16x32_bf16 v[48:51], v[208:211], v[80:83], v[48:51]\n"
    "ds_read_b128 v[112:115], v9 offset:28672\n"
    "s_cmp_eq_u32 s70, 1\n"
    "s_cbranch_scc0 .Lk_c_noask\n"
    "s_mov_b64 exec, 0xff\n"
    "buffer_atomic_or v4, v3, s[20:23], s69 offen sc0 sc1\n"
    "s_mov_b64 exec, -1\n"
    ".Lk_c_noask:\n"
    "s_waitcnt lgkmcnt(7)\n"
    "v_mfma_f32_16x16x32_bf16 v[48:51], v[212:215], v[84:87], v[48:51]\n"
    "ds_read_b128 v[116:119], v9 offset:29696\n"
    "s_waitcnt lgkmcnt(7)\n"
    "v_mfma_f32_16x16x32_bf16 v[48:51], v[216:219], v[88:91], v[48:51]\n"
    "ds_read_b128 v[120:123], v9 offset:30720\n"
    "s_waitcnt lgkmcnt(7)\n"
    "v_mfma_f32_16x16x32_bf16 v[48:51], v[220:223], v[92:95], v[48:51]\n"
    "ds_read_b128 v[124:127], v9 offset:31744\n"
    "s_waitcnt lgkmcnt(7)\n"
    "v_mfma_f32_16x16x32_bf16 v[48:51], v[224:227], v[96:99], v[48:51]\n"
    "s_waitcnt lgkmcnt(6)\n"
    "v_mfma_f32_16x16x32_bf16 v[48:51], v[228:231], v[100:103], v[48:51]\n"
    "s_waitcnt lgkmcnt(5)\n"
    "v_mfma_f32_16x16x32_bf16 v[48:51], v[232:235], v[104:107], v[48:51]\n"
    "s_waitcnt lgkmcnt(4)\n"
    "v_mfma_f32_16x16x32_bf16 v[48:51], v[236:239], v[108:111], v[48:51]\n"
    "s_waitcnt lgkmcnt(3)\n"
    "v_mfma_f32_16x16x32_bf16 v[48:51], v[240:243], v[112:115], v[48:51]\n"
    "s_waitcnt lgkmcnt(2)\n"
    "v_mfma_f32_16x16x32_bf16 v[48:51], v[244:247], v[116:119], v[48:51]\n"
    "s_waitcnt lgkmcnt(1)\n"
    "v_mfma_f32_16x16x32_bf16 v[48:51], v[248:251], v[120:123], v[48:51]\n"
    "s_waitcnt lgkmcnt(0)\n"
    "v_mfma_f32_16x16x32_bf16 v[48:51], v[252:255], v[124:127], v[48:51]\n"
    "s_add_u32 s42, s42, 131072\n"
    "s_add_u32 s43, s43, 0x200\n"
    "s_nop 5\n"
    "v_cmp_u_f32 vcc, v48, v48\n"
    "s_cbranch_vccnz .Lk_c_slow1\n"
    ".Lk_c_back1:\n"
    "s_nop 6\n"
    "v_cvt_pk_bf16_f32 v32, v48, v49\n"
    "v_cvt_pk_bf16_f32 v33, v50, v51\n"
    "v_min_u32 v34, -2, v32\n"
    "v_min_u32 v35, -2, v33\n"
    "v_min_u32 v32, -2, v32\n"
    "v_min_u32 v33, -2, v33\n"
    "s_nop 0\n"
    "v_permlane16_swap_b32 v32, v34\n"
    "v_permlane16_swap_b32 v33, v35\n"
    "buffer_store_dwordx4 v[32:35], v8, s[20:23], s42 offen sc1\n"
    "s_cmp_eq_u32 s3, 3\n"
    "s_cbranch_scc0 .Lk_c_nosig1\n"
    "s_mov_b64 exec, 0xff\n"
    "buffer_atomic_swap v21, v20, s[20:23], s43 offen sc1\n"
    "s_mov_b64 exec, -1\n"
    ".Lk_c_nosig1:\n"
    "v_add_f32 v12, v44, v45\n"
    "v_mul_f32 v13, v44, v44\n"
    "v_add_f32 v12, v12, v46\n"
    "v_fmac_f32 v13, v45, v45\n"
    "v_add_f32 v12, v12, v47\n"
    "v_fmac_f32 v13, v46, v46\n"
    "v_fmac_f32 v13, v47, v47\n"
    "v_add_f32 v40, v48, v49\n"
    "v_mul_f32 v41, v48, v48\n"
    "v_add_f32 v40, v40, v50\n"
    "v_fmac_f32 v41, v49, v49\n"
    "v_add_f32 v40, v40, v51\n"
    "v_fmac_f32 v41, v50, v50\n"
    "v_fmac_f32 v41, v51, v51\n"
    "v_mov_b32 v14, v12\n"
    "v_mov_b32 v15, v13\n"
    "v_mov_b32 v42, v40\n"
    "v_mov_b32 v43, v41\n"
    "v_permlane16_swap_b32 v12, v14\n"
    "v_permlane16_swap_b32 v13, v15\n"
    "v_permlane16_swap_b32 v40, v42\n"
    "v_permlane16_swap_b32 v41, v43\n"
    "v_add_f32 v12, v12, v14\n"
    "v_add_f32 v13, v13, v15\n"
    "v_add_f32 v40, v40, v42\n"
    "v_add_f32 v41, v41, v43\n"
    "v_mov_b32 v14, v12\n"
    "v_mov_b32 v15, v13\n"
    "v_mov_b32 v42, v40\n"
    "v_mov_b32 v43, v41\n"
    "v_permlane32_swap_b32 v12, v14\n"
    "v_permlane32_swap_b32 v13, v15\n"
    "v_permlane32_swap_b32 v40, v42\n"
    "v_permlane32_swap_b32 v41, v43\n"
    "v_add_f32 v12, v12, v14\n"
    "v_add_f32 v13, v13, v15\n"
    "v_add_f32 v40, v40, v42\n"
    "v_add_f32 v41, v41, v43\n"
    "ds_write_b64 v10, v[12:13]\n"
    "ds_write_b64 v10, v[40:41] offset:1024\n"
    "s_mov_b64 exec, 1\n"
    "ds_add_u32 v5, v5 offset:32\n"
    "s_mov_b64 exec, -1\n"
    "s_cmp_eq_u32 s70, 1\n"
    "s_cbranch_scc0 .Lk_c_rest\n"
    "s_mov_b64 exec, 0xff\n"
    "s_mov_b32 s55, 0x8000\n"
    "s_waitcnt vmcnt(1)\n"
    "v_cmp_ne_u32 vcc, s38, v4\n"
    "s_cbranch_vccz .Lk_pd6\n"
    ".Lk_pl5:\n"
    "v_mov_b32 v4, 0\n"
    "buffer_atomic_or v4, v3, s[20:23], s69 offen sc0 sc1\n"
    "s_sub_u32 s55, s55, 1\n"
    "s_waitcnt vmcnt(0)\n"
    "v_cmp_ne_u32 vcc, s38, v4\n"
    "s_cbranch_scc1 .Lk_pd6\n"
    "s_cbranch_vccnz .Lk_pl5\n"
    ".Lk_pd6:\n"
    "s_mov_b64 exec, -1\n"
    "s_setprio 2\n"
    "buffer_load_dwordx4 v[64:67], v1, s[20:23], s67 offen sc1\n"
    "buffer_load_dwordx4 v[68:71], v1, s[20:23], s68 offen sc1\n"
    "buffer_load_dwordx4 v[72:75], v1, s[20:23], s67 offen offset:1024 sc1\n"
    "buffer_load_dwordx4 v[76:79], v1, s[20:23], s68 offen offset:1024 sc1\n"
    "ds_read_b128 v[96:99], v1\n"
    "ds_read_b128 v[100:103], v1 offset:16384\n"
    "ds_read_b128 v[104:107], v1 offset:32768\n"
    "ds_read_b128 v[108:111], v1 offset:49152\n"
    "ds_read_b128 v[112:115], v2\n"
    "s_mul_i32 s53, s59, 14336\n"
    "s_add_u32 s53, s53, 131072\n"
    "v_add_u32 v12, s53, v1\n"
    "buffer_load_dwordx4 v[80:83], v1, s[20:23], s67 offen offset:2048 sc1\n"
    "v_xor_b32 v13, 0x800, v12\n"
    "ds_read_b128 v[144:147], v12\n"
    "ds_read_b128 v[148:151], v12 offset:1024\n"
    "ds_read_b128 v[152:155], v13\n"
    "ds_read_b128 v[156:159], v13 offset:1024\n"
    "ds_read_b128 v[160:163], v12 offset:4096\n"
    "ds_read_b128 v[164:167], v12 offset:5120\n"
    "ds_read_b128 v[168:171], v13 offset:4096\n"
    "buffer_load_dwordx4 v[84:87], v1, s[20:23], s68 offen offset:2048 sc1\n"
    "ds_read_b128 v[172:175], v13 offset:5120\n"
    "ds_read_b128 v[176:179], v12 offset:8192\n"
    "ds_read_b128 v[180:183], v12 offset:9216\n"
    "ds_read_b128 v[184:187], v13 offset:8192\n"
    "ds_read_b128 v[188:191], v13 offset:9216\n"
    "s_lshl_b32 s46, s66, 11\n"
    "s_add_u32 s46, s46, 1310720\n"
    "v_lshrrev_b32 v9, 4, v0\n"
    "buffer_load_dwordx4 v[88:91], v1, s[20:23], s67 offen offset:3072 sc1\n"
    "v_lshlrev_b32 v9, 4, v9\n"
    "v_add_u32 v9, 159744, v9\n"
    "v_and_b32 v12, 0x1ff, v7\n"
    "v_bfe_u32 v13, v0, 4, 1\n"
    "v_mul_u32_u24 v13, 44032, v13\n"
    "v_add_u32 v6, v12, v13\n"
    "v_lshlrev_b32 v11, 9, v0\n"
    "v_mov_b32 v10, s38\n"
    "buffer_load_dwordx4 v[92:95], v1, s[20:23], s68 offen offset:3072 sc1\n"
    "s_mul_i32 s45, s66, 172\n"
    "s_add_u32 s45, s45, s64\n"
    "s_lshl_b32 s45, s45, 9\n"
    "s_add_u32 s45, s45, 10485760\n"
    "s_lshl_b32 s44, s62, 2\n"
    "s_add_u32 s44, s44, s39\n"
    "s_lshl_b32 s44, s44, 4\n"
    "s_add_u32 s44, s44, s40\n"
    "s_add_u32 s67, s67, 0x1000\n"
    "buffer_load_dwordx4 v[192:195], v1, s[20:23], s67 offen sc1\n"
    "s_lshl_b32 s44, s44, 5\n"
    "s_add_u32 s44, s44, s63\n"
    "s_lshl_b32 s44, s44, 2\n"
    "s_add_u32 s44, s44, 1474560\n"
    "s_lshr_b32 s54, s63, 2\n"
    "s_add_u32 s54, s54, 1\n"
    "s_lshr_b32 s54, s54, 3\n"
    "s_lshl_b32 s54, s54, 30\n"
    "s_add_u32 s68, s68, 0x1000\n"
    "buffer_load_dwordx4 v[196:199], v1, s[20:23], s68 offen sc1\n"
    "v_or_b32 v8, s54, v6\n"
    "ds_read_b128 v[128:131], v9\n"
    "ds_read_b128 v[132:135], v9 offset:64\n"
    "s_mov_b64 exec, 1\n"
    "ds_add_u32 v5, v5\n"
    "s_mov_b64 exec, -1\n"
    "s_branch .Lk_d_loop0\n"
    ".Lk_c_rest:\n"
    "s_cmp_eq_u32 s3, 7\n"
    "s_cbranch_scc0 .Lk_c_no7a\n"
    "s_setprio 3\n"
    "s_mov_b32 s55, 0x100000\n"
    ".Lk_wl7:\n"
    "ds_read_b32 v12, v5 offset:32\n"
    "s_waitcnt lgkmcnt(0)\n"
    "v_readfirstlane_b32 s50, v12\n"
    "s_cmp_ge_u32 s50, 1282048\n"
    "s_cbranch_scc1 .Lk_wd8\n"
    "s_sleep 1\n"
    "s_sub_u32 s55, s55, 1\n"
    "s_cbranch_scc0 .Lk_wl7\n"
    ".Lk_wd8:\n"
    "v_and_b32 v12, 31, v0\n"
    "v_lshlrev_b32 v12, 6, v12\n"
    "v_and_or_b32 v12, v0, 32, v12\n"
    "v_add_u32 v12, 155648, v12\n"
    "ds_read_b128 v[16:19], v12\n"
    "ds_read_b128 v[24:27], v12 offset:16\n"
    "s_lshl_b32 s53, s56, 4\n"
    "s_add_u32 s53, s53, s60\n"
    "s_lshl_b32 s53, s53, 2\n"
    "s_add_u32 s53, s53, s61\n"
    "s_lshl_b32 s53, s53, 8\n"
    "s_add_u32 s53, s53, 1310720\n"
    "v_and_b32 v13, 15, v0\n"
    "v_lshlrev_b32 v13, 4, v13\n"
    "v_bfe_u32 v14, v0, 4, 1\n"
    "v_lshl_or_b32 v13, v14, 13, v13\n"
    "v_lshrrev_b32 v14, 5, v0\n"
    "v_lshl_or_b32 v13, v14, 30, v13\n"
    "s_waitcnt lgkmcnt(1)\n"
    "v_add_f32 v16, v16, v18\n"
    "v_add_f32 v18, v17, v19\n"
    "s_waitcnt lgkmcnt(0)\n"
    "v_add_f32 v24, v24, v26\n"
    "v_add_f32 v25, v25, v27\n"
    "v_add_f32 v16, v16, v24\n"
    "v_add_f32 v18, v18, v25\n"
    "v_mov_b32 v14, v16\n"
    "s_nop 1\n"
    "v_permlane32_swap_b32 v16, v14\n"
    "v_add_f32 v16, v16, v14\n"
    "v_mov_b32 v15, v18\n"
    "s_nop 1\n"
    "v_permlane32_swap_b32 v18, v15\n"
    "v_add_f32 v18, v18, v15\n"
    "v_mov_b32 v17, s38\n"
    "v_mov_b32 v19, s38\n"
    "buffer_store_dwordx4 v[16:19], v13, s[20:23], s53 offen sc1\n"
    "s_cmp_eq_u32 s2, 0\n"
    "s_cbranch_scc0 .Lk_c_no7a\n"
    "s_mov_b64 exec, 1\n"
    "v_mov_b32 v24, s36\n"
    "v_mov_b32 v25, s37\n"
    "buffer_atomic_swap_x2 v[24:25], v1, s[20:23], 0 offen sc1\n"
    "s_mov_b64 exec, -1\n"
    ".Lk_c_no7a:\n"
    "s_setprio 0\n"
    "s_cmp_eq_u32 s65, 1\n"
    "s_cbranch_scc1 .Lk_d_act1\n"
    "ds_read_b128 v[96:99], v1\n"
    "ds_read_b128 v[100:103], v1 offset:16384\n"
    "ds_read_b128 v[104:107], v1 offset:32768\n"
    "ds_read_b128 v[108:111], v1 offset:49152\n"
    "ds_read_b128 v[112:115], v2\n"
    "s_mul_i32 s53, s59, 14336\n"
    "s_add_u32 s53, s53, 131072\n"
    "v_add_u32 v12, s53, v1\n"
    "v_xor_b32 v13, 0x800, v12\n"
    "ds_read_b128 v[144:147], v12\n"
    "ds_read_b128 v[148:151], v12 offset:1024\n"
    "ds_read_b128 v[152:155], v13\n"
    "ds_read_b128 v[156:159], v13 offset:1024\n"
    "ds_read_b128 v[160:163], v12 offset:4096\n"
    "ds_read_b128 v[164:167], v12 offset:5120\n"
    "ds_read_b128 v[168:171], v13 offset:4096\n"
    "ds_read_b128 v[172:175], v13 offset:5120\n"
    "ds_read_b128 v[176:179], v12 offset:8192\n"
    "ds_read_b128 v[180:183], v12 offset:9216\n"
    "ds_read_b128 v[184:187], v13 offset:8192\n"
    "ds_read_b128 v[188:191], v13 offset:9216\n"
    "s_lshl_b32 s46, s66, 11\n"
    "s_add_u32 s46, s46, 1310720\n"
    "v_lshrrev_b32 v9, 4, v0\n"
    "v_lshlrev_b32 v9, 4, v9\n"
    "v_add_u32 v9, 159744, v9\n"
    "v_and_b32 v12, 0x1ff, v7\n"
    "v_bfe_u32 v13, v0, 4, 1\n"
    "v_mul_u32_u24 v13, 44032, v13\n"
    "v_add_u32 v6, v12, v13\n"
    "v_lshlrev_b32 v11, 9, v0\n"
    "v_mov_b32 v10, s38\n"
    "s_mul_i32 s45, s66, 172\n"
    "s_add_u32 s45, s45, s64\n"
    "s_lshl_b32 s45, s45, 9\n"
    "s_add_u32 s45, s45, 10485760\n"
    "s_lshl_b32 s44, s62, 2\n"
    "s_add_u32 s44, s44, s39\n"
    "s_lshl_b32 s44, s44, 4\n"
    "s_add_u32 s44, s44, s40\n"
    "s_lshl_b32 s44, s44, 5\n"
    "s_add_u32 s44, s44, s63\n"
    "s_lshl_b32 s44, s44, 2\n"
    "s_add_u32 s44, s44, 1474560\n"
    "s_lshr_b32 s54, s63, 2\n"
    "s_add_u32 s54, s54, 1\n"
    "s_lshr_b32 s54, s54, 3\n"
    "s_lshl_b32 s54, s54, 30\n"
    "v_or_b32 v8, s54, v6\n"
    "ds_read_b128 v[128:131], v9\n"
    "ds_read_b128 v[132:135], v9 offset:64\n"
    "s_mov_b64 exec, 1\n"
    "ds_add_u32 v5, v5\n"
    "s_mov_b64 exec, -1\n"
    "s_mov_b64 exec, 1\n"
    "ds_add_u32 v5, v5 offset:12\n"
    "s_mov_b64 exec, -1\n"
    "s_mov_b64 exec, 1\n"
    "ds_add_u32 v5, v5 offset:16\n"
    "s_mov_b64 exec, -1\n"
    "s_mov_b64 exec, 1\n"
    "ds_add_u32 v5, v5 offset:20\n"
    "s_mov_b64 exec, -1\n"
    "s_cmp_lt_u32 s3, 4\n"
    "s_cbranch_scc1 .Lk_d_post0\n"
    "s_branch .Lk_d_post1\n"
    ".Lk_d_act1:\n"
    "s_mov_b64 exec, 0xff\n"
    "v_mov_b32 v14, 0\n"
    "buffer_atomic_or v14, v3, s[20:23], s69 offen sc0 sc1\n"
    "s_mov_b64 exec, -1\n"
    "ds_read_b128 v[96:99], v1\n"
    "ds_read_b128 v[100:103], v1 offset:16384\n"
    "ds_read_b128 v[104:107], v1 offset:32768\n"
    "ds_read_b128 v[108:111], v1 offset:49152\n"
    "ds_read_b128 v[112:115], v2\n"
    "s_mul_i32 s53, s59, 14336\n"
    "s_add_u32 s53, s53, 131072\n"
    "v_add_u32 v12, s53, v1\n"
    "v_xor_b32 v13, 0x800, v12\n"
    "ds_read_b128 v[144:147], v12\n"
    "ds_read_b128 v[148:151], v12 offset:1024\n"
    "ds_read_b128 v[152:155], v13\n"
    "ds_read_b128 v[156:159], v13 offset:1024\n"
    "ds_read_b128 v[160:163], v12 offset:4096\n"
    "ds_read_b128 v[164:167], v12 offset:5120\n"
    "ds_read_b128 v[168:171], v13 offset:4096\n"
    "ds_read_b128 v[172:175], v13 offset:5120\n"
    "ds_read_b128 v[176:179], v12 offset:8192\n"
    "ds_read_b128 v[180:183], v12 offset:9216\n"
    "ds_read_b128 v[184:187], v13 offset:8192\n"
    "ds_read_b128 v[188:191], v13 offset:9216\n"
    "s_lshl_b32 s46, s66, 11\n"
    "s_add_u32 s46, s46, 1310720\n"
    "v_lshrrev_b32 v9, 4, v0\n"
    "v_lshlrev_b32 v9, 4, v9\n"
    "v_add_u32 v9, 159744, v9\n"
    "v_and_b32 v12, 0x1ff, v7\n"
    "v_bfe_u32 v13, v0, 4, 1\n"
    "v_mul_u32_u24 v13, 44032, v13\n"
    "v_add_u32 v6, v12, v13\n"
    "v_lshlrev_b32 v11, 9, v0\n"
    "v_mov_b32 v10, s38\n"
    "s_mul_i32 s45, s66, 172\n"
    "s_add_u32 s45, s45, s64\n"
    "s_lshl_b32 s45, s45, 9\n"
    "s_add_u32 s45, s45, 10485760\n"
    "s_lshl_b32 s44, s62, 2\n"
    "s_add_u32 s44, s44, s39\n"
    "s_lshl_b32 s44, s44, 4\n"
    "s_add_u32 s44, s44, s40\n"
    "s_lshl_b32 s44, s44, 5\n"
    "s_add_u32 s44, s44, s63\n"
    "s_lshl_b32 s44, s44, 2\n"
    "s_add_u32 s44, s44, 1474560\n"
    "s_lshr_b32 s54, s63, 2\n"
    "s_add_u32 s54, s54, 1\n"
    "s_lshr_b32 s54, s54, 3\n"
    "s_lshl_b32 s54, s54, 30\n"
    "v_or_b32 v8, s54, v6\n"
    "ds_read_b128 v[128:131], v9\n"
    "ds_read_b128 v[132:135], v9 offset:64\n"
    "s_mov_b64 exec, 1\n"
    "ds_add_u32 v5, v5\n"
    "s_mov_b64 exec, -1\n"
    "s_mov_b64 exec, 0xff\n"
    "s_mov_b32 s55, 0x8000\n"
    "s_waitcnt vmcnt(0)\n"
    "v_cmp_ne_u32 vcc, s38, v14\n"
    "s_cbranch_vccz .Lk_pd10\n"
    ".Lk_pl9:\n"
    "v_mov_b32 v14, 0\n"
    "buffer_atomic_or v14, v3, s[20:23], s69 offen sc0 sc1\n"
    "s_sub_u32 s55, s55, 1\n"
    "s_waitcnt vmcnt(0)\n"
    "v_cmp_ne_u32 vcc, s38, v14\n"
    "s_cbranch_scc1 .Lk_pd10\n"
    "s_cbranch_vccnz .Lk_pl9\n"
    ".Lk_pd10:\n"
    "s_mov_b64 exec, -1\n"
    "buffer_load_dwordx4 v[64:67], v1, s[20:23], s67 offen sc1\n"
    "buffer_load_dwordx4 v[68:71], v1, s[20:23], s68 offen sc1\n"
    "buffer_load_dwordx4 v[72:75], v1, s[20:23], s67 offen offset:1024 sc1\n"
    "buffer_load_dwordx4 v[76:79], v1, s[20:23], s68 offen offset:1024 sc1\n"
    "buffer_load_dwordx4 v[80:83], v1, s[20:23], s67 offen offset:2048 sc1\n"
    "buffer_load_dwordx4 v[84:87], v1, s[20:23], s68 offen offset:2048 sc1\n"
    "buffer_load_dwordx4 v[88:91], v1, s[20:23], s67 offen offset:3072 sc1\n"
    "buffer_load_dwordx4 v[92:95], v1, s[20:23], s68 offen offset:3072 sc1\n"
    "s_add_u32 s67, s67, 0x1000\n"
    "buffer_load_dwordx4 v[192:195], v1, s[20:23], s67 offen sc1\n"
    "s_add_u32 s68, s68, 0x1000\n"
    "buffer_load_dwordx4 v[196:199], v1, s[20:23], s68 offen sc1\n"
    "s_setprio 1\n"
    ".Lk_d_loop1:\n"
    "s_waitcnt vmcnt(9) lgkmcnt(4)\n"
    "v_mfma_f32_16x16x32_bf16 v[16:19], v[96:99], v[64:67], 0\n"
    "ds_read_b128 v[116:119], v2 offset:16384\n"
    "s_waitcnt vmcnt(8)\n"
    "v_mfma_f32_16x16x32_bf16 v[20:23], v[96:99], v[68:71], 0\n"
    "buffer_load_dwordx4 v[200:203], v1, s[20:23], s67 offen offset:1024 sc1\n"
    "s_waitcnt lgkmcnt(4)\n"
    "v_mfma_f32_16x16x32_bf16 v[24:27], v[100:103], v[64:67], 0\n"
    "ds_read_b128 v[96:99], v1 offset:1024\n"
    "v_mfma_f32_16x16x32_bf16 v[28:31], v[100:103], v[68:71], 0\n"
    "buffer_load_dwordx4 v[204:207], v1, s[20:23], s68 offen offset:1024 sc1\n"
    "s_waitcnt lgkmcnt(4)\n"
    "v_mfma_f32_16x16x32_bf16 v[32:35], v[104:107], v[64:67], 0\n"
    "ds_read_b128 v[100:103], v1 offset:17408\n"
    "v_mfma_f32_16x16x32_bf16 v[36:39], v[104:107], v[68:71], 0\n"
    "buffer_load_dwordx4 v[208:211], v1, s[20:23], s67 offen offset:2048 sc1\n"
    "s_waitcnt lgkmcnt(4)\n"
    "v_mfma_f32_16x16x32_bf16 v[40:43], v[108:111], v[64:67], 0\n"
    "ds_read_b128 v[104:107], v1 offset:33792\n"
    "v_mfma_f32_16x16x32_bf16 v[44:47], v[108:111], v[68:71], 0\n"
    "buffer_load_dwordx4 v[212:215], v1, s[20:23], s68 offen offset:2048 sc1\n"
    "s_waitcnt lgkmcnt(4)\n"
    "v_mfma_f32_16x16x32_bf16 v[48:51], v[112:115], v[64:67], 0\n"
    "ds_read_b128 v[108:111], v1 offset:50176\n"
    "v_mfma_f32_16x16x32_bf16 v[52:55], v[112:115], v[68:71], 0\n"
    "buffer_load_dwordx4 v[216:219], v1, s[20:23], s67 offen offset:3072 sc1\n"
    "buffer_load_dwordx4 v[120:123], v1, s[20:23], s46 offen sc1\n"
    "s_waitcnt lgkmcnt(4)\n"
    "v_mfma_f32_16x16x32_bf16 v[56:59], v[116:119], v[64:67], 0\n"
    "ds_read_b128 v[112:115], v2 offset:1024\n"
    "v_mfma_f32_16x16x32_bf16 v[60:63], v[116:119], v[68:71], 0\n"
    "buffer_load_dwordx4 v[220:223], v1, s[20:23], s68 offen offset:3072 sc1\n"
    "s_waitcnt vmcnt(13) lgkmcnt(4)\n"
    "v_mfma_f32_16x16x32_bf16 v[16:19], v[96:99], v[72:75], v[16:19]\n"
    "ds_read_b128 v[116:119], v2 offset:17408\n"
    "v_mfma_f32_16x16x32_bf16 v[20:23], v[96:99], v[76:79], v[20:23]\n"
    "s_add_u32 s67, s67, 0x1000\n"
    "buffer_load_dwordx4 v[224:227], v1, s[20:23], s67 offen sc1\n"
    "buffer_load_dwordx4 v[124:127], v1, s[20:23], s46 offen offset:1024 sc1\n"
    "s_waitcnt lgkmcnt(4)\n"
    "v_mfma_f32_16x16x32_bf16 v[24:27], v[100:103], v[72:75], v[24:27]\n"
    "ds_read_b128 v[96:99], v1 offset:2048\n"
    "v_mfma_f32_16x16x32_bf16 v[28:31], v[100:103], v[76:79], v[28:31]\n"
    "s_add_u32 s68, s68, 0x1000\n"
    "buffer_load_dwordx4 v[228:231], v1, s[20:23], s68 offen sc1\n"
    "s_waitcnt lgkmcnt(4)\n"
    "v_mfma_f32_16x16x32_bf16 v[32:35], v[104:107], v[72:75], v[32:35]\n"
    "ds_read_b128 v[100:103], v1 offset:18432\n"
    "v_mfma_f32_16x16x32_bf16 v[36:39], v[104:107], v[76:79], v[36:39]\n"
    "buffer_load_dwordx4 v[232:235], v1, s[20:23], s67 offen offset:1024 sc1\n"
    "s_waitcnt lgkmcnt(4)\n"
    "v_mfma_f32_16x16x32_bf16 v[40:43], v[108:111], v[72:75], v[40:43]\n"
    "ds_read_b128 v[104:107], v1 offset:34816\n"
    "v_mfma_f32_16x16x32_bf16 v[44:47], v[108:111], v[76:79], v[44:47]\n"
    "buffer_load_dwordx4 v[236:239], v1, s[20:23], s68 offen offset:1024 sc1\n"
    "s_waitcnt lgkmcnt(4)\n"
    "v_mfma_f32_16x16x32_bf16 v[48:51], v[112:115], v[72:75], v[48:51]\n"
    "ds_read_b128 v[108:111], v1 offset:51200\n"
    "v_mfma_f32_16x16x32_bf16 v[52:55], v[112:115], v[76:79], v[52:55]\n"
    "buffer_load_dwordx4 v[240:243], v1, s[20:23], s67 offen offset:2048 sc1\n"
    "s_waitcnt lgkmcnt(4)\n"
    "v_mfma_f32_16x16x32_bf16 v[56:59], v[116:119], v[72:75], v[56:59]\n"
    "ds_read_b128 v[112:115], v2 offset:2048\n"
    "v_mfma_f32_16x16x32_bf16 v[60:63], v[116:119], v[76:79], v[60:63]\n"
    "buffer_load_dwordx4 v[244:247], v1, s[20:23], s68 offen offset:2048 sc1\n"
    "s_waitcnt vmcnt(18) lgkmcnt(4)\n"
    "v_mfma_f32_16x16x32_bf16 v[16:19], v[96:99], v[80:83], v[16:19]\n"
    "ds_read_b128 v[116:119], v2 offset:18432\n"
    "v_mfma_f32_16x16x32_bf16 v[20:23], v[96:99], v[84:87], v[20:23]\n"
    "buffer_load_dwordx4 v[248:251], v1, s[20:23], s67 offen offset:3072 sc1\n"
    "s_waitcnt lgkmcnt(4)\n"
    "v_mfma_f32_16x16x32_bf16 v[24:27], v[100:103], v[80:83], v[24:27]\n"
    "ds_read_b128 v[96:99], v1 offset:3072\n"
    "v_mfma_f32_16x16x32_bf16 v[28:31], v[100:103], v[84:87], v[28:31]\n"
    "buffer_load_dwordx4 v[252:255], v1, s[20:23], s68 offen offset:3072 sc1\n"
    "s_waitcnt lgkmcnt(4)\n"
    "v_mfma_f32_16x16x32_bf16 v[32:35], v[104:107], v[80:83], v[32:35]\n"
    "ds_read_b128 v[100:103], v1 offset:19456\n"
    "v_mfma_f32_16x16x32_bf16 v[36:39], v[104:107], v[84:87], v[36:39]\n"
    "s_add_u32 s67, s67, 0x1000\n"
    "buffer_load_dwordx4 v[64:67], v1, s[20:23], s67 offen sc1\n"
    "s_waitcnt lgkmcnt(4)\n"
    "v_mfma_f32_16x16x32_bf16 v[40:43], v[108:111], v[80:83], v[40:43]\n"
    "ds_read_b128 v[104:107], v1 offset:35840\n"
    "v_mfma_f32_16x16x32_bf16 v[44:47], v[108:111], v[84:87], v[44:47]\n"
    "s_add_u32 s68, s68, 0x1000\n"
    "buffer_load_dwordx4 v[68:71], v1, s[20:23], s68 offen sc1\n"
    "s_waitcnt lgkmcnt(4)\n"
    "v_mfma_f32_16x16x32_bf16 v[48:51], v[112:115], v[80:83], v[48:51]\n"
    "ds_read_b128 v[108:111], v1 offset:52224\n"
    "v_mfma_f32_16x16x32_bf16 v[52:55], v[112:115], v[84:87], v[52:55]\n"
    "buffer_load_dwordx4 v[72:75], v1, s[20:23], s67 offen offset:1024 sc1\n"
    "s_waitcnt lgkmcnt(4)\n"
    "v_mfma_f32_16x16x32_bf16 v[56:59], v[116:119], v[80:83], v[56:59]\n"
    "ds_read_b128 v[112:115], v2 offset:3072\n"
    "v_mfma_f32_16x16x32_bf16 v[60:63], v[116:119], v[84:87], v[60:63]\n"
    "buffer_load_dwordx4 v[76:79], v1, s[20:23], s68 offen offset:1024 sc1\n"
    "s_waitcnt vmcnt(22) lgkmcnt(4)\n"
    "v_mfma_f32_16x16x32_bf16 v[16:19], v[96:99], v[88:91], v[16:19]\n"
    "ds_read_b128 v[116:119], v2 offset:19456\n"
    "v_mfma_f32_16x16x32_bf16 v[20:23], v[96:99], v[92:95], v[20:23]\n"
    "buffer_load_dwordx4 v[80:83], v1, s[20:23], s67 offen offset:2048 sc1\n"
    "s_waitcnt lgkmcnt(4)\n"
    "v_mfma_f32_16x16x32_bf16 v[24:27], v[100:103], v[88:91], v[24:27]\n"
    "ds_read_b128 v[96:99], v1 offset:4096\n"
    "v_mfma_f32_16x16x32_bf16 v[28:31], v[100:103], v[92:95], v[28:31]\n"
    "buffer_load_dwordx4 v[84:87], v1, s[20:23], s68 offen offset:2048 sc1\n"
    "s_waitcnt lgkmcnt(4)\n"
    "v_mfma_f32_16x16x32_bf16 v[32:35], v[104:107], v[88:91], v[32:35]\n"
    "ds_read_b128 v[100:103], v1 offset:20480\n"
    "v_mfma_f32_16x16x32_bf16 v[36:39], v[104:107], v[92:95], v[36:39]\n"
    "s_waitcnt lgkmcnt(4)\n"
    "v_mfma_f32_16x16x32_bf16 v[40:43], v[108:111], v[88:91], v[40:43]\n"
    "ds_read_b128 v[104:107], v1 offset:5120\n"
    "v_mfma_f32_16x16x32_bf16 v[44:47], v[108:111], v[92:95], v[44:47]\n"
    "s_waitcnt lgkmcnt(4)\n"
    "v_mfma_f32_16x16x32_bf16 v[48:51], v[112:115], v[88:91], v[48:51]\n"
    "ds_read_b128 v[108:111], v1 offset:21504\n"
    "v_mfma_f32_16x16x32_bf16 v[52:55], v[112:115], v[92:95], v[52:55]\n"
    "s_waitcnt lgkmcnt(4)\n"
    "v_mfma_f32_16x16x32_bf16 v[56:59], v[116:119], v[88:91], v[56:59]\n"
    "ds_read_b128 v[112:115], v1 offset:6144\n"
    "v_mfma_f32_16x16x32_bf16 v[60:63], v[116:119], v[92:95], v[60:63]\n"
    "buffer_load_dwordx4 v[88:91], v1, s[20:23], s67 offen offset:3072 sc1\n"
    "s_waitcnt vmcnt(23) lgkmcnt(4)\n"
    "v_mfma_f32_16x16x32_bf16 v[16:19], v[96:99], v[192:195], v[16:19]\n"
    "ds_read_b128 v[116:119], v1 offset:22528\n"
    "v_mfma_f32_16x16x32_bf16 v[20:23], v[96:99], v[196:199], v[20:23]\n"
    "buffer_load_dwordx4 v[92:95], v1, s[20:23], s68 offen offset:3072 sc1\n"
    "s_waitcnt lgkmcnt(4)\n"
    "v_mfma_f32_16x16x32_bf16 v[24:27], v[100:103], v[192:195], v[24:27]\n"
    "ds_read_b128 v[96:99], v1 offset:7168\n"
    "v_mfma_f32_16x16x32_bf16 v[28:31], v[100:103], v[196:199], v[28:31]\n"
    "s_waitcnt vmcnt(22) lgkmcnt(4)\n"
    "v_mfma_f32_16x16x32_bf16 v[16:19], v[104:107], v[200:203], v[16:19]\n"
    "ds_read_b128 v[100:103], v1 offset:23552\n"
    "v_mfma_f32_16x16x32_bf16 v[20:23], v[104:107], v[204:207], v[20:23]\n"
    "s_waitcnt lgkmcnt(4)\n"
    "v_mfma_f32_16x16x32_bf16 v[24:27], v[108:111], v[200:203], v[24:27]\n"
    "ds_read_b128 v[104:107], v1 offset:8192\n"
    "v_mfma_f32_16x16x32_bf16 v[28:31], v[108:111], v[204:207], v[28:31]\n"
    "s_waitcnt vmcnt(20) lgkmcnt(4)\n"
    "v_mfma_f32_16x16x32_bf16 v[16:19], v[112:115], v[208:211], v[16:19]\n"
    "ds_read_b128 v[108:111], v1 offset:24576\n"
    "v_mfma_f32_16x16x32_bf16 v[20:23], v[112:115], v[212:215], v[20:23]\n"
    "s_waitcnt lgkmcnt(4)\n"
    "v_mfma_f32_16x16x32_bf16 v[24:27], v[116:119], v[208:211], v[24:27]\n"
    "ds_read_b128 v[112:115], v1 offset:9216\n"
    "v_mfma_f32_16x16x32_bf16 v[28:31], v[116:119], v[212:215], v[28:31]\n"
    "s_waitcnt vmcnt(17) lgkmcnt(4)\n"
    "v_mfma_f32_16x16x32_bf16 v[16:19], v[96:99], v[216:219], v[16:19]\n"
    "ds_read_b128 v[116:119], v1 offset:25600\n"
    "v_mfma_f32_16x16x32_bf16 v[20:23], v[96:99], v[220:223], v[20:23]\n"
    "s_waitcnt lgkmcnt(4)\n"
    "v_mfma_f32_16x16x32_bf16 v[24:27], v[100:103], v[216:219], v[24:27]\n"
    "ds_read_b128 v[96:99], v1 offset:10240\n"
    "v_mfma_f32_16x16x32_bf16 v[28:31], v[100:103], v[220:223], v[28:31]\n"
    "s_waitcnt vmcnt(14) lgkmcnt(4)\n"
    "s_mov_b32 s55, 0x8000\n"
    ".Lk_sgl1:\n"
    "v_cmp_ne_u32 vcc, s38, v121\n"
    "v_cmp_ne_u32 s[50:51], s38, v123\n"
    "s_or_b64 vcc, vcc, s[50:51]\n"
    "v_cmp_ne_u32 s[50:51], s38, v125\n"
    "s_or_b64 vcc, vcc, s[50:51]\n"
    "v_cmp_ne_u32 s[50:51], s38, v127\n"
    "s_or_b64 vcc, vcc, s[50:51]\n"
    "s_cbranch_scc0 .Lk_sgd1\n"
    "s_sub_u32 s55, s55, 1\n"
    "s_cbranch_scc1 .Lk_sgd1\n"
    "s_sleep 4\n"
    "buffer_load_dwordx4 v[120:123], v1, s[20:23], s46 offen sc1\n"
    "buffer_load_dwordx4 v[124:127], v1, s[20:23], s46 offen offset:1024 sc1\n"
    "s_waitcnt vmcnt(0)\n"
    "s_branch .Lk_sgl1\n"
    ".Lk_sgd1:\n"
    "v_mfma_f32_16x16x32_bf16 v[16:19], v[104:107], v[224:227], v[16:19]\n"
    "ds_read_b128 v[100:103], v1 offset:26624\n"
    "v_mov_b32 v121, v120\n"
    "v_mov_b32 v123, v122\n"
    "v_mfma_f32_16x16x32_bf16 v[20:23], v[104:107], v[228:231], v[20:23]\n"
    "s_waitcnt lgkmcnt(4)\n"
    "v_mov_b32 v125, v124\n"
    "v_mov_b32 v127, v126\n"
    "v_mfma_f32_16x16x32_bf16 v[24:27], v[108:111], v[224:227], v[24:27]\n"
    "ds_read_b128 v[104:107], v1 offset:11264\n"
    "v_permlane16_swap_b32 v120, v121\n"
    "v_permlane16_swap_b32 v122, v123\n"
    "v_mfma_f32_16x16x32_bf16 v[28:31], v[108:111], v[228:231], v[28:31]\n"
    "s_waitcnt vmcnt(12) lgkmcnt(4)\n"
    "v_permlane16_swap_b32 v124, v125\n"
    "v_permlane16_swap_b32 v126, v127\n"
    "v_mfma_f32_16x16x32_bf16 v[16:19], v[112:115], v[232:235], v[16:19]\n"
    "ds_read_b128 v[108:111], v1 offset:27648\n"
    "v_add_f32 v120, v120, v121\n"
    "v_add_f32 v122, v122, v123\n"
    "v_mfma_f32_16x16x32_bf16 v[20:23], v[112:115], v[236:239], v[20:23]\n"
    "s_waitcnt lgkmcnt(4)\n"
    "v_add_f32 v124, v124, v125\n"
    "v_add_f32 v126, v126, v127\n"
    "v_mfma_f32_16x16x32_bf16 v[24:27], v[116:119], v[232:235], v[24:27]\n"
    "ds_read_b128 v[112:115], v1 offset:12288\n"
    "v_mov_b32 v121, v120\n"
    "v_mov_b32 v123, v122\n"
    "v_mfma_f32_16x16x32_bf16 v[28:31], v[116:119], v[236:239], v[28:31]\n"
    "s_waitcnt vmcnt(10) lgkmcnt(4)\n"
    "v_mov_b32 v125, v124\n"
    "v_mov_b32 v127, v126\n"
    "v_mfma_f32_16x16x32_bf16 v[16:19], v[96:99], v[240:243], v[16:19]\n"
    "ds_read_b128 v[116:119], v1 offset:28672\n"
    "v_permlane32_swap_b32 v120, v121\n"
    "v_permlane32_swap_b32 v122, v123\n"
    "v_mfma_f32_16x16x32_bf16 v[20:23], v[96:99], v[244:247], v[20:23]\n"
    "s_waitcnt lgkmcnt(4)\n"
    "v_permlane32_swap_b32 v124, v125\n"
    "v_permlane32_swap_b32 v126, v127\n"
    "v_mfma_f32_16x16x32_bf16 v[24:27], v[100:103], v[240:243], v[24:27]\n"
    "ds_read_b128 v[96:99], v1 offset:13312\n"
    "v_add_f32 v120, v120, v121\n"
    "v_add_f32 v122, v122, v123\n"
    "v_mfma_f32_16x16x32_bf16 v[28:31], v[100:103], v[244:247], v[28:31]\n"
    "s_waitcnt vmcnt(8) lgkmcnt(4)\n"
    "v_add_f32 v124, v124, v125\n"
    "v_add_f32 v126, v126, v127\n"
    "v_mfma_f32_16x16x32_bf16 v[16:19], v[104:107], v[248:251], v[16:19]\n"
    "ds_read_b128 v[100:103], v1 offset:29696\n"
    "v_mul_f32 v120, 0xbb000000, v120\n"
    "v_mul_f32 v122, 0x3b000000, v122\n"
    "v_mfma_f32_16x16x32_bf16 v[20:23], v[104:107], v[252:255], v[20:23]\n"
    "s_waitcnt lgkmcnt(4)\n"
    "v_mul_f32 v124, 0xbb000000, v124\n"
    "v_mul_f32 v126, 0x3b000000, v126\n"
    "v_mfma_f32_16x16x32_bf16 v[24:27], v[108:111], v[248:251], v[24:27]\n"
    "ds_read_b128 v[104:107], v1 offset:14336\n"
    "v_fma_f32 v122, -v120, v120, v122\n"
    "v_fma_f32 v126, -v124, v124, v126\n"
    "v_mfma_f32_16x16x32_bf16 v[28:31], v[108:111], v[252:255], v[28:31]\n"
    "s_waitcnt vmcnt(6) lgkmcnt(4)\n"
    "v_max_f32 v122, 0, v122\n"
    "v_max_f32 v126, 0, v126\n"
    "v_mfma_f32_16x16x32_bf16 v[16:19], v[112:115], v[64:67], v[16:19]\n"
    "ds_read_b128 v[108:111], v1 offset:30720\n"
    "v_add_f32 v122, 0x3727c5ac, v122\n"
    "v_add_f32 v126, 0x3727c5ac, v126\n"
    "v_mfma_f32_16x16x32_bf16 v[20:23], v[112:115], v[68:71], v[20:23]\n"
    "s_waitcnt lgkmcnt(4)\n"
    "v_rsq_f32 v121, v122\n"
    "v_mfma_f32_16x16x32_bf16 v[24:27], v[116:119], v[64:67], v[24:27]\n"
    "ds_read_b128 v[112:115], v1 offset:15360\n"
    "v_rsq_f32 v125, v126\n"
    "v_mfma_f32_16x16x32_bf16 v[28:31], v[116:119], v[68:71], v[28:31]\n"
    "s_waitcnt vmcnt(4) lgkmcnt(4)\n"
    "v_mul_f32 v121, 0xbfb8aa3b, v121\n"
    "v_mul_f32 v125, 0xbfb8aa3b, v125\n"
    "v_mfma_f32_16x16x32_bf16 v[16:19], v[96:99], v[72:75], v[16:19]\n"
    "ds_read_b128 v[116:119], v1 offset:31744\n"
    "v_mfma_f32_16x16x32_bf16 v[20:23], v[96:99], v[76:79], v[20:23]\n"
    "s_waitcnt lgkmcnt(4)\n"
    "v_mfma_f32_16x16x32_bf16 v[24:27], v[100:103], v[72:75], v[24:27]\n"
    "ds_read_b128 v[96:99], v1 offset:36864\n"
    "v_mfma_f32_16x16x32_bf16 v[28:31], v[100:103], v[76:79], v[28:31]\n"
    "s_waitcnt vmcnt(2) lgkmcnt(4)\n"
    "v_mfma_f32_16x16x32_bf16 v[16:19], v[104:107], v[80:83], v[16:19]\n"
    "ds_read_b128 v[100:103], v1 offset:53248\n"
    "v_mfma_f32_16x16x32_bf16 v[20:23], v[104:107], v[84:87], v[20:23]\n"
    "s_waitcnt lgkmcnt(4)\n"
    "v_mfma_f32_16x16x32_bf16 v[24:27], v[108:111], v[80:83], v[24:27]\n"
    "ds_read_b128 v[104:107], v1 offset:37888\n"
    "v_mfma_f32_16x16x32_bf16 v[28:31], v[108:111], v[84:87], v[28:31]\n"
    "s_waitcnt vmcnt(0) lgkmcnt(4)\n"
    "v_mfma_f32_16x16x32_bf16 v[16:19], v[112:115], v[88:91], v[16:19]\n"
    "ds_read_b128 v[108:111], v1 offset:54272\n"
    "v_mfma_f32_16x16x32_bf16 v[20:23], v[112:115], v[92:95], v[20:23]\n"
    "s_waitcnt lgkmcnt(4)\n"
    "v_mfma_f32_16x16x32_bf16 v[24:27], v[116:119], v[88:91], v[24:27]\n"
    "ds_read_b128 v[112:115], v1 offset:38912\n"
    "v_mfma_f32_16x16x32_bf16 v[28:31], v[116:119], v[92:95], v[28:31]\n"
    "s_waitcnt lgkmcnt(4)\n"
    "v_mfma_f32_16x16x32_bf16 v[32:35], v[96:99], v[192:195], v[32:35]\n"
    "ds_read_b128 v[116:119], v1 offset:55296\n"
    "v_mfma_f32_16x16x32_bf16 v[36:39], v[96:99], v[196:199], v[36:39]\n"
    "s_waitcnt lgkmcnt(4)\n"
    "v_mfma_f32_16x16x32_bf16 v[40:43], v[100:103], v[192:195], v[40:43]\n"
    "ds_read_b128 v[96:99], v1 offset:39936\n"
    "v_mfma_f32_16x16x32_bf16 v[44:47], v[100:103], v[196:199], v[44:47]\n"
    "s_waitcnt lgkmcnt(4)\n"
    "v_cmp_u_f32 vcc, v16, v20\n"
    "s_cbranch_vccnz .Lk_d_slow1\n"
    ".Lk_d_back1:\n"
    "s_mov_b64 exec, 1\n"
    "ds_add_u32 v5, v5 offset:12\n"
    "s_mov_b64 exec, -1\n"
    "v_mfma_f32_16x16x32_bf16 v[32:35], v[104:107], v[200:203], v[32:35]\n"
    "ds_read_b128 v[100:103], v1 offset:56320\n"
    "v_fmac_f32 v16, v128, v120\n"
    "v_fmac_f32 v17, v129, v120\n"
    "v_mfma_f32_16x16x32_bf16 v[36:39], v[104:107], v[204:207], v[36:39]\n"
    "s_waitcnt lgkmcnt(5)\n"
    "v_fmac_f32 v18, v130, v120\n"
    "v_fmac_f32 v19, v131, v120\n"
    "v_mfma_f32_16x16x32_bf16 v[40:43], v[108:111], v[200:203], v[40:43]\n"
    "ds_read_b128 v[104:107], v1 offset:40960\n"
    "v_fmac_f32 v20, v128, v124\n"
    "v_fmac_f32 v21, v129, v124\n"
    "v_mfma_f32_16x16x32_bf16 v[44:47], v[108:111], v[204:207], v[44:47]\n"
    "s_waitcnt lgkmcnt(5)\n"
    "v_fmac_f32 v22, v130, v124\n"
    "v_fmac_f32 v23, v131, v124\n"
    "v_mfma_f32_16x16x32_bf16 v[32:35], v[112:115], v[208:211], v[32:35]\n"
    "ds_read_b128 v[108:111], v1 offset:57344\n"
    "v_mul_f32 v136, v16, v121\n"
    "v_mul_f32 v137, v17, v121\n"
    "v_mfma_f32_16x16x32_bf16 v[36:39], v[112:115], v[212:215], v[36:39]\n"
    "s_waitcnt lgkmcnt(5)\n"
    "v_mul_f32 v138, v18, v121\n"
    "v_mul_f32 v139, v19, v121\n"
    "v_mfma_f32_16x16x32_bf16 v[40:43], v[116:119], v[208:211], v[40:43]\n"
    "ds_read_b128 v[112:115], v1 offset:41984\n"
    "v_mul_f32 v140, v20, v125\n"
    "v_mul_f32 v141, v21, v125\n"
    "v_mfma_f32_16x16x32_bf16 v[44:47], v[116:119], v[212:215], v[44:47]\n"
    "s_waitcnt lgkmcnt(5)\n"
    "v_mul_f32 v142, v22, v125\n"
    "v_mul_f32 v143, v23, v125\n"
    "v_mfma_f32_16x16x32_bf16 v[32:35], v[96:99], v[216:219], v[32:35]\n"
    "ds_read_b128 v[116:119], v1 offset:58368\n"
    "v_exp_f32 v136, v136\n"
    "v_mfma_f32_16x16x32_bf16 v[36:39], v[96:99], v[220:223], v[36:39]\n"
    "s_waitcnt lgkmcnt(4)\n"
    "v_exp_f32 v137, v137\n"
    "v_mfma_f32_16x16x32_bf16 v[40:43], v[100:103], v[216:219], v[40:43]\n"
    "ds_read_b128 v[96:99], v1 offset:43008\n"
    "v_exp_f32 v138, v138\n"
    "v_mfma_f32_16x16x32_bf16 v[44:47], v[100:103], v[220:223], v[44:47]\n"
    "s_waitcnt lgkmcnt(4)\n"
    "v_exp_f32 v139, v139\n"
    "v_mfma_f32_16x16x32_bf16 v[32:35], v[104:107], v[224:227], v[32:35]\n"
    "ds_read_b128 v[100:103], v1 offset:59392\n"
    "v_exp_f32 v140, v140\n"
    "v_mfma_f32_16x16x32_bf16 v[36:39], v[104:107], v[228:231], v[36:39]\n"
    "s_waitcnt lgkmcnt(4)\n"
    "v_exp_f32 v141, v141\n"
    "v_mfma_f32_16x16x32_bf16 v[40:43], v[108:111], v[224:227], v[40:43]\n"
    "ds_read_b128 v[104:107], v1 offset:44032\n"
    "v_exp_f32 v142, v142\n"
    "v_mfma_f32_16x16x32_bf16 v[44:47], v[108:111], v[228:231], v[44:47]\n"
    "s_waitcnt lgkmcnt(4)\n"
    "v_exp_f32 v143, v143\n"
    "v_mfma_f32_16x16x32_bf16 v[32:35], v[112:115], v[232:235], v[32:35]\n"
    "ds_read_b128 v[108:111], v1 offset:60416\n"
    "v_fmac_f32 v24, v132, v120\n"
    "v_fmac_f32 v25, v133, v120\n"
    "v_mfma_f32_16x16x32_bf16 v[36:39], v[112:115], v[236:239], v[36:39]\n"
    "s_waitcnt lgkmcnt(4)\n"
    "v_fmac_f32 v26, v134, v120\n"
    "v_fmac_f32 v27, v135, v120\n"
    "v_mfma_f32_16x16x32_bf16 v[40:43], v[116:119], v[232:235], v[40:43]\n"
    "ds_read_b128 v[112:115], v1 offset:45056\n"
    "v_fmac_f32 v28, v132, v124\n"
    "v_fmac_f32 v29, v133, v124\n"
    "v_mfma_f32_16x16x32_bf16 v[44:47], v[116:119], v[236:239], v[44:47]\n"
    "s_waitcnt lgkmcnt(4)\n"
    "v_fmac_f32 v30, v134, v124\n"
    "v_fmac_f32 v31, v135, v124\n"
    "v_mfma_f32_16x16x32_bf16 v[32:35], v[96:99], v[240:243], v[32:35]\n"
    "ds_read_b128 v[116:119], v1 offset:61440\n"
    "ds_read_b128 v[128:131], v9 offset:128\n"
    "ds_read_b128 v[132:135], v9 offset:192\n"
    "v_fma_f32 v136, v136, v122, v122\n"
    "v_fma_f32 v137, v137, v122, v122\n"
    "v_mfma_f32_16x16x32_bf16 v[36:39], v[96:99], v[244:247], v[36:39]\n"
    "s_waitcnt lgkmcnt(6)\n"
    "v_fma_f32 v138, v138, v122, v122\n"
    "v_fma_f32 v139, v139, v122, v122\n"
    "v_mfma_f32_16x16x32_bf16 v[40:43], v[100:103], v[240:243], v[40:43]\n"
    "ds_read_b128 v[96:99], v1 offset:46080\n"
    "v_fma_f32 v140, v140, v126, v126\n"
    "v_fma_f32 v141, v141, v126, v126\n"
    "v_mfma_f32_16x16x32_bf16 v[44:47], v[100:103], v[244:247], v[44:47]\n"
    "s_waitcnt lgkmcnt(6)\n"
    "v_fma_f32 v142, v142, v126, v126\n"
    "v_fma_f32 v143, v143, v126, v126\n"
    "v_mfma_f32_16x16x32_bf16 v[32:35], v[104:107], v[248:251], v[32:35]\n"
    "ds_read_b128 v[100:103], v1 offset:62464\n"
    "v_rcp_f32 v136, v136\n"
    "v_mfma_f32_16x16x32_bf16 v[36:39], v[104:107], v[252:255], v[36:39]\n"
    "s_waitcnt lgkmcnt(6)\n"
    "v_rcp_f32 v137, v137\n"
    "v_mfma_f32_16x16x32_bf16 v[40:43], v[108:111], v[248:251], v[40:43]\n"
    "ds_read_b128 v[104:107], v1 offset:47104\n"
    "v_rcp_f32 v138, v138\n"
    "v_mfma_f32_16x16x32_bf16 v[44:47], v[108:111], v[252:255], v[44:47]\n"
    "s_waitcnt lgkmcnt(6)\n"
    "v_rcp_f32 v139, v139\n"
    "v_mfma_f32_16x16x32_bf16 v[32:35], v[112:115], v[64:67], v[32:35]\n"
    "ds_read_b128 v[108:111], v1 offset:63488\n"
    "v_rcp_f32 v140, v140\n"
    "v_mfma_f32_16x16x32_bf16 v[36:39], v[112:115], v[68:71], v[36:39]\n"
    "s_waitcnt lgkmcnt(6)\n"
    "v_rcp_f32 v141, v141\n"
    "v_mfma_f32_16x16x32_bf16 v[40:43], v[116:119], v[64:67], v[40:43]\n"
    "ds_read_b128 v[112:115], v1 offset:48128\n"
    "v_rcp_f32 v142, v142\n"
    "v_mfma_f32_16x16x32_bf16 v[44:47], v[116:119], v[68:71], v[44:47]\n"
    "s_waitcnt lgkmcnt(4)\n"
    "v_rcp_f32 v143, v143\n"
    "v_mfma_f32_16x16x32_bf16 v[32:35], v[96:99], v[72:75], v[32:35]\n"
    "ds_read_b128 v[116:119], v1 offset:64512\n"
    "v_mul_f32 v16, v16, v24\n"
    "v_mul_f32 v17, v17, v25\n"
    "v_mfma_f32_16x16x32_bf16 v[36:39], v[96:99], v[76:79], v[36:39]\n"
    "s_waitcnt lgkmcnt(4)\n"
    "v_mul_f32 v18, v18, v26\n"
    "v_mul_f32 v19, v19, v27\n"
    "v_mfma_f32_16x16x32_bf16 v[40:43], v[100:103], v[72:75], v[40:43]\n"
    "ds_read_b128 v[96:99], v2 offset:4096\n"
    "v_mul_f32 v20, v20, v28\n"
    "v_mul_f32 v21, v21, v29\n"
    "v_mfma_f32_16x16x32_bf16 v[44:47], v[100:103], v[76:79], v[44:47]\n"
    "s_waitcnt lgkmcnt(4)\n"
    "v_mul_f32 v22, v22, v30\n"
    "v_mul_f32 v23, v23, v31\n"
    "v_mfma_f32_16x16x32_bf16 v[32:35], v[104:107], v[80:83], v[32:35]\n"
    "ds_read_b128 v[100:103], v2 offset:20480\n"
    "v_mul_f32 v16, v16, v136\n"
    "v_mul_f32 v17, v17, v137\n"
    "v_mfma_f32_16x16x32_bf16 v[36:39], v[104:107], v[84:87], v[36:39]\n"
    "s_waitcnt lgkmcnt(4)\n"
    "v_mul_f32 v18, v18, v138\n"
    "v_mul_f32 v19, v19, v139\n"
    "v_mfma_f32_16x16x32_bf16 v[40:43], v[108:111], v[80:83], v[40:43]\n"
    "ds_read_b128 v[104:107], v2 offset:5120\n"
    "v_mul_f32 v20, v20, v140\n"
    "v_mul_f32 v21, v21, v141\n"
    "v_mfma_f32_16x16x32_bf16 v[44:47], v[108:111], v[84:87], v[44:47]\n"
    "s_waitcnt lgkmcnt(4)\n"
    "v_mul_f32 v22, v22, v142\n"
    "v_mul_f32 v23, v23, v143\n"
    "v_mfma_f32_16x16x32_bf16 v[32:35], v[112:115], v[88:91], v[32:35]\n"
    "ds_read_b128 v[108:111], v2 offset:21504\n"
    "v_cvt_pk_bf16_f32 v24, v16, v17\n"
    "v_cvt_pk_bf16_f32 v26, v20, v21\n"
    "v_mfma_f32_16x16x32_bf16 v[36:39], v[112:115], v[92:95], v[36:39]\n"
    "s_waitcnt lgkmcnt(4)\n"
    "v_cvt_pk_bf16_f32 v25, v18, v19\n"
    "v_cvt_pk_bf16_f32 v27, v22, v23\n"
    "v_mfma_f32_16x16x32_bf16 v[40:43], v[116:119], v[88:91], v[40:43]\n"
    "ds_read_b128 v[112:115], v2 offset:6144\n"
    "v_permlane16_swap_b32 v24, v26\n"
    "v_permlane16_swap_b32 v25, v27\n"
    "v_mfma_f32_16x16x32_bf16 v[44:47], v[116:119], v[92:95], v[44:47]\n"
    "s_waitcnt lgkmcnt(4)\n"
    "buffer_store_dwordx4 v[24:27], v8, s[20:23], s45 offen sc1\n"
    "v_mfma_f32_16x16x32_bf16 v[48:51], v[96:99], v[192:195], v[48:51]\n"
    "ds_read_b128 v[116:119], v2 offset:22528\n"
    "v_mfma_f32_16x16x32_bf16 v[52:55], v[96:99], v[196:199], v[52:55]\n"
    "s_waitcnt lgkmcnt(4)\n"
    "v_mfma_f32_16x16x32_bf16 v[56:59], v[100:103], v[192:195], v[56:59]\n"
    "ds_read_b128 v[96:99], v2 offset:7168\n"
    "v_mfma_f32_16x16x32_bf16 v[60:63], v[100:103], v[196:199], v[60:63]\n"
    "s_waitcnt lgkmcnt(4)\n"
    "s_mov_b64 exec, 1\n"
    "ds_add_u32 v5, v5 offset:16\n"
    "s_mov_b64 exec, -1\n"
    "v_mfma_f32_16x16x32_bf16 v[48:51], v[104:107], v[200:203], v[48:51]\n"
    "ds_read_b128 v[100:103], v2 offset:23552\n"
    "v_fmac_f32 v32, v128, v120\n"
    "v_fmac_f32 v33, v129, v120\n"
    "v_mfma_f32_16x16x32_bf16 v[52:55], v[104:107], v[204:207], v[52:55]\n"
    "s_waitcnt lgkmcnt(5)\n"
    "v_fmac_f32 v34, v130, v120\n"
    "v_fmac_f32 v35, v131, v120\n"
    "v_mfma_f32_16x16x32_bf16 v[56:59], v[108:111], v[200:203], v[56:59]\n"
    "ds_read_b128 v[104:107], v2 offset:8192\n"
    "v_fmac_f32 v36, v128, v124\n"
    "v_fmac_f32 v37, v129, v124\n"
    "v_mfma_f32_16x16x32_bf16 v[60:63], v[108:111], v[204:207], v[60:63]\n"
    "s_waitcnt lgkmcnt(5)\n"
    "v_fmac_f32 v38, v130, v124\n"
    "v_fmac_f32 v39, v131, v124\n"
    "v_mfma_f32_16x16x32_bf16 v[48:51], v[112:115], v[208:211], v[48:51]\n"
    "ds_read_b128 v[108:111], v2 offset:24576\n"
    "v_mul_f32 v136, v32, v121\n"
    "v_mul_f32 v137, v33, v121\n"
    "v_mfma_f32_16x16x32_bf16 v[52:55], v[112:115], v[212:215], v[52:55]\n"
    "s_waitcnt lgkmcnt(5)\n"
    "v_mul_f32 v138, v34, v121\n"
    "v_mul_f32 v139, v35, v121\n"
    "v_mfma_f32_16x16x32_bf16 v[56:59], v[116:119], v[208:211], v[56:59]\n"
    "ds_read_b128 v[112:115], v2 offset:9216\n"
    "v_mul_f32 v140, v36, v125\n"
    "v_mul_f32 v141, v37, v125\n"
    "v_mfma_f32_16x16x32_bf16 v[60:63], v[116:119], v[212:215], v[60:63]\n"
    "s_waitcnt lgkmcnt(5)\n"
    "v_mul_f32 v142, v38, v125\n"
    "v_mul_f32 v143, v39, v125\n"
    "v_mfma_f32_16x16x32_bf16 v[48:51], v[96:99], v[216:219], v[48:51]\n"
    "ds_read_b128 v[116:119], v2 offset:25600\n"
    "v_exp_f32 v136, v136\n"
    "v_mfma_f32_16x16x32_bf16 v[52:55], v[96:99], v[220:223], v[52:55]\n"
    "s_waitcnt lgkmcnt(4)\n"
    "v_exp_f32 v137, v137\n"
    "v_mfma_f32_16x16x32_bf16 v[56:59], v[100:103], v[216:219], v[56:59]\n"
    "ds_read_b128 v[96:99], v2 offset:10240\n"
    "v_exp_f32 v138, v138\n"
    "v_mfma_f32_16x16x32_bf16 v[60:63], v[100:103], v[220:223], v[60:63]\n"
    "s_waitcnt lgkmcnt(4)\n"
    "v_exp_f32 v139, v139\n"
    "v_mfma_f32_16x16x32_bf16 v[48:51], v[104:107], v[224:227], v[48:51]\n"
    "ds_read_b128 v[100:103], v2 offset:26624\n"
    "v_exp_f32 v140, v140\n"
    "v_mfma_f32_16x16x32_bf16 v[52:55], v[104:107], v[228:231], v[52:55]\n"
    "s_waitcnt lgkmcnt(4)\n"
    "v_exp_f32 v141, v141\n"
    "v_mfma_f32_16x16x32_bf16 v[56:59], v[108:111], v[224:227], v[56:59]\n"
    "ds_read_b128 v[104:107], v2 offset:11264\n"
    "v_exp_f32 v142, v142\n"
    "v_mfma_f32_16x16x32_bf16 v[60:63], v[108:111], v[228:231], v[60:63]\n"
    "s_waitcnt lgkmcnt(4)\n"
    "v_exp_f32 v143, v143\n"
    "v_mfma_f32_16x16x32_bf16 v[48:51], v[112:115], v[232:235], v[48:51]\n"
    "ds_read_b128 v[108:111], v2 offset:27648\n"
    "v_fmac_f32 v40, v132, v120\n"
    "v_fmac_f32 v41, v133, v120\n"
    "v_mfma_f32_16x16x32_bf16 v[52:55], v[112:115], v[236:239], v[52:55]\n"
    "s_waitcnt lgkmcnt(4)\n"
    "v_fmac_f32 v42, v134, v120\n"
    "v_fmac_f32 v43, v135, v120\n"
    "v_mfma_f32_16x16x32_bf16 v[56:59], v[116:119], v[232:235], v[56:59]\n"
    "ds_read_b128 v[112:115], v2 offset:12288\n"
    "v_fmac_f32 v44, v132, v124\n"
    "v_fmac_f32 v45, v133, v124\n"
    "v_mfma_f32_16x16x32_bf16 v[60:63], v[116:119], v[236:239], v[60:63]\n"
    "s_waitcnt lgkmcnt(4)\n"
    "v_fmac_f32 v46, v134, v124\n"
    "v_fmac_f32 v47, v135, v124\n"
    "v_mfma_f32_16x16x32_bf16 v[48:51], v[96:99], v[240:243], v[48:51]\n"
    "ds_read_b128 v[116:119], v2 offset:28672\n"
    "ds_read_b128 v[128:131], v9 offset:256\n"
    "ds_read_b128 v[132:135], v9 offset:320\n"
    "v_fma_f32 v136, v136, v122, v122\n"
    "v_fma_f32 v137, v137, v122, v122\n"
    "v_mfma_f32_16x16x32_bf16 v[52:55], v[96:99], v[244:247], v[52:55]\n"
    "s_waitcnt lgkmcnt(6)\n"
    "v_fma_f32 v138, v138, v122, v122\n"
    "v_fma_f32 v139, v139, v122, v122\n"
    "v_mfma_f32_16x16x32_bf16 v[56:59], v[100:103], v[240:243], v[56:59]\n"
    "ds_read_b128 v[96:99], v2 offset:13312\n"
    "v_fma_f32 v140, v140, v126, v126\n"
    "v_fma_f32 v141, v141, v126, v126\n"
    "v_mfma_f32_16x16x32_bf16 v[60:63], v[100:103], v[244:247], v[60:63]\n"
    "s_waitcnt lgkmcnt(6)\n"
    "v_fma_f32 v142, v142, v126, v126\n"
    "v_fma_f32 v143, v143, v126, v126\n"
    "v_mfma_f32_16x16x32_bf16 v[48:51], v[104:107], v[248:251], v[48:51]\n"
    "ds_read_b128 v[100:103], v2 offset:29696\n"
    "v_rcp_f32 v136, v136\n"
    "v_mfma_f32_16x16x32_bf16 v[52:55], v[104:107], v[252:255], v[52:55]\n"
    "s_waitcnt lgkmcnt(6)\n"
    "v_rcp_f32 v137, v137\n"
    "v_mfma_f32_16x16x32_bf16 v[56:59], v[108:111], v[248:251], v[56:59]\n"
    "ds_read_b128 v[104:107], v2 offset:14336\n"
    "v_rcp_f32 v138, v138\n"
    "v_mfma_f32_16x16x32_bf16 v[60:63], v[108:111], v[252:255], v[60:63]\n"
    "s_waitcnt lgkmcnt(6)\n"
    "v_rcp_f32 v139, v139\n"
    "v_mfma_f32_16x16x32_bf16 v[48:51], v[112:115], v[64:67], v[48:51]\n"
    "ds_read_b128 v[108:111], v2 offset:30720\n"
    "v_rcp_f32 v140, v140\n"
    "v_mfma_f32_16x16x32_bf16 v[52:55], v[112:115], v[68:71], v[52:55]\n"
    "s_waitcnt lgkmcnt(6)\n"
    "v_rcp_f32 v141, v141\n"
    "v_mfma_f32_16x16x32_bf16 v[56:59], v[116:119], v[64:67], v[56:59]\n"
    "ds_read_b128 v[112:115], v2 offset:15360\n"
    "v_rcp_f32 v142, v142\n"
    "v_mfma_f32_16x16x32_bf16 v[60:63], v[116:119], v[68:71], v[60:63]\n"
    "s_waitcnt lgkmcnt(4)\n"
    "v_rcp_f32 v143, v143\n"
    "v_mfma_f32_16x16x32_bf16 v[48:51], v[96:99], v[72:75], v[48:51]\n"
    "ds_read_b128 v[116:119], v2 offset:31744\n"
    "v_mul_f32 v32, v32, v40\n"
    "v_mul_f32 v33, v33, v41\n"
    "v_mfma_f32_16x16x32_bf16 v[52:55], v[96:99], v[76:79], v[52:55]\n"
    "s_waitcnt lgkmcnt(4)\n"
    "v_mul_f32 v34, v34, v42\n"
    "v_mul_f32 v35, v35, v43\n"
    "v_mfma_f32_16x16x32_bf16 v[56:59], v[100:103], v[72:75], v[56:59]\n"
    "v_mul_f32 v36, v36, v44\n"
    "v_mul_f32 v37, v37, v45\n"
    "v_mfma_f32_16x16x32_bf16 v[60:63], v[100:103], v[76:79], v[60:63]\n"
    "s_waitcnt lgkmcnt(3)\n"
    "v_mul_f32 v38, v38, v46\n"
    "v_mul_f32 v39, v39, v47\n"
    "v_mfma_f32_16x16x32_bf16 v[48:51], v[104:107], v[80:83], v[48:51]\n"
    "v_mul_f32 v32, v32, v136\n"
    "v_mul_f32 v33, v33, v137\n"
    "v_mfma_f32_16x16x32_bf16 v[52:55], v[104:107], v[84:87], v[52:55]\n"
    "s_waitcnt lgkmcnt(2)\n"
    "v_mul_f32 v34, v34, v138\n"
    "v_mul_f32 v35, v35, v139\n"
    "v_mfma_f32_16x16x32_bf16 v[56:59], v[108:111], v[80:83], v[56:59]\n"
    "v_mul_f32 v36, v36, v140\n"
    "v_mul_f32 v37, v37, v141\n"
    "v_mfma_f32_16x16x32_bf16 v[60:63], v[108:111], v[84:87], v[60:63]\n"
    "s_waitcnt lgkmcnt(1)\n"
    "v_mul_f32 v38, v38, v142\n"
    "v_mul_f32 v39, v39, v143\n"
    "v_mfma_f32_16x16x32_bf16 v[48:51], v[112:115], v[88:91], v[48:51]\n"
    "v_cvt_pk_bf16_f32 v40, v32, v33\n"
    "v_cvt_pk_bf16_f32 v42, v36, v37\n"
    "v_mfma_f32_16x16x32_bf16 v[52:55], v[112:115], v[92:95], v[52:55]\n"
    "s_waitcnt lgkmcnt(0)\n"
    "v_cvt_pk_bf16_f32 v41, v34, v35\n"
    "v_cvt_pk_bf16_f32 v43, v38, v39\n"
    "v_mfma_f32_16x16x32_bf16 v[56:59], v[116:119], v[88:91], v[56:59]\n"
    "v_permlane16_swap_b32 v40, v42\n"
    "v_permlane16_swap_b32 v41, v43\n"
    "v_mfma_f32_16x16x32_bf16 v[60:63], v[116:119], v[92:95], v[60:63]\n"
    "buffer_store_dwordx4 v[40:43], v6, s[20:23], s45 offen offset:512 sc1\n"
    "s_mov_b64 exec, 1\n"
    "ds_add_u32 v5, v5 offset:20\n"
    "s_mov_b64 exec, -1\n"
    "s_setprio 2\n"
    "v_fmac_f32 v48, v128, v120\n"
    "v_fmac_f32 v49, v129, v120\n"
    "v_fmac_f32 v50, v130, v120\n"
    "v_fmac_f32 v51, v131, v120\n"
    "v_fmac_f32 v52, v128, v124\n"
    "v_fmac_f32 v53, v129, v124\n"
    "v_fmac_f32 v54, v130, v124\n"
    "v_fmac_f32 v55, v131, v124\n"
    "v_mul_f32 v136, v48, v121\n"
    "v_mul_f32 v137, v49, v121\n"
    "v_mul_f32 v138, v50, v121\n"
    "v_mul_f32 v139, v51, v121\n"
    "v_mul_f32 v140, v52, v125\n"
    "v_mul_f32 v141, v53, v125\n"
    "v_mul_f32 v142, v54, v125\n"
    "v_mul_f32 v143, v55, v125\n"
    "v_exp_f32 v136, v136\n"
    "v_exp_f32 v137, v137\n"
    "v_exp_f32 v138, v138\n"
    "v_exp_f32 v139, v139\n"
    "v_exp_f32 v140, v140\n"
    "v_exp_f32 v141, v141\n"
    "v_exp_f32 v142, v142\n"
    "v_exp_f32 v143, v143\n"
    "v_fmac_f32 v56, v132, v120\n"
    "v_fmac_f32 v57, v133, v120\n"
    "v_fmac_f32 v58, v134, v120\n"
    "v_fmac_f32 v59, v135, v120\n"
    "v_fmac_f32 v60, v132, v124\n"
    "v_fmac_f32 v61, v133, v124\n"
    "v_fmac_f32 v62, v134, v124\n"
    "v_fmac_f32 v63, v135, v124\n"
    "v_fma_f32 v136, v136, v122, v122\n"
    "v_fma_f32 v137, v137, v122, v122\n"
    "v_fma_f32 v138, v138, v122, v122\n"
    "v_fma_f32 v139, v139, v122, v122\n"
    "v_fma_f32 v140, v140, v126, v126\n"
    "v_fma_f32 v141, v141, v126, v126\n"
    "v_fma_f32 v142, v142, v126, v126\n"
    "v_fma_f32 v143, v143, v126, v126\n"
    "v_rcp_f32 v136, v136\n"
    "v_rcp_f32 v137, v137\n"
    "v_rcp_f32 v138, v138\n"
    "v_rcp_f32 v139, v139\n"
    "v_rcp_f32 v140, v140\n"
    "v_rcp_f32 v141, v141\n"
    "v_rcp_f32 v142, v142\n"
    "v_rcp_f32 v143, v143\n"
    "v_mul_f32 v48, v48, v56\n"
    "v_mul_f32 v49, v49, v57\n"
    "v_mul_f32 v50, v50, v58\n"
    "v_mul_f32 v51, v51, v59\n"
    "v_mul_f32 v52, v52, v60\n"
    "v_mul_f32 v53, v53, v61\n"
    "v_mul_f32 v54, v54, v62\n"
    "v_mul_f32 v55, v55, v63\n"
    "v_mul_f32 v48, v48, v136\n"
    "v_mul_f32 v49, v49, v137\n"
    "v_mul_f32 v50, v50, v138\n"
    "v_mul_f32 v51, v51, v139\n"
    "v_mul_f32 v52, v52, v140\n"
    "v_mul_f32 v53, v53, v141\n"
    "v_mul_f32 v54, v54, v142\n"
    "v_mul_f32 v55, v55, v143\n"
    "v_cvt_pk_bf16_f32 v56, v48, v49\n"
    "v_cvt_pk_bf16_f32 v58, v52, v53\n"
    "v_cvt_pk_bf16_f32 v57, v50, v51\n"
    "v_cvt_pk_bf16_f32 v59, v54, v55\n"
    "v_permlane16_swap_b32 v56, v58\n"
    "s_nop 0\n"
    "v_permlane16_swap_b32 v57, v59\n"
    "buffer_store_dwordx4 v[56:59], v6, s[20:23], s45 offen offset:1024 sc1\n"
    ".Lk_d_post1:\n"
    "s_lshl_b32 s53, s56, 1\n"
    "s_add_u32 s53, s53, s40\n"
    "s_lshl_b32 s53, s53, 2\n"
    "s_add_u32 s53, s53, s58\n"
    "s_lshl_b32 s53, s53, 1\n"
    "s_and_b32 s54, s3, 1\n"
    "s_add_u32 s43, s53, s54\n"
    "s_mul_i32 s41, s43, 43\n"
    "s_mul_i32 s54, s59, 22\n"
    "s_add_u32 s41, s41, s54\n"
    "s_lshl_b32 s41, s41, 10\n"
    "s_add_u32 s41, s41, 10485760\n"
    "s_lshl_b32 s42, s56, 5\n"
    "s_add_u32 s42, s42, s33\n"
    "s_andn2_b32 s42, s42, 1\n"
    "s_lshl_b32 s42, s42, 1\n"
    "s_add_u32 s42, s42, s40\n"
    "s_lshl_b32 s42, s42, 7\n"
    "s_mul_i32 s54, s59, 56\n"
    "s_add_u32 s42, s42, s54\n"
    "s_add_u32 s42, s42, 1474560\n"
    "v_lshlrev_b32 v13, 2, v0\n"
    "s_mul_i32 s53, s59, 26624\n"
    "s_add_u32 s53, s53, 98304\n"
    "v_add_u32 v2, s53, v1\n"
    "v_xor_b32 v3, 0x800, v2\n"
    "s_mul_i32 s53, s59, 18432\n"
    "v_add_u32 v34, s53, v1\n"
    "v_xor_b32 v35, 0x800, v34\n"
    "v_add_u32 v32, 0x10000, v34\n"
    "v_add_u32 v33, 0x10000, v35\n"
    "s_mov_b32 s55, 0x100000\n"
    ".Lk_wl11:\n"
    "ds_read_b32 v12, v5 offset:24\n"
    "s_waitcnt lgkmcnt(0)\n"
    "v_readfirstlane_b32 s50, v12\n"
    "s_cmp_ge_u32 s50, 641024\n"
    "s_cbranch_scc1 .Lk_wd12\n"
    "s_sleep 1\n"
    "s_sub_u32 s55, s55, 1\n"
    "s_cbranch_scc0 .Lk_wl11\n"
    ".Lk_wd12:\n"
    "ds_read_b128 v[192:195], v2\n"
    "ds_read_b128 v[196:199], v2 offset:1024\n"
    "ds_read_b128 v[200:203], v3\n"
    "ds_read_b128 v[204:207], v3 offset:1024\n"
    "ds_read_b128 v[208:211], v2 offset:4096\n"
    "ds_read_b128 v[212:215], v2 offset:5120\n"
    "ds_read_b128 v[216:219], v3 offset:4096\n"
    "ds_read_b128 v[220:223], v3 offset:5120\n"
    "ds_read_b128 v[224:227], v2 offset:8192\n"
    "ds_read_b128 v[228:231], v2 offset:9216\n"
    "ds_read_b128 v[232:235], v3 offset:8192\n"
    "ds_read_b128 v[236:239], v3 offset:9216\n"
    "ds_read_b128 v[240:243], v2 offset:12288\n"
    "ds_read_b128 v[244:247], v2 offset:13312\n"
    "ds_read_b128 v[248:251], v3 offset:12288\n"
    "ds_read_b128 v[252:255], v3 offset:13312\n"
    "ds_read_b128 v[128:131], v2 offset:16384\n"
    "ds_read_b128 v[132:135], v2 offset:17408\n"
    "ds_read_b128 v[136:139], v3 offset:16384\n"
    "ds_read_b128 v[140:143], v3 offset:17408\n"
    "ds_read_b128 v[112:115], v2 offset:20480\n"
    "ds_read_b128 v[116:119], v2 offset:21504\n"
    "ds_read_b128 v[120:123], v3 offset:20480\n"
    "ds_read_b128 v[124:127], v3 offset:21504\n"
    "ds_read_b128 v[96:99], v34\n"
    "ds_read_b128 v[100:103], v34 offset:1024\n"
    "ds_read_b128 v[104:107], v35\n"
    "ds_read_b128 v[108:111], v35 offset:1024\n"
    "ds_read_b128 v[80:83], v34 offset:4096\n"
    "ds_read_b128 v[84:87], v34 offset:5120\n"
    "ds_read_b128 v[88:91], v35 offset:4096\n"
    "ds_read_b128 v[92:95], v35 offset:5120\n"
    "s_cmp_eq_u32 s65, 1\n"
    "s_cbranch_scc0 .Lk_d_nsg\n"
    "s_waitcnt vmcnt(0)\n"
    "s_mov_b64 exec, 15\n"
    "buffer_atomic_swap v10, v11, s[20:23], s44 offen sc1\n"
    "s_mov_b64 exec, -1\n"
    ".Lk_d_nsg:\n"
    "s_mov_b64 exec, 0x7fff\n"
    "v_mov_b32 v14, 0\n"
    "buffer_atomic_or v14, v13, s[20:23], s42 offen sc0 sc1\n"
    "s_mov_b64 exec, -1\n"
    "s_mov_b64 exec, 0x7fff\n"
    "s_mov_b32 s55, 0x8000\n"
    "s_waitcnt vmcnt(0)\n"
    "v_cmp_ne_u32 vcc, s38, v14\n"
    "s_cbranch_vccz .Lk_pd14\n"
    ".Lk_pl13:\n"
    "v_mov_b32 v14, 0\n"
    "buffer_atomic_or v14, v13, s[20:23], s42 offen sc0 sc1\n"
    "s_sub_u32 s55, s55, 1\n"
    "s_waitcnt vmcnt(0)\n"
    "v_cmp_ne_u32 vcc, s38, v14\n"
    "s_cbranch_scc1 .Lk_pd14\n"
    "s_cbranch_vccnz .Lk_pl13\n"
    ".Lk_pd14:\n"
    "s_mov_b64 exec, -1\n"
    "buffer_load_dwordx4 v[36:39], v1, s[20:23], s41 offen sc1\n"
    "buffer_load_dwordx4 v[40:43], v1, s[20:23], s41 offen offset:1024 sc1\n"
    "buffer_load_dwordx4 v[44:47], v1, s[20:23], s41 offen offset:2048 sc1\n"
    "buffer_load_dwordx4 v[48:51], v1, s[20:23], s41 offen offset:3072 sc1\n"
    "s_add_u32 s41, s41, 0x1000\n"
    "buffer_load_dwordx4 v[52:55], v1, s[20:23], s41 offen sc1\n"
    "buffer_load_dwordx4 v[56:59], v1, s[20:23], s41 offen offset:1024 sc1\n"
    "buffer_load_dwordx4 v[60:63], v1, s[20:23], s41 offen offset:2048 sc1\n"
    "buffer_load_dwordx4 v[64:67], v1, s[20:23], s41 offen offset:3072 sc1\n"
    "s_mov_b32 s55, 0x100000\n"
    ".Lk_wl15:\n"
    "ds_read_b32 v12, v5 offset:28\n"
    "s_waitcnt lgkmcnt(0)\n"
    "v_readfirstlane_b32 s50, v12\n"
    "s_cmp_ge_u32 s50, 641024\n"
    "s_cbranch_scc1 .Lk_wd16\n"
    "s_sleep 1\n"
    "s_sub_u32 s55, s55, 1\n"
    "s_cbranch_scc0 .Lk_wl15\n"
    ".Lk_wd16:\n"
    "s_branch .Lk_e_loop\n"
    ".Lk_d_loop0:\n"
    "s_waitcnt vmcnt(9) lgkmcnt(4)\n"
    "v_mfma_f32_16x16x32_bf16 v[16:19], v[96:99], v[64:67], 0\n"
    "ds_read_b128 v[116:119], v2 offset:16384\n"
    "s_waitcnt vmcnt(8)\n"
    "v_mfma_f32_16x16x32_bf16 v[20:23], v[96:99], v[68:71], 0\n"
    "s_waitcnt lgkmcnt(4)\n"
    "v_mfma_f32_16x16x32_bf16 v[24:27], v[100:103], v[64:67], 0\n"
    "ds_read_b128 v[96:99], v1 offset:1024\n"
    "v_mfma_f32_16x16x32_bf16 v[28:31], v[100:103], v[68:71], 0\n"
    "buffer_load_dwordx4 v[200:203], v1, s[20:23], s67 offen offset:1024 sc1\n"
    "s_waitcnt lgkmcnt(4)\n"
    "v_mfma_f32_16x16x32_bf16 v[32:35], v[104:107], v[64:67], 0\n"
    "ds_read_b128 v[100:103], v1 offset:17408\n"
    "v_mfma_f32_16x16x32_bf16 v[36:39], v[104:107], v[68:71], 0\n"
    "s_waitcnt lgkmcnt(4)\n"
    "v_mfma_f32_16x16x32_bf16 v[40:43], v[108:111], v[64:67], 0\n"
    "ds_read_b128 v[104:107], v1 offset:33792\n"
    "v_mfma_f32_16x16x32_bf16 v[44:47], v[108:111], v[68:71], 0\n"
    "buffer_load_dwordx4 v[204:207], v1, s[20:23], s68 offen offset:1024 sc1\n"
    "s_waitcnt lgkmcnt(4)\n"
    "v_mfma_f32_16x16x32_bf16 v[48:51], v[112:115], v[64:67], 0\n"
    "ds_read_b128 v[108:111], v1 offset:50176\n"
    "v_mfma_f32_16x16x32_bf16 v[52:55], v[112:115], v[68:71], 0\n"
    "s_waitcnt lgkmcnt(4)\n"
    "v_mfma_f32_16x16x32_bf16 v[56:59], v[116:119], v[64:67], 0\n"
    "ds_read_b128 v[112:115], v2 offset:1024\n"
    "v_mfma_f32_16x16x32_bf16 v[60:63], v[116:119], v[68:71], 0\n"
    "buffer_load_dwordx4 v[208:211], v1, s[20:23], s67 offen offset:2048 sc1\n"
    "s_waitcnt vmcnt(9) lgkmcnt(4)\n"
    "v_mfma_f32_16x16x32_bf16 v[16:19], v[96:99], v[72:75], v[16:19]\n"
    "ds_read_b128 v[116:119], v2 offset:17408\n"
    "v_mfma_f32_16x16x32_bf16 v[20:23], v[96:99], v[76:79], v[20:23]\n"
    "s_waitcnt lgkmcnt(4)\n"
    "v_mfma_f32_16x16x32_bf16 v[24:27], v[100:103], v[72:75], v[24:27]\n"
    "ds_read_b128 v[96:99], v1 offset:2048\n"
    "v_mfma_f32_16x16x32_bf16 v[28:31], v[100:103], v[76:79], v[28:31]\n"
    "buffer_load_dwordx4 v[212:215], v1, s[20:23], s68 offen offset:2048 sc1\n"
    "s_waitcnt lgkmcnt(4)\n"
    "v_mfma_f32_16x16x32_bf16 v[32:35], v[104:107], v[72:75], v[32:35]\n"
    "ds_read_b128 v[100:103], v1 offset:18432\n"
    "v_mfma_f32_16x16x32_bf16 v[36:39], v[104:107], v[76:79], v[36:39]\n"
    "s_waitcnt lgkmcnt(4)\n"
    "v_mfma_f32_16x16x32_bf16 v[40:43], v[108:111], v[72:75], v[40:43]\n"
    "ds_read_b128 v[104:107], v1 offset:34816\n"
    "v_mfma_f32_16x16x32_bf16 v[44:47], v[108:111], v[76:79], v[44:47]\n"
    "buffer_load_dwordx4 v[216:219], v1, s[20:23], s67 offen offset:3072 sc1\n"
    "s_waitcnt lgkmcnt(4)\n"
    "v_mfma_f32_16x16x32_bf16 v[48:51], v[112:115], v[72:75], v[48:51]\n"
    "ds_read_b128 v[108:111], v1 offset:51200\n"
    "v_mfma_f32_16x16x32_bf16 v[52:55], v[112:115], v[76:79], v[52:55]\n"
    "s_waitcnt lgkmcnt(4)\n"
    "v_mfma_f32_16x16x32_bf16 v[56:59], v[116:119], v[72:75], v[56:59]\n"
    "ds_read_b128 v[112:115], v2 offset:2048\n"
    "v_mfma_f32_16x16x32_bf16 v[60:63], v[116:119], v[76:79], v[60:63]\n"
    "buffer_load_dwordx4 v[220:223], v1, s[20:23], s68 offen offset:3072 sc1\n"
    "s_waitcnt vmcnt(10) lgkmcnt(4)\n"
    "v_mfma_f32_16x16x32_bf16 v[16:19], v[96:99], v[80:83], v[16:19]\n"
    "ds_read_b128 v[116:119], v2 offset:18432\n"
    "v_mfma_f32_16x16x32_bf16 v[20:23], v[96:99], v[84:87], v[20:23]\n"
    "s_waitcnt lgkmcnt(4)\n"
    "v_mfma_f32_16x16x32_bf16 v[24:27], v[100:103], v[80:83], v[24:27]\n"
    "ds_read_b128 v[96:99], v1 offset:3072\n"
    "v_mfma_f32_16x16x32_bf16 v[28:31], v[100:103], v[84:87], v[28:31]\n"
    "s_add_u32 s67, s67, 0x1000\n"
    "buffer_load_dwordx4 v[224:227], v1, s[20:23], s67 offen sc1\n"
    "s_waitcnt lgkmcnt(4)\n"
    "v_mfma_f32_16x16x32_bf16 v[32:35], v[104:107], v[80:83], v[32:35]\n"
    "ds_read_b128 v[100:103], v1 offset:19456\n"
    "v_mfma_f32_16x16x32_bf16 v[36:39], v[104:107], v[84:87], v[36:39]\n"
    "s_waitcnt lgkmcnt(4)\n"
    "v_mfma_f32_16x16x32_bf16 v[40:43], v[108:111], v[80:83], v[40:43]\n"
    "ds_read_b128 v[104:107], v1 offset:35840\n"
    "v_mfma_f32_16x16x32_bf16 v[44:47], v[108:111], v[84:87], v[44:47]\n"
    "s_add_u32 s68, s68, 0x1000\n"
    "buffer_load_dwordx4 v[228:231], v1, s[20:23], s68 offen sc1\n"
    "s_waitcnt lgkmcnt(4)\n"
    "v_mfma_f32_16x16x32_bf16 v[48:51], v[112:115], v[80:83], v[48:51]\n"
    "ds_read_b128 v[108:111], v1 offset:52224\n"
    "v_mfma_f32_16x16x32_bf16 v[52:55], v[112:115], v[84:87], v[52:55]\n"
    "s_waitcnt lgkmcnt(4)\n"
    "v_mfma_f32_16x16x32_bf16 v[56:59], v[116:119], v[80:83], v[56:59]\n"
    "ds_read_b128 v[112:115], v2 offset:3072\n"
    "v_mfma_f32_16x16x32_bf16 v[60:63], v[116:119], v[84:87], v[60:63]\n"
    "buffer_load_dwordx4 v[232:235], v1, s[20:23], s67 offen offset:1024 sc1\n"
    "s_waitcnt vmcnt(11) lgkmcnt(4)\n"
    "v_mfma_f32_16x16x32_bf16 v[16:19], v[96:99], v[88:91], v[16:19]\n"
    "ds_read_b128 v[116:119], v2 offset:19456\n"
    "v_mfma_f32_16x16x32_bf16 v[20:23], v[96:99], v[92:95], v[20:23]\n"
    "s_waitcnt lgkmcnt(4)\n"
    "v_mfma_f32_16x16x32_bf16 v[24:27], v[100:103], v[88:91], v[24:27]\n"
    "ds_read_b128 v[96:99], v1 offset:4096\n"
    "v_mfma_f32_16x16x32_bf16 v[28:31], v[100:103], v[92:95], v[28:31]\n"
    "buffer_load_dwordx4 v[236:239], v1, s[20:23], s68 offen offset:1024 sc1\n"
    "s_waitcnt lgkmcnt(4)\n"
    "v_mfma_f32_16x16x32_bf16 v[32:35], v[104:107], v[88:91], v[32:35]\n"
    "ds_read_b128 v[100:103], v1 offset:20480\n"
    "v_mfma_f32_16x16x32_bf16 v[36:39], v[104:107], v[92:95], v[36:39]\n"
    "s_waitcnt lgkmcnt(4)\n"
    "v_mfma_f32_16x16x32_bf16 v[40:43], v[108:111], v[88:91], v[40:43]\n"
    "ds_read_b128 v[104:107], v1 offset:36864\n"
    "v_mfma_f32_16x16x32_bf16 v[44:47], v[108:111], v[92:95], v[44:47]\n"
    "buffer_load_dwordx4 v[240:243], v1, s[20:23], s67 offen offset:2048 sc1\n"
    "s_waitcnt lgkmcnt(4)\n"
    "v_mfma_f32_16x16x32_bf16 v[48:51], v[112:115], v[88:91], v[48:51]\n"
    "ds_read_b128 v[108:111], v1 offset:53248\n"
    "v_mfma_f32_16x16x32_bf16 v[52:55], v[112:115], v[92:95], v[52:55]\n"
    "s_waitcnt lgkmcnt(4)\n"
    "v_mfma_f32_16x16x32_bf16 v[56:59], v[116:119], v[88:91], v[56:59]\n"
    "ds_read_b128 v[112:115], v2 offset:4096\n"
    "v_mfma_f32_16x16x32_bf16 v[60:63], v[116:119], v[92:95], v[60:63]\n"
    "buffer_load_dwordx4 v[244:247], v1, s[20:23], s68 offen offset:2048 sc1\n"
    "s_waitcnt vmcnt(12) lgkmcnt(4)\n"
    "v_mfma_f32_16x16x32_bf16 v[16:19], v[96:99], v[192:195], v[16:19]\n"
    "ds_read_b128 v[116:119], v2 offset:20480\n"
    "v_mfma_f32_16x16x32_bf16 v[20:23], v[96:99], v[196:199], v[20:23]\n"
    "s_waitcnt lgkmcnt(4)\n"
    "v_mfma_f32_16x16x32_bf16 v[24:27], v[100:103], v[192:195], v[24:27]\n"
    "ds_read_b128 v[96:99], v1 offset:5120\n"
    "v_mfma_f32_16x16x32_bf16 v[28:31], v[100:103], v[196:199], v[28:31]\n"
    "buffer_load_dwordx4 v[248:251], v1, s[20:23], s67 offen offset:3072 sc1\n"
    "s_waitcnt lgkmcnt(4)\n"
    "v_mfma_f32_16x16x32_bf16 v[32:35], v[104:107], v[192:195], v[32:35]\n"
    "ds_read_b128 v[100:103], v1 offset:21504\n"
    "v_mfma_f32_16x16x32_bf16 v[36:39], v[104:107], v[196:199], v[36:39]\n"
    "s_waitcnt lgkmcnt(4)\n"
    "v_mfma_f32_16x16x32_bf16 v[40:43], v[108:111], v[192:195], v[40:43]\n"
    "ds_read_b128 v[104:107], v1 offset:37888\n"
    "v_mfma_f32_16x16x32_bf16 v[44:47], v[108:111], v[196:199], v[44:47]\n"
    "buffer_load_dwordx4 v[252:255], v1, s[20:23], s68 offen offset:3072 sc1\n"
    "s_waitcnt lgkmcnt(4)\n"
    "v_mfma_f32_16x16x32_bf16 v[48:51], v[112:115], v[192:195], v[48:51]\n"
    "ds_read_b128 v[108:111], v1 offset:54272\n"
    "v_mfma_f32_16x16x32_bf16 v[52:55], v[112:115], v[196:199], v[52:55]\n"
    "s_waitcnt lgkmcnt(4)\n"
    "v_mfma_f32_16x16x32_bf16 v[56:59], v[116:119], v[192:195], v[56:59]\n"
    "ds_read_b128 v[112:115], v2 offset:5120\n"
    "v_mfma_f32_16x16x32_bf16 v[60:63], v[116:119], v[196:199], v[60:63]\n"
    "s_add_u32 s67, s67, 0x1000\n"
    "buffer_load_dwordx4 v[64:67], v1, s[20:23], s67 offen sc1\n"
    "s_waitcnt vmcnt(13) lgkmcnt(4)\n"
    "v_mfma_f32_16x16x32_bf16 v[16:19], v[96:99], v[200:203], v[16:19]\n"
    "ds_read_b128 v[116:119], v2 offset:21504\n"
    "v_mfma_f32_16x16x32_bf16 v[20:23], v[96:99], v[204:207], v[20:23]\n"
    "buffer_load_dwordx4 v[120:123], v1, s[20:23], s46 offen sc1\n"
    "s_waitcnt lgkmcnt(4)\n"
    "v_mfma_f32_16x16x32_bf16 v[24:27], v[100:103], v[200:203], v[24:27]\n"
    "ds_read_b128 v[96:99], v1 offset:6144\n"
    "v_mfma_f32_16x16x32_bf16 v[28:31], v[100:103], v[204:207], v[28:31]\n"
    "s_add_u32 s68, s68, 0x1000\n"
    "buffer_load_dwordx4 v[68:71], v1, s[20:23], s68 offen sc1\n"
    "s_waitcnt lgkmcnt(4)\n"
    "v_mfma_f32_16x16x32_bf16 v[32:35], v[104:107], v[200:203], v[32:35]\n"
    "ds_read_b128 v[100:103], v1 offset:22528\n"
    "v_mfma_f32_16x16x32_bf16 v[36:39], v[104:107], v[204:207], v[36:39]\n"
    "buffer_load_dwordx4 v[124:127], v1, s[20:23], s46 offen offset:1024 sc1\n"
    "s_waitcnt lgkmcnt(4)\n"
    "v_mfma_f32_16x16x32_bf16 v[40:43], v[108:111], v[200:203], v[40:43]\n"
    "ds_read_b128 v[104:107], v1 offset:38912\n"
    "v_mfma_f32_16x16x32_bf16 v[44:47], v[108:111], v[204:207], v[44:47]\n"
    "buffer_load_dwordx4 v[72:75], v1, s[20:23], s67 offen offset:1024 sc1\n"
    "s_waitcnt lgkmcnt(4)\n"
    "v_mfma_f32_16x16x32_bf16 v[48:51], v[112:115], v[200:203], v[48:51]\n"
    "ds_read_b128 v[108:111], v1 offset:55296\n"
    "v_mfma_f32_16x16x32_bf16 v[52:55], v[112:115], v[204:207], v[52:55]\n"
    "s_waitcnt lgkmcnt(4)\n"
    "v_mfma_f32_16x16x32_bf16 v[56:59], v[116:119], v[200:203], v[56:59]\n"
    "ds_read_b128 v[112:115], v2 offset:6144\n"
    "v_mfma_f32_16x16x32_bf16 v[60:63], v[116:119], v[204:207], v[60:63]\n"
    "buffer_load_dwordx4 v[76:79], v1, s[20:23], s68 offen offset:1024 sc1\n"
    "s_waitcnt vmcnt(16) lgkmcnt(4)\n"
    "v_mfma_f32_16x16x32_bf16 v[16:19], v[96:99], v[208:211], v[16:19]\n"
    "ds_read_b128 v[116:119], v2 offset:22528\n"
    "v_mfma_f32_16x16x32_bf16 v[20:23], v[96:99], v[212:215], v[20:23]\n"
    "s_waitcnt lgkmcnt(4)\n"
    "v_mfma_f32_16x16x32_bf16 v[24:27], v[100:103], v[208:211], v[24:27]\n"
    "ds_read_b128 v[96:99], v1 offset:7168\n"
    "v_mfma_f32_16x16x32_bf16 v[28:31], v[100:103], v[212:215], v[28:31]\n"
    "buffer_load_dwordx4 v[80:83], v1, s[20:23], s67 offen offset:2048 sc1\n"
    "s_waitcnt lgkmcnt(4)\n"
    "v_mfma_f32_16x16x32_bf16 v[32:35], v[104:107], v[208:211], v[32:35]\n"
    "ds_read_b128 v[100:103], v1 offset:23552\n"
    "v_mfma_f32_16x16x32_bf16 v[36:39], v[104:107], v[212:215], v[36:39]\n"
    "s_waitcnt lgkmcnt(4)\n"
    "v_mfma_f32_16x16x32_bf16 v[40:43], v[108:111], v[208:211], v[40:43]\n"
    "ds_read_b128 v[104:107], v1 offset:39936\n"
    "v_mfma_f32_16x16x32_bf16 v[44:47], v[108:111], v[212:215], v[44:47]\n"
    "buffer_load_dwordx4 v[84:87], v1, s[20:23], s68 offen offset:2048 sc1\n"
    "s_waitcnt lgkmcnt(4)\n"
    "v_mfma_f32_16x16x32_bf16 v[48:51], v[112:115], v[208:211], v[48:51]\n"
    "ds_read_b128 v[108:111], v1 offset:56320\n"
    "v_mfma_f32_16x16x32_bf16 v[52:55], v[112:115], v[212:215], v[52:55]\n"
    "s_waitcnt lgkmcnt(4)\n"
    "v_mfma_f32_16x16x32_bf16 v[56:59], v[116:119], v[208:211], v[56:59]\n"
    "ds_read_b128 v[112:115], v2 offset:7168\n"
    "v_mfma_f32_16x16x32_bf16 v[60:63], v[116:119], v[212:215], v[60:63]\n"
    "buffer_load_dwordx4 v[88:91], v1, s[20:23], s67 offen offset:3072 sc1\n"
    "s_waitcnt vmcnt(17) lgkmcnt(4)\n"
    "v_mfma_f32_16x16x32_bf16 v[16:19], v[96:99], v[216:219], v[16:19]\n"
    "ds_read_b128 v[116:119], v2 offset:23552\n"
    "v_mfma_f32_16x16x32_bf16 v[20:23], v[96:99], v[220:223], v[20:23]\n"
    "s_waitcnt lgkmcnt(4)\n"
    "v_mfma_f32_16x16x32_bf16 v[24:27], v[100:103], v[216:219], v[24:27]\n"
    "ds_read_b128 v[96:99], v1 offset:8192\n"
    "v_mfma_f32_16x16x32_bf16 v[28:31], v[100:103], v[220:223], v[28:31]\n"
    "buffer_load_dwordx4 v[92:95], v1, s[20:23], s68 offen offset:3072 sc1\n"
    "s_waitcnt lgkmcnt(4)\n"
    "v_mfma_f32_16x16x32_bf16 v[32:35], v[104:107], v[216:219], v[32:35]\n"
    "ds_read_b128 v[100:103], v1 offset:24576\n"
    "v_mfma_f32_16x16x32_bf16 v[36:39], v[104:107], v[220:223], v[36:39]\n"
    "s_waitcnt lgkmcnt(4)\n"
    "v_mfma_f32_16x16x32_bf16 v[40:43], v[108:111], v[216:219], v[40:43]\n"
    "ds_read_b128 v[104:107], v1 offset:9216\n"
    "v_mfma_f32_16x16x32_bf16 v[44:47], v[108:111], v[220:223], v[44:47]\n"
    "s_waitcnt lgkmcnt(4)\n"
    "v_mfma_f32_16x16x32_bf16 v[48:51], v[112:115], v[216:219], v[48:51]\n"
    "ds_read_b128 v[108:111], v1 offset:25600\n"
    "v_mfma_f32_16x16x32_bf16 v[52:55], v[112:115], v[220:223], v[52:55]\n"
    "s_waitcnt lgkmcnt(4)\n"
    "v_mfma_f32_16x16x32_bf16 v[56:59], v[116:119], v[216:219], v[56:59]\n"
    "ds_read_b128 v[112:115], v1 offset:10240\n"
    "v_mfma_f32_16x16x32_bf16 v[60:63], v[116:119], v[220:223], v[60:63]\n"
    "s_waitcnt vmcnt(16) lgkmcnt(4)\n"
    "v_mfma_f32_16x16x32_bf16 v[16:19], v[96:99], v[224:227], v[16:19]\n"
    "ds_read_b128 v[116:119], v1 offset:26624\n"
    "v_mfma_f32_16x16x32_bf16 v[20:23], v[96:99], v[228:231], v[20:23]\n"
    "s_waitcnt lgkmcnt(4)\n"
    "v_mfma_f32_16x16x32_bf16 v[24:27], v[100:103], v[224:227], v[24:27]\n"
    "ds_read_b128 v[96:99], v1 offset:11264\n"
    "v_mfma_f32_16x16x32_bf16 v[28:31], v[100:103], v[228:231], v[28:31]\n"
    "s_waitcnt vmcnt(14) lgkmcnt(4)\n"
    "s_mov_b32 s55, 0x8000\n"
    ".Lk_sgl0:\n"
    "s_waitcnt vmcnt(8)\n"
    "v_cmp_ne_u32 vcc, s38, v121\n"
    "v_cmp_ne_u32 s[50:51], s38, v123\n"
    "s_or_b64 vcc, vcc, s[50:51]\n"
    "s_waitcnt vmcnt(6)\n"
    "v_cmp_ne_u32 s[50:51], s38, v125\n"
    "s_or_b64 vcc, vcc, s[50:51]\n"
    "v_cmp_ne_u32 s[50:51], s38, v127\n"
    "s_or_b64 vcc, vcc, s[50:51]\n"
    "s_cbranch_scc0 .Lk_sgd0\n"
    "s_sub_u32 s55, s55, 1\n"
    "s_cbranch_scc1 .Lk_sgd0\n"
    "s_sleep 4\n"
    "buffer_load_dwordx4 v[120:123], v1, s[20:23], s46 offen sc1\n"
    "buffer_load_dwordx4 v[124:127], v1, s[20:23], s46 offen offset:1024 sc1\n"
    "s_waitcnt vmcnt(0)\n"
    "s_branch .Lk_sgl0\n"
    ".Lk_sgd0:\n"
    "v_mfma_f32_16x16x32_bf16 v[16:19], v[104:107], v[232:235], v[16:19]\n"
    "ds_read_b128 v[100:103], v1 offset:27648\n"
    "v_mov_b32 v121, v120\n"
    "v_mov_b32 v123, v122\n"
    "v_mov_b32 v125, v124\n"
    "v_mfma_f32_16x16x32_bf16 v[20:23], v[104:107], v[236:239], v[20:23]\n"
    "s_waitcnt lgkmcnt(4)\n"
    "v_mov_b32 v127, v126\n"
    "v_permlane16_swap_b32 v120, v121\n"
    "v_permlane16_swap_b32 v122, v123\n"
    "v_mfma_f32_16x16x32_bf16 v[24:27], v[108:111], v[232:235], v[24:27]\n"
    "ds_read_b128 v[104:107], v1 offset:12288\n"
    "v_permlane16_swap_b32 v124, v125\n"
    "v_permlane16_swap_b32 v126, v127\n"
    "v_add_f32 v120, v120, v121\n"
    "v_mfma_f32_16x16x32_bf16 v[28:31], v[108:111], v[236:239], v[28:31]\n"
    "s_waitcnt lgkmcnt(4)\n"
    "v_add_f32 v122, v122, v123\n"
    "v_add_f32 v124, v124, v125\n"
    "v_add_f32 v126, v126, v127\n"
    "v_mfma_f32_16x16x32_bf16 v[16:19], v[112:115], v[240:243], v[16:19]\n"
    "ds_read_b128 v[108:111], v1 offset:28672\n"
    "v_mov_b32 v121, v120\n"
    "v_mov_b32 v123, v122\n"
    "v_mov_b32 v125, v124\n"
    "v_mfma_f32_16x16x32_bf16 v[20:23], v[112:115], v[244:247], v[20:23]\n"
    "s_waitcnt lgkmcnt(4)\n"
    "v_mov_b32 v127, v126\n"
    "v_permlane32_swap_b32 v120, v121\n"
    "v_permlane32_swap_b32 v122, v123\n"
    "v_mfma_f32_16x16x32_bf16 v[24:27], v[116:119], v[240:243], v[24:27]\n"
    "ds_read_b128 v[112:115], v1 offset:13312\n"
    "v_permlane32_swap_b32 v124, v125\n"
    "v_permlane32_swap_b32 v126, v127\n"
    "v_add_f32 v120, v120, v121\n"
    "v_mfma_f32_16x16x32_bf16 v[28:31], v[116:119], v[244:247], v[28:31]\n"
    "s_waitcnt lgkmcnt(4)\n"
    "v_add_f32 v122, v122, v123\n"
    "v_add_f32 v124, v124, v125\n"
    "v_add_f32 v126, v126, v127\n"
    "v_mfma_f32_16x16x32_bf16 v[16:19], v[96:99], v[248:251], v[16:19]\n"
    "ds_read_b128 v[116:119], v1 offset:29696\n"
    "v_mul_f32 v120, 0xbb000000, v120\n"
    "v_mul_f32 v122, 0x3b000000, v122\n"
    "v_mul_f32 v124, 0xbb000000, v124\n"
    "v_mfma_f32_16x16x32_bf16 v[20:23], v[96:99], v[252:255], v[20:23]\n"
    "s_waitcnt lgkmcnt(4)\n"
    "v_mul_f32 v126, 0x3b000000, v126\n"
    "v_fma_f32 v122, -v120, v120, v122\n"
    "v_fma_f32 v126, -v124, v124, v126\n"
    "v_mfma_f32_16x16x32_bf16 v[24:27], v[100:103], v[248:251], v[24:27]\n"
    "ds_read_b128 v[96:99], v1 offset:14336\n"
    "v_max_f32 v122, 0, v122\n"
    "v_max_f32 v126, 0, v126\n"
    "v_add_f32 v122, 0x3727c5ac, v122\n"
    "v_mfma_f32_16x16x32_bf16 v[28:31], v[100:103], v[252:255], v[28:31]\n"
    "s_waitcnt lgkmcnt(4)\n"
    "v_add_f32 v126, 0x3727c5ac, v126\n"
    "v_rsq_f32 v121, v122\n"
    "v_mfma_f32_16x16x32_bf16 v[16:19], v[104:107], v[64:67], v[16:19]\n"
    "ds_read_b128 v[100:103], v1 offset:30720\n"
    "v_rsq_f32 v125, v126\n"
    "v_mul_f32 v121, 0xbfb8aa3b, v121\n"
    "v_mfma_f32_16x16x32_bf16 v[20:23], v[104:107], v[68:71], v[20:23]\n"
    "s_waitcnt lgkmcnt(4)\n"
    "v_mul_f32 v125, 0xbfb8aa3b, v125\n"
    "v_mfma_f32_16x16x32_bf16 v[24:27], v[108:111], v[64:67], v[24:27]\n"
    "ds_read_b128 v[104:107], v1 offset:15360\n"
    "v_mfma_f32_16x16x32_bf16 v[28:31], v[108:111], v[68:71], v[28:31]\n"
    "s_waitcnt vmcnt(4) lgkmcnt(4)\n"
    "v_mfma_f32_16x16x32_bf16 v[16:19], v[112:115], v[72:75], v[16:19]\n"
    "ds_read_b128 v[108:111], v1 offset:31744\n"
    "v_mfma_f32_16x16x32_bf16 v[20:23], v[112:115], v[76:79], v[20:23]\n"
    "s_waitcnt lgkmcnt(4)\n"
    "v_mfma_f32_16x16x32_bf16 v[24:27], v[116:119], v[72:75], v[24:27]\n"
    "ds_read_b128 v[112:115], v1 offset:40960\n"
    "v_mfma_f32_16x16x32_bf16 v[28:31], v[116:119], v[76:79], v[28:31]\n"
    "s_waitcnt vmcnt(2) lgkmcnt(4)\n"
    "v_mfma_f32_16x16x32_bf16 v[16:19], v[96:99], v[80:83], v[16:19]\n"
    "ds_read_b128 v[116:119], v1 offset:57344\n"
    "v_mfma_f32_16x16x32_bf16 v[20:23], v[96:99], v[84:87], v[20:23]\n"
    "s_waitcnt lgkmcnt(4)\n"
    "v_mfma_f32_16x16x32_bf16 v[24:27], v[100:103], v[80:83], v[24:27]\n"
    "ds_read_b128 v[96:99], v1 offset:41984\n"
    "v_mfma_f32_16x16x32_bf16 v[28:31], v[100:103], v[84:87], v[28:31]\n"
    "s_waitcnt vmcnt(0) lgkmcnt(4)\n"
    "v_mfma_f32_16x16x32_bf16 v[16:19], v[104:107], v[88:91], v[16:19]\n"
    "ds_read_b128 v[100:103], v1 offset:58368\n"
    "v_mfma_f32_16x16x32_bf16 v[20:23], v[104:107], v[92:95], v[20:23]\n"
    "s_waitcnt lgkmcnt(4)\n"
    "v_mfma_f32_16x16x32_bf16 v[24:27], v[108:111], v[88:91], v[24:27]\n"
    "ds_read_b128 v[104:107], v1 offset:43008\n"
    "v_mfma_f32_16x16x32_bf16 v[28:31], v[108:111], v[92:95], v[28:31]\n"
    "s_waitcnt lgkmcnt(4)\n"
    "v_mfma_f32_16x16x32_bf16 v[32:35], v[112:115], v[224:227], v[32:35]\n"
    "ds_read_b128 v[108:111], v1 offset:59392\n"
    "v_mfma_f32_16x16x32_bf16 v[36:39], v[112:115], v[228:231], v[36:39]\n"
    "s_waitcnt lgkmcnt(4)\n"
    "v_mfma_f32_16x16x32_bf16 v[40:43], v[116:119], v[224:227], v[40:43]\n"
    "ds_read_b128 v[112:115], v1 offset:44032\n"
    "v_mfma_f32_16x16x32_bf16 v[44:47], v[116:119], v[228:231], v[44:47]\n"
    "s_waitcnt lgkmcnt(4)\n"
    "v_cmp_u_f32 vcc, v16, v20\n"
    "s_cbranch_vccnz .Lk_d_slow0\n"
    ".Lk_d_back0:\n"
    "s_mov_b64 exec, 1\n"
    "ds_add_u32 v5, v5 offset:12\n"
    "s_mov_b64 exec, -1\n"
    "v_mfma_f32_16x16x32_bf16 v[32:35], v[96:99], v[232:235], v[32:35]\n"
    "ds_read_b128 v[116:119], v1 offset:60416\n"
    "v_fmac_f32 v16, v128, v120\n"
    "v_fmac_f32 v17, v129, v120\n"
    "v_fmac_f32 v18, v130, v120\n"
    "v_mfma_f32_16x16x32_bf16 v[36:39], v[96:99], v[236:239], v[36:39]\n"
    "s_waitcnt lgkmcnt(5)\n"
    "v_fmac_f32 v19, v131, v120\n"
    "v_fmac_f32 v20, v128, v124\n"
    "v_fmac_f32 v21, v129, v124\n"
    "v_mfma_f32_16x16x32_bf16 v[40:43], v[100:103], v[232:235], v[40:43]\n"
    "ds_read_b128 v[96:99], v1 offset:45056\n"
    "v_fmac_f32 v22, v130, v124\n"
    "v_fmac_f32 v23, v131, v124\n"
    "v_mul_f32 v136, v16, v121\n"
    "v_mfma_f32_16x16x32_bf16 v[44:47], v[100:103], v[236:239], v[44:47]\n"
    "s_waitcnt lgkmcnt(5)\n"
    "v_mul_f32 v137, v17, v121\n"
    "v_mul_f32 v138, v18, v121\n"
    "v_mul_f32 v139, v19, v121\n"
    "v_mfma_f32_16x16x32_bf16 v[32:35], v[104:107], v[240:243], v[32:35]\n"
    "ds_read_b128 v[100:103], v1 offset:61440\n"
    "v_mul_f32 v140, v20, v125\n"
    "v_mul_f32 v141, v21, v125\n"
    "v_mul_f32 v142, v22, v125\n"
    "v_mfma_f32_16x16x32_bf16 v[36:39], v[104:107], v[244:247], v[36:39]\n"
    "s_waitcnt lgkmcnt(5)\n"
    "v_mul_f32 v143, v23, v125\n"
    "v_exp_f32 v136, v136\n"
    "v_mfma_f32_16x16x32_bf16 v[40:43], v[108:111], v[240:243], v[40:43]\n"
    "ds_read_b128 v[104:107], v1 offset:46080\n"
    "v_exp_f32 v137, v137\n"
    "v_mfma_f32_16x16x32_bf16 v[44:47], v[108:111], v[244:247], v[44:47]\n"
    "s_waitcnt lgkmcnt(5)\n"
    "v_exp_f32 v138, v138\n"
    "v_mfma_f32_16x16x32_bf16 v[32:35], v[112:115], v[248:251], v[32:35]\n"
    "ds_read_b128 v[108:111], v1 offset:62464\n"
    "v_exp_f32 v139, v139\n"
    "v_mfma_f32_16x16x32_bf16 v[36:39], v[112:115], v[252:255], v[36:39]\n"
    "s_waitcnt lgkmcnt(4)\n"
    "v_exp_f32 v140, v140\n"
    "v_mfma_f32_16x16x32_bf16 v[40:43], v[116:119], v[248:251], v[40:43]\n"
    "ds_read_b128 v[112:115], v1 offset:47104\n"
    "v_exp_f32 v141, v141\n"
    "v_mfma_f32_16x16x32_bf16 v[44:47], v[116:119], v[252:255], v[44:47]\n"
    "s_waitcnt lgkmcnt(4)\n"
    "v_exp_f32 v142, v142\n"
    "v_mfma_f32_16x16x32_bf16 v[32:35], v[96:99], v[64:67], v[32:35]\n"
    "ds_read_b128 v[116:119], v1 offset:63488\n"
    "v_exp_f32 v143, v143\n"
    "v_fmac_f32 v24, v132, v120\n"
    "v_mfma_f32_16x16x32_bf16 v[36:39], v[96:99], v[68:71], v[36:39]\n"
    "s_waitcnt lgkmcnt(4)\n"
    "v_fmac_f32 v25, v133, v120\n"
    "v_fmac_f32 v26, v134, v120\n"
    "v_fmac_f32 v27, v135, v120\n"
    "v_mfma_f32_16x16x32_bf16 v[40:43], v[100:103], v[64:67], v[40:43]\n"
    "ds_read_b128 v[96:99], v1 offset:48128\n"
    "v_fmac_f32 v28, v132, v124\n"
    "v_fmac_f32 v29, v133, v124\n"
    "v_fmac_f32 v30, v134, v124\n"
    "v_mfma_f32_16x16x32_bf16 v[44:47], v[100:103], v[68:71], v[44:47]\n"
    "s_waitcnt lgkmcnt(4)\n"
    "v_fmac_f32 v31, v135, v124\n"
    "ds_read_b128 v[128:131], v9 offset:128\n"
    "ds_read_b128 v[132:135], v9 offset:192\n"
    "v_fma_f32 v136, v136, v122, v122\n"
    "v_fma_f32 v137, v137, v122, v122\n"
    "v_mfma_f32_16x16x32_bf16 v[32:35], v[104:107], v[72:75], v[32:35]\n"
    "ds_read_b128 v[100:103], v1 offset:64512\n"
    "v_fma_f32 v138, v138, v122, v122\n"
    "v_fma_f32 v139, v139, v122, v122\n"
    "v_fma_f32 v140, v140, v126, v126\n"
    "v_mfma_f32_16x16x32_bf16 v[36:39], v[104:107], v[76:79], v[36:39]\n"
    "s_waitcnt lgkmcnt(6)\n"
    "v_fma_f32 v141, v141, v126, v126\n"
    "v_fma_f32 v142, v142, v126, v126\n"
    "v_fma_f32 v143, v143, v126, v126\n"
    "v_mfma_f32_16x16x32_bf16 v[40:43], v[108:111], v[72:75], v[40:43]\n"
    "ds_read_b128 v[104:107], v2 offset:8192\n"
    "v_rcp_f32 v136, v136\n"
    "v_mfma_f32_16x16x32_bf16 v[44:47], v[108:111], v[76:79], v[44:47]\n"
    "s_waitcnt lgkmcnt(6)\n"
    "v_rcp_f32 v137, v137\n"
    "v_mfma_f32_16x16x32_bf16 v[32:35], v[112:115], v[80:83], v[32:35]\n"
    "ds_read_b128 v[108:111], v2 offset:24576\n"
    "v_rcp_f32 v138, v138\n"
    "v_mfma_f32_16x16x32_bf16 v[36:39], v[112:115], v[84:87], v[36:39]\n"
    "s_waitcnt lgkmcnt(6)\n"
    "v_rcp_f32 v139, v139\n"
    "v_mfma_f32_16x16x32_bf16 v[40:43], v[116:119], v[80:83], v[40:43]\n"
    "ds_read_b128 v[112:115], v2 offset:9216\n"
    "v_rcp_f32 v140, v140\n"
    "v_mfma_f32_16x16x32_bf16 v[44:47], v[116:119], v[84:87], v[44:47]\n"
    "s_waitcnt lgkmcnt(6)\n"
    "v_rcp_f32 v141, v141\n"
    "v_mfma_f32_16x16x32_bf16 v[32:35], v[96:99], v[88:91], v[32:35]\n"
    "ds_read_b128 v[116:119], v2 offset:25600\n"
    "v_rcp_f32 v142, v142\n"
    "v_mfma_f32_16x16x32_bf16 v[36:39], v[96:99], v[92:95], v[36:39]\n"
    "s_waitcnt lgkmcnt(4)\n"
    "v_rcp_f32 v143, v143\n"
    "v_mul_f32 v16, v16, v24\n"
    "v_mfma_f32_16x16x32_bf16 v[40:43], v[100:103], v[88:91], v[40:43]\n"
    "ds_read_b128 v[96:99], v2 offset:10240\n"
    "v_mul_f32 v17, v17, v25\n"
    "v_mul_f32 v18, v18, v26\n"
    "v_mul_f32 v19, v19, v27\n"
    "v_mfma_f32_16x16x32_bf16 v[44:47], v[100:103], v[92:95], v[44:47]\n"
    "s_waitcnt lgkmcnt(4)\n"
    "v_mul_f32 v20, v20, v28\n"
    "v_mul_f32 v21, v21, v29\n"
    "v_mul_f32 v22, v22, v30\n"
    "v_mfma_f32_16x16x32_bf16 v[48:51], v[104:107], v[224:227], v[48:51]\n"
    "ds_read_b128 v[100:103], v2 offset:26624\n"
    "v_mul_f32 v23, v23, v31\n"
    "v_mul_f32 v16, v16, v136\n"
    "v_mul_f32 v17, v17, v137\n"
    "v_mfma_f32_16x16x32_bf16 v[52:55], v[104:107], v[228:231], v[52:55]\n"
    "s_waitcnt lgkmcnt(4)\n"
    "v_mul_f32 v18, v18, v138\n"
    "v_mul_f32 v19, v19, v139\n"
    "v_mul_f32 v20, v20, v140\n"
    "v_mfma_f32_16x16x32_bf16 v[56:59], v[108:111], v[224:227], v[56:59]\n"
    "ds_read_b128 v[104:107], v2 offset:11264\n"
    "v_mul_f32 v21, v21, v141\n"
    "v_mul_f32 v22, v22, v142\n"
    "v_mul_f32 v23, v23, v143\n"
    "v_mfma_f32_16x16x32_bf16 v[60:63], v[108:111], v[228:231], v[60:63]\n"
    "s_waitcnt lgkmcnt(4)\n"
    "v_cvt_pk_bf16_f32 v24, v16, v17\n"
    "v_cvt_pk_bf16_f32 v26, v20, v21\n"
    "v_cvt_pk_bf16_f32 v25, v18, v19\n"
    "s_mov_b64 exec, 1\n"
    "ds_add_u32 v5, v5 offset:16\n"
    "s_mov_b64 exec, -1\n"
    "v_mfma_f32_16x16x32_bf16 v[48:51], v[112:115], v[232:235], v[48:51]\n"
    "ds_read_b128 v[108:111], v2 offset:27648\n"
    "v_cvt_pk_bf16_f32 v27, v22, v23\n"
    "v_permlane16_swap_b32 v24, v26\n"
    "s_nop 0\n"
    "v_permlane16_swap_b32 v25, v27\n"
    "v_mfma_f32_16x16x32_bf16 v[52:55], v[112:115], v[236:239], v[52:55]\n"
    "s_waitcnt lgkmcnt(5)\n"
    "buffer_store_dwordx4 v[24:27], v8, s[20:23], s45 offen sc1\n"
    "v_fmac_f32 v32, v128, v120\n"
    "v_fmac_f32 v33, v129, v120\n"
    "v_fmac_f32 v34, v130, v120\n"
    "v_mfma_f32_16x16x32_bf16 v[56:59], v[116:119], v[232:235], v[56:59]\n"
    "ds_read_b128 v[112:115], v2 offset:12288\n"
    "v_fmac_f32 v35, v131, v120\n"
    "v_fmac_f32 v36, v128, v124\n"
    "v_fmac_f32 v37, v129, v124\n"
    "v_mfma_f32_16x16x32_bf16 v[60:63], v[116:119], v[236:239], v[60:63]\n"
    "s_waitcnt lgkmcnt(5)\n"
    "v_fmac_f32 v38, v130, v124\n"
    "v_fmac_f32 v39, v131, v124\n"
    "v_mul_f32 v136, v32, v121\n"
    "v_mfma_f32_16x16x32_bf16 v[48:51], v[96:99], v[240:243], v[48:51]\n"
    "ds_read_b128 v[116:119], v2 offset:28672\n"
    "v_mul_f32 v137, v33, v121\n"
    "v_mul_f32 v138, v34, v121\n"
    "v_mul_f32 v139, v35, v121\n"
    "v_mfma_f32_16x16x32_bf16 v[52:55], v[96:99], v[244:247], v[52:55]\n"
    "s_waitcnt lgkmcnt(5)\n"
    "v_mul_f32 v140, v36, v125\n"
    "v_mul_f32 v141, v37, v125\n"
    "v_mul_f32 v142, v38, v125\n"
    "v_mfma_f32_16x16x32_bf16 v[56:59], v[100:103], v[240:243], v[56:59]\n"
    "ds_read_b128 v[96:99], v2 offset:13312\n"
    "v_mul_f32 v143, v39, v125\n"
    "v_exp_f32 v136, v136\n"
    "v_mfma_f32_16x16x32_bf16 v[60:63], v[100:103], v[244:247], v[60:63]\n"
    "s_waitcnt lgkmcnt(5)\n"
    "v_exp_f32 v137, v137\n"
    "v_mfma_f32_16x16x32_bf16 v[48:51], v[104:107], v[248:251], v[48:51]\n"
    "ds_read_b128 v[100:103], v2 offset:29696\n"
    "v_exp_f32 v138, v138\n"
    "v_mfma_f32_16x16x32_bf16 v[52:55], v[104:107], v[252:255], v[52:55]\n"
    "s_waitcnt lgkmcnt(4)\n"
    "v_exp_f32 v139, v139\n"
    "v_mfma_f32_16x16x32_bf16 v[56:59], v[108:111], v[248:251], v[56:59]\n"
    "ds_read_b128 v[104:107], v2 offset:14336\n"
    "v_exp_f32 v140, v140\n"
    "v_mfma_f32_16x16x32_bf16 v[60:63], v[108:111], v[252:255], v[60:63]\n"
    "s_waitcnt lgkmcnt(4)\n"
    "v_exp_f32 v141, v141\n"
    "v_mfma_f32_16x16x32_bf16 v[48:51], v[112:115], v[64:67], v[48:51]\n"
    "ds_read_b128 v[108:111], v2 offset:30720\n"
    "v_exp_f32 v142, v142\n"
    "v_mfma_f32_16x16x32_bf16 v[52:55], v[112:115], v[68:71], v[52:55]\n"
    "s_waitcnt lgkmcnt(4)\n"
    "v_exp_f32 v143, v143\n"
    "v_fmac_f32 v40, v132, v120\n"
    "v_mfma_f32_16x16x32_bf16 v[56:59], v[116:119], v[64:67], v[56:59]\n"
    "ds_read_b128 v[112:115], v2 offset:15360\n"
    "v_fmac_f32 v41, v133, v120\n"
    "v_fmac_f32 v42, v134, v120\n"
    "v_fmac_f32 v43, v135, v120\n"
    "v_mfma_f32_16x16x32_bf16 v[60:63], v[116:119], v[68:71], v[60:63]\n"
    "s_waitcnt lgkmcnt(4)\n"
    "v_fmac_f32 v44, v132, v124\n"
    "v_fmac_f32 v45, v133, v124\n"
    "v_fmac_f32 v46, v134, v124\n"
    "v_mfma_f32_16x16x32_bf16 v[48:51], v[96:99], v[72:75], v[48:51]\n"
    "ds_read_b128 v[116:119], v2 offset:31744\n"
    "v_fmac_f32 v47, v135, v124\n"
    "ds_read_b128 v[128:131], v9 offset:256\n"
    "ds_read_b128 v[132:135], v9 offset:320\n"
    "v_fma_f32 v136, v136, v122, v122\n"
    "v_fma_f32 v137, v137, v122, v122\n"
    "v_mfma_f32_16x16x32_bf16 v[52:55], v[96:99], v[76:79], v[52:55]\n"
    "s_waitcnt lgkmcnt(6)\n"
    "v_fma_f32 v138, v138, v122, v122\n"
    "v_fma_f32 v139, v139, v122, v122\n"
    "v_fma_f32 v140, v140, v126, v126\n"
    "v_mfma_f32_16x16x32_bf16 v[56:59], v[100:103], v[72:75], v[56:59]\n"
    "v_fma_f32 v141, v141, v126, v126\n"
    "v_fma_f32 v142, v142, v126, v126\n"
    "v_fma_f32 v143, v143, v126, v126\n"
    "v_mfma_f32_16x16x32_bf16 v[60:63], v[100:103], v[76:79], v[60:63]\n"
    "s_waitcnt lgkmcnt(5)\n"
    "v_rcp_f32 v136, v136\n"
    "v_mfma_f32_16x16x32_bf16 v[48:51], v[104:107], v[80:83], v[48:51]\n"
    "v_rcp_f32 v137, v137\n"
    "v_mfma_f32_16x16x32_bf16 v[52:55], v[104:107], v[84:87], v[52:55]\n"
    "s_waitcnt lgkmcnt(4)\n"
    "v_rcp_f32 v138, v138\n"
    "v_mfma_f32_16x16x32_bf16 v[56:59], v[108:111], v[80:83], v[56:59]\n"
    "v_rcp_f32 v139, v139\n"
    "v_mfma_f32_16x16x32_bf16 v[60:63], v[108:111], v[84:87], v[60:63]\n"
    "s_waitcnt lgkmcnt(3)\n"
    "v_rcp_f32 v140, v140\n"
    "v_mfma_f32_16x16x32_bf16 v[48:51], v[112:115], v[88:91], v[48:51]\n"
    "v_rcp_f32 v141, v141\n"
    "v_mfma_f32_16x16x32_bf16 v[52:55], v[112:115], v[92:95], v[52:55]\n"
    "s_waitcnt lgkmcnt(2)\n"
    "v_rcp_f32 v142, v142\n"
    "v_mfma_f32_16x16x32_bf16 v[56:59], v[116:119], v[88:91], v[56:59]\n"
    "v_rcp_f32 v143, v143\n"
    "v_mul_f32 v32, v32, v40\n"
    "v_mfma_f32_16x16x32_bf16 v[60:63], v[116:119], v[92:95], v[60:63]\n"
    "v_mul_f32 v33, v33, v41\n"
    "v_mul_f32 v34, v34, v42\n"
    "v_mul_f32 v35, v35, v43\n"
    "s_mov_b64 exec, 1\n"
    "ds_add_u32 v5, v5 offset:20\n"
    "s_mov_b64 exec, -1\n"
    "s_setprio 2\n"
    "s_mov_b32 s55, 0x100000\n"
    ".Lk_wl17:\n"
    "ds_read_b32 v12, v5\n"
    "s_waitcnt lgkmcnt(0)\n"
    "v_readfirstlane_b32 s50, v12\n"
    "s_cmp_ge_u32 s50, 1282048\n"
    "s_cbranch_scc1 .Lk_wd18\n"
    "s_sleep 1\n"
    "s_sub_u32 s55, s55, 1\n"
    "s_cbranch_scc0 .Lk_wl17\n"
    ".Lk_wd18:\n"
    "v_mul_f32 v36, v36, v44\n"
    "v_mul_f32 v37, v37, v45\n"
    "v_mul_f32 v38, v38, v46\n"
    "v_mul_f32 v39, v39, v47\n"
    "v_mul_f32 v32, v32, v136\n"
    "v_mul_f32 v33, v33, v137\n"
    "v_mul_f32 v34, v34, v138\n"
    "v_mul_f32 v35, v35, v139\n"
    "v_mul_f32 v36, v36, v140\n"
    "v_mul_f32 v37, v37, v141\n"
    "v_mul_f32 v38, v38, v142\n"
    "v_mul_f32 v39, v39, v143\n"
    "v_cvt_pk_bf16_f32 v40, v32, v33\n"
    "v_cvt_pk_bf16_f32 v42, v36, v37\n"
    "v_cvt_pk_bf16_f32 v41, v34, v35\n"
    "v_cvt_pk_bf16_f32 v43, v38, v39\n"
    "v_permlane16_swap_b32 v40, v42\n"
    "s_nop 0\n"
    "v_permlane16_swap_b32 v41, v43\n"
    "buffer_store_dwordx4 v[40:43], v6, s[20:23], s45 offen offset:512 sc1\n"
    "v_fmac_f32 v48, v128, v120\n"
    "v_fmac_f32 v49, v129, v120\n"
    "v_fmac_f32 v50, v130, v120\n"
    "v_fmac_f32 v51, v131, v120\n"
    "v_fmac_f32 v52, v128, v124\n"
    "v_fmac_f32 v53, v129, v124\n"
    "v_fmac_f32 v54, v130, v124\n"
    "v_fmac_f32 v55, v131, v124\n"
    "v_mul_f32 v136, v48, v121\n"
    "v_mul_f32 v137, v49, v121\n"
    "v_mul_f32 v138, v50, v121\n"
    "v_mul_f32 v139, v51, v121\n"
    "v_mul_f32 v140, v52, v125\n"
    "v_mul_f32 v141, v53, v125\n"
    "v_mul_f32 v142, v54, v125\n"
    "v_mul_f32 v143, v55, v125\n"
    "v_exp_f32 v136, v136\n"
    "v_exp_f32 v137, v137\n"
    "v_exp_f32 v138, v138\n"
    "v_exp_f32 v139, v139\n"
    "v_exp_f32 v140, v140\n"
    "v_exp_f32 v141, v141\n"
    "v_exp_f32 v142, v142\n"
    "v_exp_f32 v143, v143\n"
    "v_fmac_f32 v56, v132, v120\n"
    "v_fmac_f32 v57, v133, v120\n"
    "v_fmac_f32 v58, v134, v120\n"
    "v_fmac_f32 v59, v135, v120\n"
    "v_fmac_f32 v60, v132, v124\n"
    "v_fmac_f32 v61, v133, v124\n"
    "v_fmac_f32 v62, v134, v124\n"
    "v_fmac_f32 v63, v135, v124\n"
    "v_fma_f32 v136, v136, v122, v122\n"
    "v_fma_f32 v137, v137, v122, v122\n"
    "v_fma_f32 v138, v138, v122, v122\n"
    "v_fma_f32 v139, v139, v122, v122\n"
    "v_fma_f32 v140, v140, v126, v126\n"
    "v_fma_f32 v141, v141, v126, v126\n"
    "v_fma_f32 v142, v142, v126, v126\n"
    "v_fma_f32 v143, v143, v126, v126\n"
    "v_rcp_f32 v136, v136\n"
    "v_rcp_f32 v137, v137\n"
    "v_rcp_f32 v138, v138\n"
    "v_rcp_f32 v139, v139\n"
    "v_rcp_f32 v140, v140\n"
    "v_rcp_f32 v141, v141\n"
    "v_rcp_f32 v142, v142\n"
    "v_rcp_f32 v143, v143\n"
    "v_mul_f32 v48, v48, v56\n"
    "v_mul_f32 v49, v49, v57\n"
    "v_mul_f32 v50, v50, v58\n"
    "v_mul_f32 v51, v51, v59\n"
    "v_mul_f32 v52, v52, v60\n"
    "v_mul_f32 v53, v53, v61\n"
    "v_mul_f32 v54, v54, v62\n"
    "v_mul_f32 v55, v55, v63\n"
    "v_mul_f32 v48, v48, v136\n"
    "v_mul_f32 v49, v49, v137\n"
    "v_mul_f32 v50, v50, v138\n"
    "v_mul_f32 v51, v51, v139\n"
    "v_mul_f32 v52, v52, v140\n"
    "v_mul_f32 v53, v53, v141\n"
    "v_mul_f32 v54, v54, v142\n"
    "v_mul_f32 v55, v55, v143\n"
    "v_cvt_pk_bf16_f32 v56, v48, v49\n"
    "v_cvt_pk_bf16_f32 v58, v52, v53\n"
    "v_cvt_pk_bf16_f32 v57, v50, v51\n"
    "v_cvt_pk_bf16_f32 v59, v54, v55\n"
    "v_permlane16_swap_b32 v56, v58\n"
    "s_nop 0\n"
    "v_permlane16_swap_b32 v57, v59\n"
    "buffer_store_dwordx4 v[56:59], v6, s[20:23], s45 offen offset:1024 sc1\n"
    "s_mul_i32 s52, s39, 12288\n"
    "s_add_u32 m0, s52, 98304\n"
    "s_add_u32 s52, s52, s57\n"
    "s_lshl_b32 s54, s59, 16\n"
    "s_add_u32 s52, s52, s54\n"
    "s_add_u32 s52, s52, 12288\n"
    "buffer_load_dwordx4 v1, s[20:23], s52 offen sc1 lds\n"
    "buffer_load_dwordx4 v1, s[20:23], s52 offen offset:1024 sc1 lds\n"
    "buffer_load_dwordx4 v1, s[20:23], s52 offen offset:2048 sc1 lds\n"
    "buffer_load_dwordx4 v1, s[20:23], s52 offen offset:3072 sc1 lds\n"
    "s_add_u32 m0, m0, 0x1000\n"
    "s_add_u32 s52, s52, 0x1000\n"
    "buffer_load_dwordx4 v1, s[20:23], s52 offen sc1 lds\n"
    "buffer_load_dwordx4 v1, s[20:23], s52 offen offset:1024 sc1 lds\n"
    "buffer_load_dwordx4 v1, s[20:23], s52 offen offset:2048 sc1 lds\n"
    "buffer_load_dwordx4 v1, s[20:23], s52 offen offset:3072 sc1 lds\n"
    "s_add_u32 m0, m0, 0x1000\n"
    "s_add_u32 s52, s52, 0x1000\n"
    "buffer_load_dwordx4 v1, s[20:23], s52 offen sc1 lds\n"
    "buffer_load_dwordx4 v1, s[20:23], s52 offen offset:1024 sc1 lds\n"
    "buffer_load_dwordx4 v1, s[20:23], s52 offen offset:2048 sc1 lds\n"
    "buffer_load_dwordx4 v1, s[20:23], s52 offen offset:3072 sc1 lds\n"
    "s_lshl_b32 s52, s39, 10\n"
    "s_add_u32 m0, s52, 147456\n"
    "s_add_u32 s52, s52, s57\n"
    "s_add_u32 s52, s52, 86016\n"
    "buffer_load_dwordx4 v1, s[20:23], s52 offen sc1 lds\n"
    "s_lshl_b32 s49, s3, 13\n"
    "s_add_u32 s52, s49, s57\n"
    "s_mul_i32 s54, s59, 73728\n"
    "s_add_u32 s52, s52, s54\n"
    "s_add_u32 s52, s52, 36864\n"
    "s_lshl_b32 s53, s56, 1\n"
    "s_add_u32 s53, s53, s40\n"
    "s_lshl_b32 s53, s53, 2\n"
    "s_add_u32 s53, s53, s58\n"
    "s_lshl_b32 s53, s53, 1\n"
    "s_and_b32 s54, s3, 1\n"
    "s_add_u32 s43, s53, s54\n"
    "s_mul_i32 s41, s43, 43\n"
    "s_mul_i32 s54, s59, 22\n"
    "s_add_u32 s41, s41, s54\n"
    "s_lshl_b32 s41, s41, 10\n"
    "s_add_u32 s41, s41, 10485760\n"
    "s_lshl_b32 s42, s56, 5\n"
    "s_add_u32 s42, s42, s33\n"
    "s_andn2_b32 s42, s42, 1\n"
    "s_lshl_b32 s42, s42, 1\n"
    "s_add_u32 s42, s42, s40\n"
    "s_lshl_b32 s42, s42, 7\n"
    "s_mul_i32 s54, s59, 56\n"
    "s_add_u32 s42, s42, s54\n"
    "s_add_u32 s42, s42, 1474560\n"
    "v_lshlrev_b32 v13, 2, v0\n"
    "s_mul_i32 s53, s59, 26624\n"
    "s_add_u32 s53, s53, 98304\n"
    "v_add_u32 v2, s53, v1\n"
    "v_xor_b32 v3, 0x800, v2\n"
    "s_mul_i32 s53, s59, 18432\n"
    "v_add_u32 v34, s53, v1\n"
    "v_xor_b32 v35, 0x800, v34\n"
    "v_add_u32 v32, 0x10000, v34\n"
    "v_add_u32 v33, 0x10000, v35\n"
    "v_mov_b32 v14, 0\n"
    "s_waitcnt vmcnt(13)\n"
    "s_mov_b64 exec, 15\n"
    "buffer_atomic_swap v10, v11, s[20:23], s44 offen sc1\n"
    "s_mov_b64 exec, -1\n"
    "s_branch .Lk_d_post0b\n"
    ".Lk_d_post0:\n"
    "s_mov_b32 s55, 0x100000\n"
    ".Lk_wl19:\n"
    "ds_read_b32 v12, v5\n"
    "s_waitcnt lgkmcnt(0)\n"
    "v_readfirstlane_b32 s50, v12\n"
    "s_cmp_ge_u32 s50, 1282048\n"
    "s_cbranch_scc1 .Lk_wd20\n"
    "s_sleep 1\n"
    "s_sub_u32 s55, s55, 1\n"
    "s_cbranch_scc0 .Lk_wl19\n"
    ".Lk_wd20:\n"
    "s_mul_i32 s52, s39, 12288\n"
    "s_add_u32 m0, s52, 98304\n"
    "s_add_u32 s52, s52, s57\n"
    "s_lshl_b32 s54, s59, 16\n"
    "s_add_u32 s52, s52, s54\n"
    "s_add_u32 s52, s52, 12288\n"
    "buffer_load_dwordx4 v1, s[20:23], s52 offen sc1 lds\n"
    "buffer_load_dwordx4 v1, s[20:23], s52 offen offset:1024 sc1 lds\n"
    "buffer_load_dwordx4 v1, s[20:23], s52 offen offset:2048 sc1 lds\n"
    "buffer_load_dwordx4 v1, s[20:23], s52 offen offset:3072 sc1 lds\n"
    "s_add_u32 m0, m0, 0x1000\n"
    "s_add_u32 s52, s52, 0x1000\n"
    "buffer_load_dwordx4 v1, s[20:23], s52 offen sc1 lds\n"
    "buffer_load_dwordx4 v1, s[20:23], s52 offen offset:1024 sc1 lds\n"
    "buffer_load_dwordx4 v1, s[20:23], s52 offen offset:2048 sc1 lds\n"
    "buffer_load_dwordx4 v1, s[20:23], s52 offen offset:3072 sc1 lds\n"
    "s_add_u32 m0, m0, 0x1000\n"
    "s_add_u32 s52, s52, 0x1000\n"
    "buffer_load_dwordx4 v1, s[20:23], s52 offen sc1 lds\n"
    "buffer_load_dwordx4 v1, s[20:23], s52 offen offset:1024 sc1 lds\n"
    "buffer_load_dwordx4 v1, s[20:23], s52 offen offset:2048 sc1 lds\n"
    "buffer_load_dwordx4 v1, s[20:23], s52 offen offset:3072 sc1 lds\n"
    "s_lshl_b32 s52, s39, 10\n"
    "s_add_u32 m0, s52, 147456\n"
    "s_add_u32 s52, s52, s57\n"
    "s_add_u32 s52, s52, 86016\n"
    "buffer_load_dwordx4 v1, s[20:23], s52 offen sc1 lds\n"
    "s_lshl_b32 s49, s3, 13\n"
    "s_add_u32 s52, s49, s57\n"
    "s_mul_i32 s54, s59, 73728\n"
    "s_add_u32 s52, s52, s54\n"
    "s_add_u32 s52, s52, 36864\n"
    "s_lshl_b32 s53, s56, 1\n"
    "s_add_u32 s53, s53, s40\n"
    "s_lshl_b32 s53, s53, 2\n"
    "s_add_u32 s53, s53, s58\n"
    "s_lshl_b32 s53, s53, 1\n"
    "s_and_b32 s54, s3, 1\n"
    "s_add_u32 s43, s53, s54\n"
    "s_mul_i32 s41, s43, 43\n"
    "s_mul_i32 s54, s59, 22\n"
    "s_add_u32 s41, s41, s54\n"
    "s_lshl_b32 s41, s41, 10\n"
    "s_add_u32 s41, s41, 10485760\n"
    "s_lshl_b32 s42, s56, 5\n"
    "s_add_u32 s42, s42, s33\n"
    "s_andn2_b32 s42, s42, 1\n"
    "s_lshl_b32 s42, s42, 1\n"
    "s_add_u32 s42, s42, s40\n"
    "s_lshl_b32 s42, s42, 7\n"
    "s_mul_i32 s54, s59, 56\n"
    "s_add_u32 s42, s42, s54\n"
    "s_add_u32 s42, s42, 1474560\n"
    "v_lshlrev_b32 v13, 2, v0\n"
    "s_mul_i32 s53, s59, 26624\n"
    "s_add_u32 s53, s53, 98304\n"
    "v_add_u32 v2, s53, v1\n"
    "v_xor_b32 v3, 0x800, v2\n"
    "s_mul_i32 s53, s59, 18432\n"
    "v_add_u32 v34, s53, v1\n"
    "v_xor_b32 v35, 0x800, v34\n"
    "v_add_u32 v32, 0x10000, v34\n"
    "v_add_u32 v33, 0x10000, v35\n"
    "v_mov_b32 v14, 0\n"
    ".Lk_d_post0b:\n"
    "s_setprio 3\n"
    "s_mov_b32 s55, 0x100000\n"
    ".Lk_wl21:\n"
    "ds_read_b32 v12, v5 offset:12\n"
    "s_waitcnt lgkmcnt(0)\n"
    "v_readfirstlane_b32 s50, v12\n"
    "s_cmp_ge_u32 s50, 1282048\n"
    "s_cbranch_scc1 .Lk_wd22\n"
    "s_sleep 1\n"
    "s_sub_u32 s55, s55, 1\n"
    "s_cbranch_scc0 .Lk_wl21\n"
    ".Lk_wd22:\n"
    "s_setprio 0\n"
    "s_add_u32 m0, s49, 0\n"
    "s_add_u32 s54, s52, 0\n"
    "buffer_load_dwordx4 v1, s[20:23], s54 offen sc1 lds\n"
    "buffer_load_dwordx4 v1, s[20:23], s54 offen offset:1024 sc1 lds\n"
    "buffer_load_dwordx4 v1, s[20:23], s54 offen offset:2048 sc1 lds\n"
    "buffer_load_dwordx4 v1, s[20:23], s54 offen offset:3072 sc1 lds\n"
    "s_add_u32 m0, s49, 4096\n"
    "s_add_u32 s54, s52, 4096\n"
    "buffer_load_dwordx4 v1, s[20:23], s54 offen sc1 lds\n"
    "buffer_load_dwordx4 v1, s[20:23], s54 offen offset:1024 sc1 lds\n"
    "buffer_load_dwordx4 v1, s[20:23], s54 offen offset:2048 sc1 lds\n"
    "buffer_load_dwordx4 v1, s[20:23], s54 offen offset:3072 sc1 lds\n"
    "s_setprio 3\n"
    "s_mov_b64 exec, 0x7fff\n"
    "buffer_atomic_or v14, v13, s[20:23], s42 offen sc0 sc1\n"
    "s_mov_b64 exec, -1\n"
    "s_setprio 3\n"
    "s_mov_b32 s55, 0x100000\n"
    ".Lk_wl23:\n"
    "ds_read_b32 v12, v5 offset:16\n"
    "s_waitcnt lgkmcnt(0)\n"
    "v_readfirstlane_b32 s50, v12\n"
    "s_cmp_ge_u32 s50, 1282048\n"
    "s_cbranch_scc1 .Lk_wd24\n"
    "s_sleep 1\n"
    "s_sub_u32 s55, s55, 1\n"
    "s_cbranch_scc0 .Lk_wl23\n"
    ".Lk_wd24:\n"
    "s_setprio 0\n"
    "s_add_u32 m0, s49, 32768\n"
    "s_add_u32 s54, s52, 16384\n"
    "buffer_load_dwordx4 v1, s[20:23], s54 offen sc1 lds\n"
    "buffer_load_dwordx4 v1, s[20:23], s54 offen offset:1024 sc1 lds\n"
    "buffer_load_dwordx4 v1, s[20:23], s54 offen offset:2048 sc1 lds\n"
    "buffer_load_dwordx4 v1, s[20:23], s54 offen offset:3072 sc1 lds\n"
    "s_add_u32 m0, s49, 36864\n"
    "s_add_u32 s54, s52, 20480\n"
    "buffer_load_dwordx4 v1, s[20:23], s54 offen sc1 lds\n"
    "buffer_load_dwordx4 v1, s[20:23], s54 offen offset:1024 sc1 lds\n"
    "buffer_load_dwordx4 v1, s[20:23], s54 offen offset:2048 sc1 lds\n"
    "buffer_load_dwordx4 v1, s[20:23], s54 offen offset:3072 sc1 lds\n"
    "s_setprio 3\n"
    "s_waitcnt vmcnt(9)\n"
    "s_mov_b64 exec, 1\n"
    "ds_add_u32 v5, v5 offset:24\n"
    "s_mov_b64 exec, -1\n"
    "s_mov_b64 exec, 0x7fff\n"
    "s_mov_b32 s55, 0x8000\n"
    "s_waitcnt vmcnt(8)\n"
    "v_cmp_ne_u32 vcc, s38, v14\n"
    "s_cbranch_vccz .Lk_pd26\n"
    ".Lk_pl25:\n"
    "v_mov_b32 v14, 0\n"
    "buffer_atomic_or v14, v13, s[20:23], s42 offen sc0 sc1\n"
    "s_sub_u32 s55, s55, 1\n"
    "s_waitcnt vmcnt(0)\n"
    "v_cmp_ne_u32 vcc, s38, v14\n"
    "s_cbranch_scc1 .Lk_pd26\n"
    "s_cbranch_vccnz .Lk_pl25\n"
    ".Lk_pd26:\n"
    "s_mov_b64 exec, -1\n"
    "buffer_load_dwordx4 v[36:39], v1, s[20:23], s41 offen sc1\n"
    "buffer_load_dwordx4 v[40:43], v1, s[20:23], s41 offen offset:1024 sc1\n"
    "buffer_load_dwordx4 v[44:47], v1, s[20:23], s41 offen offset:2048 sc1\n"
    "buffer_load_dwordx4 v[48:51], v1, s[20:23], s41 offen offset:3072 sc1\n"
    "s_add_u32 s41, s41, 0x1000\n"
    "buffer_load_dwordx4 v[52:55], v1, s[20:23], s41 offen sc1\n"
    "buffer_load_dwordx4 v[56:59], v1, s[20:23], s41 offen offset:1024 sc1\n"
    "buffer_load_dwordx4 v[60:63], v1, s[20:23], s41 offen offset:2048 sc1\n"
    "buffer_load_dwordx4 v[64:67], v1, s[20:23], s41 offen offset:3072 sc1\n"
    "s_mov_b32 s55, 0x100000\n"
    ".Lk_wl27:\n"
    "ds_read_b32 v12, v5 offset:24\n"
    "s_waitcnt lgkmcnt(0)\n"
    "v_readfirstlane_b32 s50, v12\n"
    "s_cmp_ge_u32 s50, 641024\n"
    "s_cbranch_scc1 .Lk_wd28\n"
    "s_sleep 1\n"
    "s_sub_u32 s55, s55, 1\n"
    "s_cbranch_scc0 .Lk_wl27\n"
    ".Lk_wd28:\n"
    "ds_read_b128 v[192:195], v2\n"
    "ds_read_b128 v[196:199], v2 offset:1024\n"
    "ds_read_b128 v[200:203], v3\n"
    "ds_read_b128 v[204:207], v3 offset:1024\n"
    "ds_read_b128 v[208:211], v2 offset:4096\n"
    "ds_read_b128 v[212:215], v2 offset:5120\n"
    "ds_read_b128 v[216:219], v3 offset:4096\n"
    "ds_read_b128 v[220:223], v3 offset:5120\n"
    "ds_read_b128 v[224:227], v2 offset:8192\n"
    "ds_read_b128 v[228:231], v2 offset:9216\n"
    "ds_read_b128 v[232:235], v3 offset:8192\n"
    "ds_read_b128 v[236:239], v3 offset:9216\n"
    "ds_read_b128 v[240:243], v2 offset:12288\n"
    "ds_read_b128 v[244:247], v2 offset:13312\n"
    "ds_read_b128 v[248:251], v3 offset:12288\n"
    "ds_read_b128 v[252:255], v3 offset:13312\n"
    "ds_read_b128 v[128:131], v2 offset:16384\n"
    "ds_read_b128 v[132:135], v2 offset:17408\n"
    "ds_read_b128 v[136:139], v3 offset:16384\n"
    "ds_read_b128 v[140:143], v3 offset:17408\n"
    "ds_read_b128 v[112:115], v2 offset:20480\n"
    "ds_read_b128 v[116:119], v2 offset:21504\n"
    "ds_read_b128 v[120:123], v3 offset:20480\n"
    "ds_read_b128 v[124:127], v3 offset:21504\n"
    "ds_read_b128 v[96:99], v34\n"
    "ds_read_b128 v[100:103], v34 offset:1024\n"
    "ds_read_b128 v[104:107], v35\n"
    "ds_read_b128 v[108:111], v35 offset:1024\n"
    "ds_read_b128 v[80:83], v34 offset:4096\n"
    "ds_read_b128 v[84:87], v34 offset:5120\n"
    "ds_read_b128 v[88:91], v35 offset:4096\n"
    "ds_read_b128 v[92:95], v35 offset:5120\n"
    "s_setprio 3\n"
    "s_mov_b32 s55, 0x100000\n"
    ".Lk_wl29:\n"
    "ds_read_b32 v12, v5 offset:20\n"
    "s_waitcnt lgkmcnt(0)\n"
    "v_readfirstlane_b32 s50, v12\n"
    "s_cmp_ge_u32 s50, 1282048\n"
    "s_cbranch_scc1 .Lk_wd30\n"
    "s_sleep 1\n"
    "s_sub_u32 s55, s55, 1\n"
    "s_cbranch_scc0 .Lk_wl29\n"
    ".Lk_wd30:\n"
    "s_setprio 0\n"
    "s_add_u32 m0, s49, 65536\n"
    "s_add_u32 s54, s52, 32768\n"
    "buffer_load_dwordx4 v1, s[20:23], s54 offen sc1 lds\n"
    "buffer_load_dwordx4 v1, s[20:23], s54 offen offset:1024 sc1 lds\n"
    "buffer_load_dwordx4 v1, s[20:23], s54 offen offset:2048 sc1 lds\n"
    "buffer_load_dwordx4 v1, s[20:23], s54 offen offset:3072 sc1 lds\n"
    "s_add_u32 m0, s49, 69632\n"
    "s_add_u32 s54, s52, 36864\n"
    "buffer_load_dwordx4 v1, s[20:23], s54 offen sc1 lds\n"
    "buffer_load_dwordx4 v1, s[20:23], s54 offen offset:1024 sc1 lds\n"
    "buffer_load_dwordx4 v1, s[20:23], s54 offen offset:2048 sc1 lds\n"
    "buffer_load_dwordx4 v1, s[20:23], s54 offen offset:3072 sc1 lds\n"
    "s_setprio 3\n"
    "s_mov_b32 s71, 0\n"
    "s_lshl_b32 s53, s43, 3\n"
    "s_and_b32 s54, s33, 7\n"
    "s_add_u32 s53, s53, s54\n"
    "s_lshl_b32 s53, s53, 1\n"
    "s_add_u32 s53, s53, s59\n"
    "s_lshl_b32 s53, s53, 10\n"
    "s_add_u32 s53, s53, 8388608\n"
    "v_and_b32 v12, 0x1ff, v7\n"
    "v_bfe_u32 v13, v0, 4, 1\n"
    "v_lshl_or_b32 v12, v13, 3, v12\n"
    "s_lshl_b32 s47, s39, 11\n"
    "s_xor_b32 s54, s40, 1\n"
    "s_mul_i32 s54, s54, 151552\n"
    "v_add_u32 v76, s47, v1\n"
    "v_xor_b32 v77, 0x1000, v76\n"
    "v_add_u32 v76, s54, v76\n"
    "v_add_u32 v77, s54, v77\n"
    "s_mov_b32 s24, s16\n"
    "s_and_b32 s25, s17, 0xffff\n"
    "s_mov_b32 s26, 0x200000\n"
    "s_mov_b32 s27, 0x20000\n"
    "s_lshl_b32 s47, s43, 7\n"
    "s_and_b32 s54, s33, 7\n"
    "s_add_u32 s47, s47, s54\n"
    "s_lshl_b32 s47, s47, 1\n"
    "s_add_u32 s47, s47, s59\n"
    "s_lshl_b32 s47, s47, 6\n"
    "v_and_b32 v14, 15, v0\n"
    "v_lshlrev_b32 v14, 10, v14\n"
    "v_lshl_or_b32 v14, v13, 5, v14\n"
    "v_lshrrev_b32 v15, 5, v0\n"
    "v_lshl_or_b32 v78, v15, 4, v14\n"
    "s_setprio 0\n"
    "s_waitcnt vmcnt(15)\n"
    "v_mfma_f32_16x16x32_bf16 v[24:27], v[152:155], v[36:39], 0\n"
    "s_add_u32 s41, s41, 0x1000\n"
    "buffer_load_dwordx4 v[68:71], v1, s[20:23], s41 offen sc1\n"
    "ds_read_b128 v[152:155], v35 offset:8192\n"
    "v_mfma_f32_16x16x32_bf16 v[28:31], v[156:159], v[36:39], 0\n"
    "ds_read_b128 v[156:159], v35 offset:9216\n"
    "v_mfma_f32_16x16x32_bf16 v[16:19], v[144:147], v[36:39], 0\n"
    "ds_read_b128 v[144:147], v34 offset:8192\n"
    "v_mfma_f32_16x16x32_bf16 v[20:23], v[148:151], v[36:39], 0\n"
    "ds_read_b128 v[148:151], v34 offset:9216\n"
    "s_mov_b64 exec, 1\n"
    "ds_add_u32 v5, v5 offset:40\n"
    "s_mov_b64 exec, -1\n"
    "s_waitcnt vmcnt(15)\n"
    "v_mfma_f32_16x16x32_bf16 v[24:27], v[168:171], v[40:43], v[24:27]\n"
    "buffer_load_dwordx4 v[36:39], v1, s[20:23], s41 offen offset:1024 sc1\n"
    "ds_read_b128 v[168:171], v35 offset:12288\n"
    "v_mfma_f32_16x16x32_bf16 v[28:31], v[172:175], v[40:43], v[28:31]\n"
    "ds_read_b128 v[172:175], v35 offset:13312\n"
    "v_mfma_f32_16x16x32_bf16 v[16:19], v[160:163], v[40:43], v[16:19]\n"
    "ds_read_b128 v[160:163], v34 offset:12288\n"
    "v_mfma_f32_16x16x32_bf16 v[20:23], v[164:167], v[40:43], v[20:23]\n"
    "ds_read_b128 v[164:167], v34 offset:13312\n"
    "s_setprio 3\n"
    "s_mov_b32 s55, 0x100000\n"
    ".Lk_wl31:\n"
    "ds_read_b32 v13, v5 offset:40\n"
    "s_waitcnt lgkmcnt(0)\n"
    "v_readfirstlane_b32 s50, v13\n"
    "s_cmp_ge_u32 s50, 641024\n"
    "s_cbranch_scc1 .Lk_wd32\n"
    "s_sleep 1\n"
    "s_sub_u32 s55, s55, 1\n"
    "s_cbranch_scc0 .Lk_wl31\n"
    ".Lk_wd32:\n"
    "s_setprio 0\n"
    "s_waitcnt vmcnt(15)\n"
    "v_mfma_f32_16x16x32_bf16 v[24:27], v[184:187], v[44:47], v[24:27]\n"
    "buffer_load_dwordx4 v[40:43], v1, s[20:23], s41 offen offset:2048 sc1\n"
    "ds_read_b128 v[184:187], v35 offset:32768\n"
    "v_mfma_f32_16x16x32_bf16 v[28:31], v[188:191], v[44:47], v[28:31]\n"
    "ds_read_b128 v[188:191], v35 offset:33792\n"
    "v_mfma_f32_16x16x32_bf16 v[16:19], v[176:179], v[44:47], v[16:19]\n"
    "ds_read_b128 v[176:179], v34 offset:32768\n"
    "v_mfma_f32_16x16x32_bf16 v[20:23], v[180:183], v[44:47], v[20:23]\n"
    "ds_read_b128 v[180:183], v34 offset:33792\n"
    "s_waitcnt vmcnt(15)\n"
    "v_mfma_f32_16x16x32_bf16 v[24:27], v[200:203], v[48:51], v[24:27]\n"
    "buffer_load_dwordx4 v[44:47], v1, s[20:23], s41 offen offset:3072 sc1\n"
    "ds_read_b128 v[200:203], v35 offset:36864\n"
    "v_mfma_f32_16x16x32_bf16 v[28:31], v[204:207], v[48:51], v[28:31]\n"
    "ds_read_b128 v[204:207], v35 offset:37888\n"
    "v_mfma_f32_16x16x32_bf16 v[16:19], v[192:195], v[48:51], v[16:19]\n"
    "ds_read_b128 v[192:195], v34 offset:36864\n"
    "v_mfma_f32_16x16x32_bf16 v[20:23], v[196:199], v[48:51], v[20:23]\n"
    "ds_read_b128 v[196:199], v34 offset:37888\n"
    "s_waitcnt vmcnt(15)\n"
    "v_mfma_f32_16x16x32_bf16 v[24:27], v[216:219], v[52:55], v[24:27]\n"
    "s_add_u32 s41, s41, 0x1000\n"
    "buffer_load_dwordx4 v[48:51], v1, s[20:23], s41 offen sc1\n"
    "ds_read_b128 v[216:219], v35 offset:40960\n"
    "v_mfma_f32_16x16x32_bf16 v[28:31], v[220:223], v[52:55], v[28:31]\n"
    "ds_read_b128 v[220:223], v35 offset:41984\n"
    "v_mfma_f32_16x16x32_bf16 v[16:19], v[208:211], v[52:55], v[16:19]\n"
    "ds_read_b128 v[208:211], v34 offset:40960\n"
    "v_mfma_f32_16x16x32_bf16 v[20:23], v[212:215], v[52:55], v[20:23]\n"
    "ds_read_b128 v[212:215], v34 offset:41984\n"
    "s_waitcnt vmcnt(15)\n"
    "v_mfma_f32_16x16x32_bf16 v[24:27], v[232:235], v[56:59], v[24:27]\n"
    "buffer_load_dwordx4 v[52:55], v1, s[20:23], s41 offen offset:1024 sc1\n"
    "ds_read_b128 v[232:235], v35 offset:45056\n"
    "v_mfma_f32_16x16x32_bf16 v[28:31], v[236:239], v[56:59], v[28:31]\n"
    "ds_read_b128 v[236:239], v35 offset:46080\n"
    "v_mfma_f32_16x16x32_bf16 v[16:19], v[224:227], v[56:59], v[16:19]\n"
    "ds_read_b128 v[224:227], v34 offset:45056\n"
    "v_mfma_f32_16x16x32_bf16 v[20:23], v[228:231], v[56:59], v[20:23]\n"
    "ds_read_b128 v[228:231], v34 offset:46080\n"
    "s_waitcnt vmcnt(6)\n"
    "s_mov_b64 exec, 1\n"
    "ds_add_u32 v5, v5 offset:28\n"
    "s_mov_b64 exec, -1\n"
    "s_setprio 3\n"
    "s_setprio 3\n"
    "s_mov_b32 s55, 0x100000\n"
    ".Lk_wl33:\n"
    "ds_read_b32 v13, v5 offset:28\n"
    "s_waitcnt lgkmcnt(0)\n"
    "v_readfirstlane_b32 s50, v13\n"
    "s_cmp_ge_u32 s50, 641024\n"
    "s_cbranch_scc1 .Lk_wd34\n"
    "s_sleep 1\n"
    "s_sub_u32 s55, s55, 1\n"
    "s_cbranch_scc0 .Lk_wl33\n"
    ".Lk_wd34:\n"
    "s_setprio 3\n"
    "v_mfma_f32_16x16x32_bf16 v[24:27], v[248:251], v[60:63], v[24:27]\n"
    "buffer_load_dwordx4 v[56:59], v1, s[20:23], s41 offen offset:2048 sc1\n"
    "ds_read_b128 v[248:251], v33\n"
    "v_mfma_f32_16x16x32_bf16 v[28:31], v[252:255], v[60:63], v[28:31]\n"
    "ds_read_b128 v[252:255], v33 offset:1024\n"
    "v_mfma_f32_16x16x32_bf16 v[16:19], v[240:243], v[60:63], v[16:19]\n"
    "buffer_load_dwordx2 v[8:9], v12, s[20:23], s53 offen sc1\n"
    "ds_read_b128 v[240:243], v32\n"
    "v_mfma_f32_16x16x32_bf16 v[20:23], v[244:247], v[60:63], v[20:23]\n"
    "ds_read_b128 v[244:247], v32 offset:1024\n"
    "v_mfma_f32_16x16x32_bf16 v[24:27], v[136:139], v[64:67], v[24:27]\n"
    "buffer_load_dwordx4 v[60:63], v1, s[20:23], s41 offen offset:3072 sc1\n"
    "ds_read_b128 v[136:139], v33 offset:4096\n"
    "v_mfma_f32_16x16x32_bf16 v[28:31], v[140:143], v[64:67], v[28:31]\n"
    "ds_read_b128 v[140:143], v33 offset:5120\n"
    "v_mfma_f32_16x16x32_bf16 v[16:19], v[128:131], v[64:67], v[16:19]\n"
    "buffer_load_dwordx2 v[10:11], v12, s[20:23], s53 offen offset:512 sc1\n"
    "ds_read_b128 v[128:131], v32 offset:4096\n"
    "v_mfma_f32_16x16x32_bf16 v[20:23], v[132:135], v[64:67], v[20:23]\n"
    "ds_read_b128 v[132:135], v32 offset:5120\n"
    "s_waitcnt vmcnt(9)\n"
    "v_mfma_f32_16x16x32_bf16 v[24:27], v[120:123], v[68:71], v[24:27]\n"
    "s_add_u32 s41, s41, 0x1000\n"
    "buffer_load_dwordx4 v[64:67], v1, s[20:23], s41 offen sc1\n"
    "ds_read_b128 v[120:123], v33 offset:8192\n"
    "v_mfma_f32_16x16x32_bf16 v[28:31], v[124:127], v[68:71], v[28:31]\n"
    "ds_read_b128 v[124:127], v33 offset:9216\n"
    "v_mfma_f32_16x16x32_bf16 v[16:19], v[112:115], v[68:71], v[16:19]\n"
    "ds_read_b128 v[112:115], v32 offset:8192\n"
    "v_mfma_f32_16x16x32_bf16 v[20:23], v[116:119], v[68:71], v[20:23]\n"
    "ds_read_b128 v[116:119], v32 offset:9216\n"
    "s_waitcnt vmcnt(9)\n"
    "v_mfma_f32_16x16x32_bf16 v[24:27], v[104:107], v[36:39], v[24:27]\n"
    "buffer_load_dwordx4 v[68:71], v1, s[20:23], s41 offen offset:1024 sc1\n"
    "ds_read_b128 v[104:107], v33 offset:12288\n"
    "v_mfma_f32_16x16x32_bf16 v[28:31], v[108:111], v[36:39], v[28:31]\n"
    "ds_read_b128 v[108:111], v33 offset:13312\n"
    "v_mfma_f32_16x16x32_bf16 v[16:19], v[96:99], v[36:39], v[16:19]\n"
    "ds_read_b128 v[96:99], v32 offset:12288\n"
    "v_mfma_f32_16x16x32_bf16 v[20:23], v[100:103], v[36:39], v[20:23]\n"
    "ds_read_b128 v[100:103], v32 offset:13312\n"
    "s_waitcnt vmcnt(9)\n"
    "v_mfma_f32_16x16x32_bf16 v[24:27], v[88:91], v[40:43], v[24:27]\n"
    "buffer_load_dwordx4 v[36:39], v1, s[20:23], s41 offen offset:2048 sc1\n"
    "ds_read_b128 v[88:91], v3 offset:49152\n"
    "v_mfma_f32_16x16x32_bf16 v[28:31], v[92:95], v[40:43], v[28:31]\n"
    "ds_read_b128 v[92:95], v3 offset:50176\n"
    "v_mfma_f32_16x16x32_bf16 v[16:19], v[80:83], v[40:43], v[16:19]\n"
    "ds_read_b128 v[80:83], v2 offset:49152\n"
    "v_mfma_f32_16x16x32_bf16 v[20:23], v[84:87], v[40:43], v[20:23]\n"
    "ds_read_b128 v[84:87], v2 offset:50176\n"
    "s_waitcnt vmcnt(9)\n"
    "v_mfma_f32_16x16x32_bf16 v[24:27], v[152:155], v[44:47], v[24:27]\n"
    "buffer_load_dwordx4 v[40:43], v1, s[20:23], s41 offen offset:3072 sc1\n"
    "v_mfma_f32_16x16x32_bf16 v[28:31], v[156:159], v[44:47], v[28:31]\n"
    "v_mfma_f32_16x16x32_bf16 v[16:19], v[144:147], v[44:47], v[16:19]\n"
    "v_mfma_f32_16x16x32_bf16 v[20:23], v[148:151], v[44:47], v[20:23]\n"
    "s_waitcnt vmcnt(9)\n"
    "v_mfma_f32_16x16x32_bf16 v[24:27], v[168:171], v[48:51], v[24:27]\n"
    "s_add_u32 s41, s41, 0x1000\n"
    "buffer_load_dwordx4 v[44:47], v1, s[20:23], s41 offen sc1\n"
    "v_mfma_f32_16x16x32_bf16 v[28:31], v[172:175], v[48:51], v[28:31]\n"
    "v_mfma_f32_16x16x32_bf16 v[16:19], v[160:163], v[48:51], v[16:19]\n"
    "v_mfma_f32_16x16x32_bf16 v[20:23], v[164:167], v[48:51], v[20:23]\n"
    "s_waitcnt vmcnt(9)\n"
    "v_mfma_f32_16x16x32_bf16 v[24:27], v[184:187], v[52:55], v[24:27]\n"
    "buffer_load_dwordx4 v[48:51], v1, s[20:23], s41 offen offset:1024 sc1\n"
    "v_mfma_f32_16x16x32_bf16 v[28:31], v[188:191], v[52:55], v[28:31]\n"
    "v_mfma_f32_16x16x32_bf16 v[16:19], v[176:179], v[52:55], v[16:19]\n"
    "v_mfma_f32_16x16x32_bf16 v[20:23], v[180:183], v[52:55], v[20:23]\n"
    "s_waitcnt vmcnt(9)\n"
    "v_mfma_f32_16x16x32_bf16 v[24:27], v[200:203], v[56:59], v[24:27]\n"
    "v_mfma_f32_16x16x32_bf16 v[28:31], v[204:207], v[56:59], v[28:31]\n"
    "v_mfma_f32_16x16x32_bf16 v[16:19], v[192:195], v[56:59], v[16:19]\n"
    "v_mfma_f32_16x16x32_bf16 v[20:23], v[196:199], v[56:59], v[20:23]\n"
    "s_waitcnt vmcnt(7)\n"
    "v_mfma_f32_16x16x32_bf16 v[24:27], v[216:219], v[60:63], v[24:27]\n"
    "v_mfma_f32_16x16x32_bf16 v[28:31], v[220:223], v[60:63], v[28:31]\n"
    "v_mfma_f32_16x16x32_bf16 v[16:19], v[208:211], v[60:63], v[16:19]\n"
    "v_mfma_f32_16x16x32_bf16 v[20:23], v[212:215], v[60:63], v[20:23]\n"
    "s_waitcnt vmcnt(5)\n"
    "v_mfma_f32_16x16x32_bf16 v[24:27], v[232:235], v[64:67], v[24:27]\n"
    "v_mfma_f32_16x16x32_bf16 v[28:31], v[236:239], v[64:67], v[28:31]\n"
    "v_mfma_f32_16x16x32_bf16 v[16:19], v[224:227], v[64:67], v[16:19]\n"
    "v_mfma_f32_16x16x32_bf16 v[20:23], v[228:231], v[64:67], v[20:23]\n"
    "s_waitcnt vmcnt(4) lgkmcnt(15)\n"
    "v_mfma_f32_16x16x32_bf16 v[24:27], v[248:251], v[68:71], v[24:27]\n"
    "v_max3_u32 v15, v8, v9, v10\n"
    "v_mfma_f32_16x16x32_bf16 v[28:31], v[252:255], v[68:71], v[28:31]\n"
    "v_max_u32 v15, v15, v11\n"
    "v_mfma_f32_16x16x32_bf16 v[16:19], v[240:243], v[68:71], v[16:19]\n"
    "v_cmp_eq_u32 s[72:73], -1, v15\n"
    "v_mfma_f32_16x16x32_bf16 v[20:23], v[244:247], v[68:71], v[20:23]\n"
    "v_lshlrev_b32 v144, 16, v8\n"
    "s_waitcnt vmcnt(3)\n"
    "v_mfma_f32_16x16x32_bf16 v[24:27], v[136:139], v[36:39], v[24:27]\n"
    "v_and_b32 v145, 0xffff0000, v8\n"
    "s_waitcnt lgkmcnt(14)\n"
    "v_mfma_f32_16x16x32_bf16 v[28:31], v[140:143], v[36:39], v[28:31]\n"
    "v_lshlrev_b32 v146, 16, v9\n"
    "s_waitcnt lgkmcnt(13)\n"
    "v_mfma_f32_16x16x32_bf16 v[16:19], v[128:131], v[36:39], v[16:19]\n"
    "v_and_b32 v147, 0xffff0000, v9\n"
    "s_waitcnt lgkmcnt(12)\n"
    "v_mfma_f32_16x16x32_bf16 v[20:23], v[132:135], v[36:39], v[20:23]\n"
    "v_lshlrev_b32 v148, 16, v10\n"
    "s_waitcnt vmcnt(2) lgkmcnt(11)\n"
    "v_mfma_f32_16x16x32_bf16 v[24:27], v[120:123], v[40:43], v[24:27]\n"
    "v_and_b32 v149, 0xffff0000, v10\n"
    "s_waitcnt lgkmcnt(10)\n"
    "v_mfma_f32_16x16x32_bf16 v[28:31], v[124:127], v[40:43], v[28:31]\n"
    "v_lshlrev_b32 v150, 16, v11\n"
    "s_waitcnt lgkmcnt(9)\n"
    "v_mfma_f32_16x16x32_bf16 v[16:19], v[112:115], v[40:43], v[16:19]\n"
    "v_and_b32 v151, 0xffff0000, v11\n"
    "s_waitcnt lgkmcnt(8)\n"
    "v_mfma_f32_16x16x32_bf16 v[20:23], v[116:119], v[40:43], v[20:23]\n"
    "s_waitcnt vmcnt(1) lgkmcnt(7)\n"
    "v_mfma_f32_16x16x32_bf16 v[24:27], v[104:107], v[44:47], v[24:27]\n"
    "s_waitcnt lgkmcnt(6)\n"
    "v_mfma_f32_16x16x32_bf16 v[28:31], v[108:111], v[44:47], v[28:31]\n"
    "s_waitcnt lgkmcnt(5)\n"
    "v_mfma_f32_16x16x32_bf16 v[16:19], v[96:99], v[44:47], v[16:19]\n"
    "s_waitcnt lgkmcnt(4)\n"
    "v_mfma_f32_16x16x32_bf16 v[20:23], v[100:103], v[44:47], v[20:23]\n"
    "s_cmp_eq_u32 s59, 1\n"
    "s_cbranch_scc1 .Lk_e_no21\n"
    "s_waitcnt vmcnt(0) lgkmcnt(3)\n"
    "v_mfma_f32_16x16x32_bf16 v[24:27], v[88:91], v[48:51], v[24:27]\n"
    "s_waitcnt lgkmcnt(2)\n"
    "v_mfma_f32_16x16x32_bf16 v[28:31], v[92:95], v[48:51], v[28:31]\n"
    "s_waitcnt lgkmcnt(1)\n"
    "v_mfma_f32_16x16x32_bf16 v[16:19], v[80:83], v[48:51], v[16:19]\n"
    "s_waitcnt lgkmcnt(0)\n"
    "v_mfma_f32_16x16x32_bf16 v[20:23], v[84:87], v[48:51], v[20:23]\n"
    "s_branch .Lk_e_no21\n"
    ".Lk_e_loop:\n"
    "s_mov_b32 s71, s40\n"
    ".Lk_e_again:\n"
    "s_lshl_b32 s53, s43, 3\n"
    "s_and_b32 s54, s33, 7\n"
    "s_add_u32 s53, s53, s54\n"
    "s_lshl_b32 s53, s53, 1\n"
    "s_add_u32 s53, s53, s59\n"
    "s_lshl_b32 s53, s53, 10\n"
    "s_add_u32 s53, s53, 8388608\n"
    "v_and_b32 v12, 0x1ff, v7\n"
    "v_bfe_u32 v13, v0, 4, 1\n"
    "v_lshl_or_b32 v12, v13, 3, v12\n"
    "s_lshl_b32 s47, s39, 11\n"
    "s_xor_b32 s54, s40, 1\n"
    "s_mul_i32 s54, s54, 151552\n"
    "v_add_u32 v76, s47, v1\n"
    "v_xor_b32 v77, 0x1000, v76\n"
    "v_add_u32 v76, s54, v76\n"
    "v_add_u32 v77, s54, v77\n"
    "s_mov_b32 s24, s16\n"
    "s_and_b32 s25, s17, 0xffff\n"
    "s_mov_b32 s26, 0x200000\n"
    "s_mov_b32 s27, 0x20000\n"
    "s_lshl_b32 s47, s43, 7\n"
    "s_and_b32 s54, s33, 7\n"
    "s_add_u32 s47, s47, s54\n"
    "s_lshl_b32 s47, s47, 1\n"
    "s_add_u32 s47, s47, s59\n"
    "s_lshl_b32 s47, s47, 6\n"
    "v_and_b32 v14, 15, v0\n"
    "v_lshlrev_b32 v14, 10, v14\n"
    "v_lshl_or_b32 v14, v13, 5, v14\n"
    "v_lshrrev_b32 v15, 5, v0\n"
    "v_lshl_or_b32 v78, v15, 4, v14\n"
    "s_waitcnt vmcnt(7)\n"
    "v_mfma_f32_16x16x32_bf16 v[24:27], v[152:155], v[36:39], 0\n"
    "s_add_u32 s41, s41, 0x1000\n"
    "buffer_load_dwordx4 v[68:71], v1, s[20:23], s41 offen sc1\n"
    "ds_read_b128 v[152:155], v35 offset:8192\n"
    "v_mfma_f32_16x16x32_bf16 v[28:31], v[156:159], v[36:39], 0\n"
    "ds_read_b128 v[156:159], v35 offset:9216\n"
    "v_mfma_f32_16x16x32_bf16 v[16:19], v[144:147], v[36:39], 0\n"
    "ds_read_b128 v[144:147], v34 offset:8192\n"
    "v_mfma_f32_16x16x32_bf16 v[20:23], v[148:151], v[36:39], 0\n"
    "ds_read_b128 v[148:151], v34 offset:9216\n"
    "s_waitcnt vmcnt(7)\n"
    "v_mfma_f32_16x16x32_bf16 v[24:27], v[168:171], v[40:43], v[24:27]\n"
    "buffer_load_dwordx4 v[36:39], v1, s[20:23], s41 offen offset:1024 sc1\n"
    "ds_read_b128 v[168:171], v35 offset:12288\n"
    "v_mfma_f32_16x16x32_bf16 v[28:31], v[172:175], v[40:43], v[28:31]\n"
    "ds_read_b128 v[172:175], v35 offset:13312\n"
    "v_mfma_f32_16x16x32_bf16 v[16:19], v[160:163], v[40:43], v[16:19]\n"
    "ds_read_b128 v[160:163], v34 offset:12288\n"
    "v_mfma_f32_16x16x32_bf16 v[20:23], v[164:167], v[40:43], v[20:23]\n"
    "ds_read_b128 v[164:167], v34 offset:13312\n"
    "s_waitcnt vmcnt(7)\n"
    "v_mfma_f32_16x16x32_bf16 v[24:27], v[184:187], v[44:47], v[24:27]\n"
    "buffer_load_dwordx4 v[40:43], v1, s[20:23], s41 offen offset:2048 sc1\n"
    "ds_read_b128 v[184:187], v35 offset:32768\n"
    "v_mfma_f32_16x16x32_bf16 v[28:31], v[188:191], v[44:47], v[28:31]\n"
    "ds_read_b128 v[188:191], v35 offset:33792\n"
    "v_mfma_f32_16x16x32_bf16 v[16:19], v[176:179], v[44:47], v[16:19]\n"
    "ds_read_b128 v[176:179], v34 offset:32768\n"
    "v_mfma_f32_16x16x32_bf16 v[20:23], v[180:183], v[44:47], v[20:23]\n"
    "ds_read_b128 v[180:183], v34 offset:33792\n"
    "s_waitcnt vmcnt(7)\n"
    "v_mfma_f32_16x16x32_bf16 v[24:27], v[200:203], v[48:51], v[24:27]\n"
    "buffer_load_dwordx4 v[44:47], v1, s[20:23], s41 offen offset:3072 sc1\n"
    "ds_read_b128 v[200:203], v35 offset:36864\n"
    "v_mfma_f32_16x16x32_bf16 v[28:31], v[204:207], v[48:51], v[28:31]\n"
    "ds_read_b128 v[204:207], v35 offset:37888\n"
    "v_mfma_f32_16x16x32_bf16 v[16:19], v[192:195], v[48:51], v[16:19]\n"
    "ds_read_b128 v[192:195], v34 offset:36864\n"
    "v_mfma_f32_16x16x32_bf16 v[20:23], v[196:199], v[48:51], v[20:23]\n"
    "ds_read_b128 v[196:199], v34 offset:37888\n"
    "s_waitcnt vmcnt(7)\n"
    "v_mfma_f32_16x16x32_bf16 v[24:27], v[216:219], v[52:55], v[24:27]\n"
    "s_add_u32 s41, s41, 0x1000\n"
    "buffer_load_dwordx4 v[48:51], v1, s[20:23], s41 offen sc1\n"
    "ds_read_b128 v[216:219], v35 offset:40960\n"
    "v_mfma_f32_16x16x32_bf16 v[28:31], v[220:223], v[52:55], v[28:31]\n"
    "ds_read_b128 v[220:223], v35 offset:41984\n"
    "v_mfma_f32_16x16x32_bf16 v[16:19], v[208:211], v[52:55], v[16:19]\n"
    "ds_read_b128 v[208:211], v34 offset:40960\n"
    "v_mfma_f32_16x16x32_bf16 v[20:23], v[212:215], v[52:55], v[20:23]\n"
    "ds_read_b128 v[212:215], v34 offset:41984\n"
    "s_waitcnt vmcnt(7)\n"
    "v_mfma_f32_16x16x32_bf16 v[24:27], v[232:235], v[56:59], v[24:27]\n"
    "buffer_load_dwordx4 v[52:55], v1, s[20:23], s41 offen offset:1024 sc1\n"
    "ds_read_b128 v[232:235], v35 offset:45056\n"
    "v_mfma_f32_16x16x32_bf16 v[28:31], v[236:239], v[56:59], v[28:31]\n"
    "ds_read_b128 v[236:239], v35 offset:46080\n"
    "v_mfma_f32_16x16x32_bf16 v[16:19], v[224:227], v[56:59], v[16:19]\n"
    "ds_read_b128 v[224:227], v34 offset:45056\n"
    "v_mfma_f32_16x16x32_bf16 v[20:23], v[228:231], v[56:59], v[20:23]\n"
    "ds_read_b128 v[228:231], v34 offset:46080\n"
    "s_waitcnt vmcnt(7)\n"
    "v_mfma_f32_16x16x32_bf16 v[24:27], v[248:251], v[60:63], v[24:27]\n"
    "buffer_load_dwordx4 v[56:59], v1, s[20:23], s41 offen offset:2048 sc1\n"
    "ds_read_b128 v[248:251], v33\n"
    "v_mfma_f32_16x16x32_bf16 v[28:31], v[252:255], v[60:63], v[28:31]\n"
    "ds_read_b128 v[252:255], v33 offset:1024\n"
    "v_mfma_f32_16x16x32_bf16 v[16:19], v[240:243], v[60:63], v[16:19]\n"
    "buffer_load_dwordx2 v[8:9], v12, s[20:23], s53 offen sc1\n"
    "ds_read_b128 v[240:243], v32\n"
    "v_mfma_f32_16x16x32_bf16 v[20:23], v[244:247], v[60:63], v[20:23]\n"
    "ds_read_b128 v[244:247], v32 offset:1024\n"
    "s_waitcnt vmcnt(8)\n"
    "v_mfma_f32_16x16x32_bf16 v[24:27], v[136:139], v[64:67], v[24:27]\n"
    "buffer_load_dwordx4 v[60:63], v1, s[20:23], s41 offen offset:3072 sc1\n"
    "ds_read_b128 v[136:139], v33 offset:4096\n"
    "v_mfma_f32_16x16x32_bf16 v[28:31], v[140:143], v[64:67], v[28:31]\n"
    "ds_read_b128 v[140:143], v33 offset:5120\n"
    "v_mfma_f32_16x16x32_bf16 v[16:19], v[128:131], v[64:67], v[16:19]\n"
    "buffer_load_dwordx2 v[10:11], v12, s[20:23], s53 offen offset:512 sc1\n"
    "ds_read_b128 v[128:131], v32 offset:4096\n"
    "v_mfma_f32_16x16x32_bf16 v[20:23], v[132:135], v[64:67], v[20:23]\n"
    "ds_read_b128 v[132:135], v32 offset:5120\n"
    "s_waitcnt vmcnt(9)\n"
    "v_mfma_f32_16x16x32_bf16 v[24:27], v[120:123], v[68:71], v[24:27]\n"
    "s_add_u32 s41, s41, 0x1000\n"
    "buffer_load_dwordx4 v[64:67], v1, s[20:23], s41 offen sc1\n"
    "ds_read_b128 v[120:123], v33 offset:8192\n"
    "v_mfma_f32_16x16x32_bf16 v[28:31], v[124:127], v[68:71], v[28:31]\n"
    "ds_read_b128 v[124:127], v33 offset:9216\n"
    "v_mfma_f32_16x16x32_bf16 v[16:19], v[112:115], v[68:71], v[16:19]\n"
    "ds_read_b128 v[112:115], v32 offset:8192\n"
    "v_mfma_f32_16x16x32_bf16 v[20:23], v[116:119], v[68:71], v[20:23]\n"
    "ds_read_b128 v[116:119], v32 offset:9216\n"
    "s_waitcnt vmcnt(9)\n"
    "v_mfma_f32_16x16x32_bf16 v[24:27], v[104:107], v[36:39], v[24:27]\n"
    "buffer_load_dwordx4 v[68:71], v1, s[20:23], s41 offen offset:1024 sc1\n"
    "ds_read_b128 v[104:107], v33 offset:12288\n"
    "v_mfma_f32_16x16x32_bf16 v[28:31], v[108:111], v[36:39], v[28:31]\n"
    "ds_read_b128 v[108:111], v33 offset:13312\n"
    "v_mfma_f32_16x16x32_bf16 v[16:19], v[96:99], v[36:39], v[16:19]\n"
    "ds_read_b128 v[96:99], v32 offset:12288\n"
    "v_mfma_f32_16x16x32_bf16 v[20:23], v[100:103], v[36:39], v[20:23]\n"
    "ds_read_b128 v[100:103], v32 offset:13312\n"
    "s_waitcnt vmcnt(9)\n"
    "v_mfma_f32_16x16x32_bf16 v[24:27], v[88:91], v[40:43], v[24:27]\n"
    "buffer_load_dwordx4 v[36:39], v1, s[20:23], s41 offen offset:2048 sc1\n"
    "ds_read_b128 v[88:91], v3 offset:49152\n"
    "v_mfma_f32_16x16x32_bf16 v[28:31], v[92:95], v[40:43], v[28:31]\n"
    "ds_read_b128 v[92:95], v3 offset:50176\n"
    "v_mfma_f32_16x16x32_bf16 v[16:19], v[80:83], v[40:43], v[16:19]\n"
    "ds_read_b128 v[80:83], v2 offset:49152\n"
    "v_mfma_f32_16x16x32_bf16 v[20:23], v[84:87], v[40:43], v[20:23]\n"
    "ds_read_b128 v[84:87], v2 offset:50176\n"
    "s_waitcnt vmcnt(9) lgkmcnt(15)\n"
    "v_mfma_f32_16x16x32_bf16 v[24:27], v[152:155], v[44:47], v[24:27]\n"
    "buffer_load_dwordx4 v[40:43], v1, s[20:23], s41 offen offset:3072 sc1\n"
    "v_mfma_f32_16x16x32_bf16 v[28:31], v[156:159], v[44:47], v[28:31]\n"
    "v_mfma_f32_16x16x32_bf16 v[16:19], v[144:147], v[44:47], v[16:19]\n"
    "v_mfma_f32_16x16x32_bf16 v[20:23], v[148:151], v[44:47], v[20:23]\n"
    "s_waitcnt vmcnt(9)\n"
    "v_mfma_f32_16x16x32_bf16 v[24:27], v[168:171], v[48:51], v[24:27]\n"
    "s_add_u32 s41, s41, 0x1000\n"
    "buffer_load_dwordx4 v[44:47], v1, s[20:23], s41 offen sc1\n"
    "v_mfma_f32_16x16x32_bf16 v[28:31], v[172:175], v[48:51], v[28:31]\n"
    "v_mfma_f32_16x16x32_bf16 v[16:19], v[160:163], v[48:51], v[16:19]\n"
    "v_mfma_f32_16x16x32_bf16 v[20:23], v[164:167], v[48:51], v[20:23]\n"
    "s_cmp_eq_u32 s71, 1\n"
    "s_cbranch_scc0 .Lk_e_nob\n"
    "s_waitcnt lgkmcnt(0)\n"
    "s_barrier\n"
    ".Lk_e_nob:\n"
    "s_waitcnt vmcnt(9)\n"
    "v_mfma_f32_16x16x32_bf16 v[24:27], v[184:187], v[52:55], v[24:27]\n"
    "buffer_load_dwordx4 v[48:51], v1, s[20:23], s41 offen offset:1024 sc1\n"
    "v_mfma_f32_16x16x32_bf16 v[28:31], v[188:191], v[52:55], v[28:31]\n"
    "v_mfma_f32_16x16x32_bf16 v[16:19], v[176:179], v[52:55], v[16:19]\n"
    "v_mfma_f32_16x16x32_bf16 v[20:23], v[180:183], v[52:55], v[20:23]\n"
    "s_waitcnt vmcnt(9)\n"
    "v_mfma_f32_16x16x32_bf16 v[24:27], v[200:203], v[56:59], v[24:27]\n"
    "v_mfma_f32_16x16x32_bf16 v[28:31], v[204:207], v[56:59], v[28:31]\n"
    "v_mfma_f32_16x16x32_bf16 v[16:19], v[192:195], v[56:59], v[16:19]\n"
    "v_mfma_f32_16x16x32_bf16 v[20:23], v[196:199], v[56:59], v[20:23]\n"
    "s_waitcnt vmcnt(7)\n"
    "v_mfma_f32_16x16x32_bf16 v[24:27], v[216:219], v[60:63], v[24:27]\n"
    "v_mfma_f32_16x16x32_bf16 v[28:31], v[220:223], v[60:63], v[28:31]\n"
    "v_mfma_f32_16x16x32_bf16 v[16:19], v[208:211], v[60:63], v[16:19]\n"
    "v_mfma_f32_16x16x32_bf16 v[20:23], v[212:215], v[60:63], v[20:23]\n"
    "s_waitcnt vmcnt(5)\n"
    "v_mfma_f32_16x16x32_bf16 v[24:27], v[232:235], v[64:67], v[24:27]\n"
    "v_mfma_f32_16x16x32_bf16 v[28:31], v[236:239], v[64:67], v[28:31]\n"
    "v_mfma_f32_16x16x32_bf16 v[16:19], v[224:227], v[64:67], v[16:19]\n"
    "v_mfma_f32_16x16x32_bf16 v[20:23], v[228:231], v[64:67], v[20:23]\n"
    "s_waitcnt vmcnt(4)\n"
    "v_mfma_f32_16x16x32_bf16 v[24:27], v[248:251], v[68:71], v[24:27]\n"
    "v_max3_u32 v15, v8, v9, v10\n"
    "v_mfma_f32_16x16x32_bf16 v[28:31], v[252:255], v[68:71], v[28:31]\n"
    "v_max_u32 v15, v15, v11\n"
    "v_mfma_f32_16x16x32_bf16 v[16:19], v[240:243], v[68:71], v[16:19]\n"
    "v_cmp_eq_u32 s[72:73], -1, v15\n"
    "v_mfma_f32_16x16x32_bf16 v[20:23], v[244:247], v[68:71], v[20:23]\n"
    "v_lshlrev_b32 v144, 16, v8\n"
    "s_waitcnt vmcnt(3)\n"
    "v_mfma_f32_16x16x32_bf16 v[24:27], v[136:139], v[36:39], v[24:27]\n"
    "v_and_b32 v145, 0xffff0000, v8\n"
    "v_mfma_f32_16x16x32_bf16 v[28:31], v[140:143], v[36:39], v[28:31]\n"
    "v_lshlrev_b32 v146, 16, v9\n"
    "v_mfma_f32_16x16x32_bf16 v[16:19], v[128:131], v[36:39], v[16:19]\n"
    "v_and_b32 v147, 0xffff0000, v9\n"
    "v_mfma_f32_16x16x32_bf16 v[20:23], v[132:135], v[36:39], v[20:23]\n"
    "v_lshlrev_b32 v148, 16, v10\n"
    "s_waitcnt vmcnt(2)\n"
    "v_mfma_f32_16x16x32_bf16 v[24:27], v[120:123], v[40:43], v[24:27]\n"
    "v_and_b32 v149, 0xffff0000, v10\n"
    "v_mfma_f32_16x16x32_bf16 v[28:31], v[124:127], v[40:43], v[28:31]\n"
    "v_lshlrev_b32 v150, 16, v11\n"
    "v_mfma_f32_16x16x32_bf16 v[16:19], v[112:115], v[40:43], v[16:19]\n"
    "v_and_b32 v151, 0xffff0000, v11\n"
    "v_mfma_f32_16x16x32_bf16 v[20:23], v[116:119], v[40:43], v[20:23]\n"
    "s_waitcnt vmcnt(1)\n"
    "v_mfma_f32_16x16x32_bf16 v[24:27], v[104:107], v[44:47], v[24:27]\n"
    "v_mfma_f32_16x16x32_bf16 v[28:31], v[108:111], v[44:47], v[28:31]\n"
    "v_mfma_f32_16x16x32_bf16 v[16:19], v[96:99], v[44:47], v[16:19]\n"
    "v_mfma_f32_16x16x32_bf16 v[20:23], v[100:103], v[44:47], v[20:23]\n"
    "s_cmp_eq_u32 s59, 1\n"
    "s_cbranch_scc1 .Lk_e_no21\n"
    "s_waitcnt vmcnt(0)\n"
    "v_mfma_f32_16x16x32_bf16 v[24:27], v[88:91], v[48:51], v[24:27]\n"
    "v_mfma_f32_16x16x32_bf16 v[28:31], v[92:95], v[48:51], v[28:31]\n"
    "v_mfma_f32_16x16x32_bf16 v[16:19], v[80:83], v[48:51], v[16:19]\n"
    "v_mfma_f32_16x16x32_bf16 v[20:23], v[84:87], v[48:51], v[20:23]\n"
    ".Lk_e_no21:\n"
    "s_cmp_lg_u64 s[72:73], 0\n"
    "s_cbranch_scc1 .Lk_e_slow\n"
    ".Lk_e_back:\n"
    "s_nop 2\n"
    "ds_write_b128 v76, v[24:27]\n"
    "ds_write_b128 v76, v[28:31] offset:1024\n"
    "s_nop 0\n"
    "v_add_f32 v16, v16, v144\n"
    "v_add_f32 v17, v17, v145\n"
    "v_add_f32 v18, v18, v146\n"
    "v_add_f32 v19, v19, v147\n"
    "v_add_f32 v20, v20, v148\n"
    "v_add_f32 v21, v21, v149\n"
    "v_add_f32 v22, v22, v150\n"
    "v_add_f32 v23, v23, v151\n"
    "s_waitcnt lgkmcnt(0)\n"
    "s_barrier\n"
    "ds_read_b128 v[24:27], v77\n"
    "ds_read_b128 v[28:31], v77 offset:1024\n"
    "s_waitcnt lgkmcnt(1)\n"
    "v_add_f32 v16, v16, v24\n"
    "v_add_f32 v17, v17, v25\n"
    "v_add_f32 v18, v18, v26\n"
    "v_add_f32 v19, v19, v27\n"
    "s_waitcnt lgkmcnt(0)\n"
    "v_add_f32 v20, v20, v28\n"
    "v_add_f32 v21, v21, v29\n"
    "v_add_f32 v22, v22, v30\n"
    "v_add_f32 v23, v23, v31\n"
    "v_cvt_pk_bf16_f32 v8, v16, v17\n"
    "v_cvt_pk_bf16_f32 v10, v20, v21\n"
    "v_cvt_pk_bf16_f32 v9, v18, v19\n"
    "v_cvt_pk_bf16_f32 v11, v22, v23\n"
    "v_permlane16_swap_b32 v8, v10\n"
    "s_nop 0\n"
    "v_permlane16_swap_b32 v9, v11\n"
    "buffer_store_dwordx4 v[8:11], v78, s[24:27], s47 offen\n"
    "s_endpgm\n"
    ".Lk_c_slow0:\n"
    "s_mov_b32 s55, 0x4000\n"
    ".Lk_c_sl0:\n"
    "s_lshl_b32 s53, s3, 11\n"
    "s_sub_u32 s53, s41, s53\n"
    "s_sub_u32 s53, s53, 131072\n"
    "v_mov_b32 v12, 0\n"
    "buffer_load_dwordx4 v[64:67], v1, s[20:23], s53 offen sc1\n"
    "buffer_load_dwordx4 v[68:71], v1, s[20:23], s53 offen offset:1024 sc1\n"
    "buffer_load_dwordx4 v[72:75], v1, s[20:23], s53 offen offset:2048 sc1\n"
    "buffer_load_dwordx4 v[76:79], v1, s[20:23], s53 offen offset:3072 sc1\n"
    "s_add_u32 s53, s53, 0x1000\n"
    "buffer_load_dwordx4 v[80:83], v1, s[20:23], s53 offen sc1\n"
    "buffer_load_dwordx4 v[84:87], v1, s[20:23], s53 offen offset:1024 sc1\n"
    "buffer_load_dwordx4 v[88:91], v1, s[20:23], s53 offen offset:2048 sc1\n"
    "buffer_load_dwordx4 v[92:95], v1, s[20:23], s53 offen offset:3072 sc1\n"
    "s_add_u32 s53, s53, 0x1000\n"
    "buffer_load_dwordx4 v[96:99], v1, s[20:23], s53 offen sc1\n"
    "buffer_load_dwordx4 v[100:103], v1, s[20:23], s53 offen offset:1024 sc1\n"
    "buffer_load_dwordx4 v[104:107], v1, s[20:23], s53 offen offset:2048 sc1\n"
    "buffer_load_dwordx4 v[108:111], v1, s[20:23], s53 offen offset:3072 sc1\n"
    "s_add_u32 s53, s53, 0x1000\n"
    "buffer_load_dwordx4 v[112:115], v1, s[20:23], s53 offen sc1\n"
    "buffer_load_dwordx4 v[116:119], v1, s[20:23], s53 offen offset:1024 sc1\n"
    "buffer_load_dwordx4 v[120:123], v1, s[20:23], s53 offen offset:2048 sc1\n"
    "buffer_load_dwordx4 v[124:127], v1, s[20:23], s53 offen offset:3072 sc1\n"
    "s_add_u32 s53, s53, 0x1000\n"
    "s_waitcnt vmcnt(15)\n"
    "v_max3_u32 v12, v12, v64, v65\n"
    "v_max3_u32 v12, v12, v66, v67\n"
    "s_waitcnt vmcnt(14)\n"
    "v_max3_u32 v12, v12, v68, v69\n"
    "v_max3_u32 v12, v12, v70, v71\n"
    "s_waitcnt vmcnt(13)\n"
    "v_max3_u32 v12, v12, v72, v73\n"
    "v_max3_u32 v12, v12, v74, v75\n"
    "s_waitcnt vmcnt(12)\n"
    "v_max3_u32 v12, v12, v76, v77\n"
    "v_max3_u32 v12, v12, v78, v79\n"
    "s_waitcnt vmcnt(11)\n"
    "v_max3_u32 v12, v12, v80, v81\n"
    "v_max3_u32 v12, v12, v82, v83\n"
    "s_waitcnt vmcnt(10)\n"
    "v_max3_u32 v12, v12, v84, v85\n"
    "v_max3_u32 v12, v12, v86, v87\n"
    "s_waitcnt vmcnt(9)\n"
    "v_max3_u32 v12, v12, v88, v89\n"
    "v_max3_u32 v12, v12, v90, v91\n"
    "s_waitcnt vmcnt(8)\n"
    "v_max3_u32 v12, v12, v92, v93\n"
    "v_max3_u32 v12, v12, v94, v95\n"
    "s_waitcnt vmcnt(7)\n"
    "v_max3_u32 v12, v12, v96, v97\n"
    "v_max3_u32 v12, v12, v98, v99\n"
    "s_waitcnt vmcnt(6)\n"
    "v_max3_u32 v12, v12, v100, v101\n"
    "v_max3_u32 v12, v12, v102, v103\n"
    "s_waitcnt vmcnt(5)\n"
    "v_max3_u32 v12, v12, v104, v105\n"
    "v_max3_u32 v12, v12, v106, v107\n"
    "s_waitcnt vmcnt(4)\n"
    "v_max3_u32 v12, v12, v108, v109\n"
    "v_max3_u32 v12, v12, v110, v111\n"
    "s_waitcnt vmcnt(3)\n"
    "v_max3_u32 v12, v12, v112, v113\n"
    "v_max3_u32 v12, v12, v114, v115\n"
    "s_waitcnt vmcnt(2)\n"
    "v_max3_u32 v12, v12, v116, v117\n"
    "v_max3_u32 v12, v12, v118, v119\n"
    "s_waitcnt vmcnt(1)\n"
    "v_max3_u32 v12, v12, v120, v121\n"
    "v_max3_u32 v12, v12, v122, v123\n"
    "s_waitcnt vmcnt(0)\n"
    "v_max3_u32 v12, v12, v124, v125\n"
    "v_max3_u32 v12, v12, v126, v127\n"
    "s_sub_u32 s55, s55, 1\n"
    "v_cmp_eq_u32 vcc, -1, v12\n"
    "s_cbranch_scc1 .Lk_c_sd0\n"
    "s_cbranch_vccnz .Lk_c_sl0\n"
    ".Lk_c_sd0:\n"
    "v_mfma_f32_16x16x32_bf16 v[44:47], v[192:195], v[64:67], v[24:27]\n"
    "v_mfma_f32_16x16x32_bf16 v[44:47], v[196:199], v[68:71], v[44:47]\n"
    "v_mfma_f32_16x16x32_bf16 v[44:47], v[200:203], v[72:75], v[44:47]\n"
    "v_mfma_f32_16x16x32_bf16 v[44:47], v[204:207], v[76:79], v[44:47]\n"
    "v_mfma_f32_16x16x32_bf16 v[44:47], v[208:211], v[80:83], v[44:47]\n"
    "v_mfma_f32_16x16x32_bf16 v[44:47], v[212:215], v[84:87], v[44:47]\n"
    "v_mfma_f32_16x16x32_bf16 v[44:47], v[216:219], v[88:91], v[44:47]\n"
    "v_mfma_f32_16x16x32_bf16 v[44:47], v[220:223], v[92:95], v[44:47]\n"
    "v_mfma_f32_16x16x32_bf16 v[44:47], v[224:227], v[96:99], v[44:47]\n"
    "v_mfma_f32_16x16x32_bf16 v[44:47], v[228:231], v[100:103], v[44:47]\n"
    "v_mfma_f32_16x16x32_bf16 v[44:47], v[232:235], v[104:107], v[44:47]\n"
    "v_mfma_f32_16x16x32_bf16 v[44:47], v[236:239], v[108:111], v[44:47]\n"
    "v_mfma_f32_16x16x32_bf16 v[44:47], v[240:243], v[112:115], v[44:47]\n"
    "v_mfma_f32_16x16x32_bf16 v[44:47], v[244:247], v[116:119], v[44:47]\n"
    "v_mfma_f32_16x16x32_bf16 v[44:47], v[248:251], v[120:123], v[44:47]\n"
    "v_mfma_f32_16x16x32_bf16 v[44:47], v[252:255], v[124:127], v[44:47]\n"
    "s_branch .Lk_c_back0\n"
    ".Lk_c_slow1:\n"
    "s_mov_b32 s55, 0x4000\n"
    ".Lk_c_sl1:\n"
    "s_lshl_b32 s53, s3, 11\n"
    "s_sub_u32 s53, s41, s53\n"
    "s_sub_u32 s53, s53, 0\n"
    "v_mov_b32 v12, 0\n"
    "buffer_load_dwordx4 v[64:67], v1, s[20:23], s53 offen sc1\n"
    "buffer_load_dwordx4 v[68:71], v1, s[20:23], s53 offen offset:1024 sc1\n"
    "buffer_load_dwordx4 v[72:75], v1, s[20:23], s53 offen offset:2048 sc1\n"
    "buffer_load_dwordx4 v[76:79], v1, s[20:23], s53 offen offset:3072 sc1\n"
    "s_add_u32 s53, s53, 0x1000\n"
    "buffer_load_dwordx4 v[80:83], v1, s[20:23], s53 offen sc1\n"
    "buffer_load_dwordx4 v[84:87], v1, s[20:23], s53 offen offset:1024 sc1\n"
    "buffer_load_dwordx4 v[88:91], v1, s[20:23], s53 offen offset:2048 sc1\n"
    "buffer_load_dwordx4 v[92:95], v1, s[20:23], s53 offen offset:3072 sc1\n"
    "s_add_u32 s53, s53, 0x1000\n"
    "buffer_load_dwordx4 v[96:99], v1, s[20:23], s53 offen sc1\n"
    "buffer_load_dwordx4 v[100:103], v1, s[20:23], s53 offen offset:1024 sc1\n"
    "buffer_load_dwordx4 v[104:107], v1, s[20:23], s53 offen offset:2048 sc1\n"
    "buffer_load_dwordx4 v[108:111], v1, s[20:23], s53 offen offset:3072 sc1\n"
    "s_add_u32 s53, s53, 0x1000\n"
    "buffer_load_dwordx4 v[112:115], v1, s[20:23], s53 offen sc1\n"
    "buffer_load_dwordx4 v[116:119], v1, s[20:23], s53 offen offset:1024 sc1\n"
    "buffer_load_dwordx4 v[120:123], v1, s[20:23], s53 offen offset:2048 sc1\n"
    "buffer_load_dwordx4 v[124:127], v1, s[20:23], s53 offen offset:3072 sc1\n"
    "s_add_u32 s53, s53, 0x1000\n"
    "s_waitcnt vmcnt(15)\n"
    "v_max3_u32 v12, v12, v64, v65\n"
    "v_max3_u32 v12, v12, v66, v67\n"
    "s_waitcnt vmcnt(14)\n"
    "v_max3_u32 v12, v12, v68, v69\n"
    "v_max3_u32 v12, v12, v70, v71\n"
    "s_waitcnt vmcnt(13)\n"
    "v_max3_u32 v12, v12, v72, v73\n"
    "v_max3_u32 v12, v12, v74, v75\n"
    "s_waitcnt vmcnt(12)\n"
    "v_max3_u32 v12, v12, v76, v77\n"
    "v_max3_u32 v12, v12, v78, v79\n"
    "s_waitcnt vmcnt(11)\n"
    "v_max3_u32 v12, v12, v80, v81\n"
    "v_max3_u32 v12, v12, v82, v83\n"
    "s_waitcnt vmcnt(10)\n"
    "v_max3_u32 v12, v12, v84, v85\n"
    "v_max3_u32 v12, v12, v86, v87\n"
    "s_waitcnt vmcnt(9)\n"
    "v_max3_u32 v12, v12, v88, v89\n"
    "v_max3_u32 v12, v12, v90, v91\n"
    "s_waitcnt vmcnt(8)\n"
    "v_max3_u32 v12, v12, v92, v93\n"
    "v_max3_u32 v12, v12, v94, v95\n"
    "s_waitcnt vmcnt(7)\n"
    "v_max3_u32 v12, v12, v96, v97\n"
    "v_max3_u32 v12, v12, v98, v99\n"
    "s_waitcnt vmcnt(6)\n"
    "v_max3_u32 v12, v12, v100, v101\n"
    "v_max3_u32 v12, v12, v102, v103\n"
    "s_waitcnt vmcnt(5)\n"
    "v_max3_u32 v12, v12, v104, v105\n"
    "v_max3_u32 v12, v12, v106, v107\n"
    "s_waitcnt vmcnt(4)\n"
    "v_max3_u32 v12, v12, v108, v109\n"
    "v_max3_u32 v12, v12, v110, v111\n"
    "s_waitcnt vmcnt(3)\n"
    "v_max3_u32 v12, v12, v112, v113\n"
    "v_max3_u32 v12, v12, v114, v115\n"
    "s_waitcnt vmcnt(2)\n"
    "v_max3_u32 v12, v12, v116, v117\n"
    "v_max3_u32 v12, v12, v118, v119\n"
    "s_waitcnt vmcnt(1)\n"
    "v_max3_u32 v12, v12, v120, v121\n"
    "v_max3_u32 v12, v12, v122, v123\n"
    "s_waitcnt vmcnt(0)\n"
    "v_max3_u32 v12, v12, v124, v125\n"
    "v_max3_u32 v12, v12, v126, v127\n"
    "s_sub_u32 s55, s55, 1\n"
    "v_cmp_eq_u32 vcc, -1, v12\n"
    "s_cbranch_scc1 .Lk_c_sd1\n"
    "s_cbranch_vccnz .Lk_c_sl1\n"
    ".Lk_c_sd1:\n"
    "v_mfma_f32_16x16x32_bf16 v[48:51], v[192:195], v[64:67], v[28:31]\n"
    "v_mfma_f32_16x16x32_bf16 v[48:51], v[196:199], v[68:71], v[48:51]\n"
    "v_mfma_f32_16x16x32_bf16 v[48:51], v[200:203], v[72:75], v[48:51]\n"
    "v_mfma_f32_16x16x32_bf16 v[48:51], v[204:207], v[76:79], v[48:51]\n"
    "v_mfma_f32_16x16x32_bf16 v[48:51], v[208:211], v[80:83], v[48:51]\n"
    "v_mfma_f32_16x16x32_bf16 v[48:51], v[212:215], v[84:87], v[48:51]\n"
    "v_mfma_f32_16x16x32_bf16 v[48:51], v[216:219], v[88:91], v[48:51]\n"
    "v_mfma_f32_16x16x32_bf16 v[48:51], v[220:223], v[92:95], v[48:51]\n"
    "v_mfma_f32_16x16x32_bf16 v[48:51], v[224:227], v[96:99], v[48:51]\n"
    "v_mfma_f32_16x16x32_bf16 v[48:51], v[228:231], v[100:103], v[48:51]\n"
    "v_mfma_f32_16x16x32_bf16 v[48:51], v[232:235], v[104:107], v[48:51]\n"
    "v_mfma_f32_16x16x32_bf16 v[48:51], v[236:239], v[108:111], v[48:51]\n"
    "v_mfma_f32_16x16x32_bf16 v[48:51], v[240:243], v[112:115], v[48:51]\n"
    "v_mfma_f32_16x16x32_bf16 v[48:51], v[244:247], v[116:119], v[48:51]\n"
    "v_mfma_f32_16x16x32_bf16 v[48:51], v[248:251], v[120:123], v[48:51]\n"
    "v_mfma_f32_16x16x32_bf16 v[48:51], v[252:255], v[124:127], v[48:51]\n"
    "s_branch .Lk_c_back1\n"
    ".Lk_d_slow0:\n"
    "s_cmp_eq_u32 s71, 1\n"
    "s_cbranch_scc1 .Lk_d_back0\n"
    "s_mov_b32 s71, 1\n"
    "s_waitcnt vmcnt(0) lgkmcnt(0)\n"
    "s_lshl_b32 s67, s66, 15\n"
    "s_add_u32 s67, s67, 8388608\n"
    "s_add_u32 s68, s67, 0x4000\n"
    "s_mov_b32 s55, 0x4000\n"
    ".Lk_d_sc0a:\n"
    "s_mov_b32 s50, s67\n"
    "s_mov_b32 s51, 32\n"
    "v_mov_b32 v15, 0\n"
    ".Lk_d_sc0b:\n"
    "buffer_load_dwordx4 v[16:19], v1, s[20:23], s50 offen sc1\n"
    "s_waitcnt vmcnt(0)\n"
    "v_max3_u32 v15, v15, v16, v17\n"
    "v_max3_u32 v15, v15, v18, v19\n"
    "s_add_u32 s50, s50, 0x400\n"
    "s_sub_u32 s51, s51, 1\n"
    "s_cmp_lg_u32 s51, 0\n"
    "s_cbranch_scc1 .Lk_d_sc0b\n"
    "s_sub_u32 s55, s55, 1\n"
    "v_cmp_eq_u32 vcc, -1, v15\n"
    "s_cbranch_scc1 .Lk_d_sc0c\n"
    "s_cbranch_vccnz .Lk_d_sc0a\n"
    ".Lk_d_sc0c:\n"
    "ds_read_b128 v[96:99], v1\n"
    "ds_read_b128 v[100:103], v1 offset:16384\n"
    "ds_read_b128 v[104:107], v1 offset:32768\n"
    "ds_read_b128 v[108:111], v1 offset:49152\n"
    "ds_read_b128 v[112:115], v2\n"
    "buffer_load_dwordx4 v[64:67], v1, s[20:23], s67 offen sc1\n"
    "buffer_load_dwordx4 v[68:71], v1, s[20:23], s68 offen sc1\n"
    "buffer_load_dwordx4 v[72:75], v1, s[20:23], s67 offen offset:1024 sc1\n"
    "buffer_load_dwordx4 v[76:79], v1, s[20:23], s68 offen offset:1024 sc1\n"
    "buffer_load_dwordx4 v[80:83], v1, s[20:23], s67 offen offset:2048 sc1\n"
    "buffer_load_dwordx4 v[84:87], v1, s[20:23], s68 offen offset:2048 sc1\n"
    "buffer_load_dwordx4 v[88:91], v1, s[20:23], s67 offen offset:3072 sc1\n"
    "buffer_load_dwordx4 v[92:95], v1, s[20:23], s68 offen offset:3072 sc1\n"
    "s_add_u32 s67, s67, 0x1000\n"
    "buffer_load_dwordx4 v[192:195], v1, s[20:23], s67 offen sc1\n"
    "s_add_u32 s68, s68, 0x1000\n"
    "buffer_load_dwordx4 v[196:199], v1, s[20:23], s68 offen sc1\n"
    "s_branch .Lk_d_loop0\n"
    ".Lk_d_slow1:\n"
    "s_cmp_eq_u32 s71, 1\n"
    "s_cbranch_scc1 .Lk_d_back1\n"
    "s_mov_b32 s71, 1\n"
    "s_waitcnt vmcnt(0) lgkmcnt(0)\n"
    "s_lshl_b32 s67, s66, 15\n"
    "s_add_u32 s67, s67, 8388608\n"
    "s_add_u32 s68, s67, 0x4000\n"
    "s_mov_b32 s55, 0x4000\n"
    ".Lk_d_sc1a:\n"
    "s_mov_b32 s50, s67\n"
    "s_mov_b32 s51, 32\n"
    "v_mov_b32 v15, 0\n"
    ".Lk_d_sc1b:\n"
    "buffer_load_dwordx4 v[16:19], v1, s[20:23], s50 offen sc1\n"
    "s_waitcnt vmcnt(0)\n"
    "v_max3_u32 v15, v15, v16, v17\n"
    "v_max3_u32 v15, v15, v18, v19\n"
    "s_add_u32 s50, s50, 0x400\n"
    "s_sub_u32 s51, s51, 1\n"
    "s_cmp_lg_u32 s51, 0\n"
    "s_cbranch_scc1 .Lk_d_sc1b\n"
    "s_sub_u32 s55, s55, 1\n"
    "v_cmp_eq_u32 vcc, -1, v15\n"
    "s_cbranch_scc1 .Lk_d_sc1c\n"
    "s_cbranch_vccnz .Lk_d_sc1a\n"
    ".Lk_d_sc1c:\n"
    "ds_read_b128 v[96:99], v1\n"
    "ds_read_b128 v[100:103], v1 offset:16384\n"
    "ds_read_b128 v[104:107], v1 offset:32768\n"
    "ds_read_b128 v[108:111], v1 offset:49152\n"
    "ds_read_b128 v[112:115], v2\n"
    "buffer_load_dwordx4 v[64:67], v1, s[20:23], s67 offen sc1\n"
    "buffer_load_dwordx4 v[68:71], v1, s[20:23], s68 offen sc1\n"
    "buffer_load_dwordx4 v[72:75], v1, s[20:23], s67 offen offset:1024 sc1\n"
    "buffer_load_dwordx4 v[76:79], v1, s[20:23], s68 offen offset:1024 sc1\n"
    "buffer_load_dwordx4 v[80:83], v1, s[20:23], s67 offen offset:2048 sc1\n"
    "buffer_load_dwordx4 v[84:87], v1, s[20:23], s68 offen offset:2048 sc1\n"
    "buffer_load_dwordx4 v[88:91], v1, s[20:23], s67 offen offset:3072 sc1\n"
    "buffer_load_dwordx4 v[92:95], v1, s[20:23], s68 offen offset:3072 sc1\n"
    "s_add_u32 s67, s67, 0x1000\n"
    "buffer_load_dwordx4 v[192:195], v1, s[20:23], s67 offen sc1\n"
    "s_add_u32 s68, s68, 0x1000\n"
    "buffer_load_dwordx4 v[196:199], v1, s[20:23], s68 offen sc1\n"
    "s_branch .Lk_d_loop1\n"
    ".Lk_e_slow:\n"
    "s_mov_b32 s55, 0x8000\n"
    ".Lk_e_shl:\n"
    "buffer_load_dwordx2 v[8:9], v12, s[20:23], s53 offen sc1\n"
    "buffer_load_dwordx2 v[10:11], v12, s[20:23], s53 offen offset:512 sc1\n"
    "s_waitcnt vmcnt(0)\n"
    "v_max3_u32 v15, v8, v9, v10\n"
    "v_max_u32 v15, v15, v11\n"
    "s_sub_u32 s55, s55, 1\n"
    "v_cmp_eq_u32 vcc, -1, v15\n"
    "s_cbranch_scc1 .Lk_e_shd\n"
    "s_cbranch_vccnz .Lk_e_shl\n"
    ".Lk_e_shd:\n"
    "v_lshlrev_b32 v144, 16, v8\n"
    "v_and_b32 v145, 0xffff0000, v8\n"
    "v_lshlrev_b32 v146, 16, v9\n"
    "v_and_b32 v147, 0xffff0000, v9\n"
    "v_lshlrev_b32 v148, 16, v10\n"
    "v_and_b32 v149, 0xffff0000, v10\n"
    "v_lshlrev_b32 v150, 16, v11\n"
    "v_and_b32 v151, 0xffff0000, v11\n"
    "s_branch .Lk_e_back\n"
    ".Lk_b_fb:\n"
    "s_cmp_eq_u32 s46, 1\n"
    "s_cbranch_scc1 .Lk_b_ok\n"
    "v_mov_b32 v128, 0\n"
    "v_mov_b32 v129, 0\n"
    "v_mov_b32 v130, 0\n"
    "v_mov_b32 v131, 0\n"
    "v_mov_b32 v132, 0\n"
    "v_mov_b32 v133, 0\n"
    "v_mov_b32 v134, 0\n"
    "v_mov_b32 v135, 0\n"
    "v_mov_b32 v136, 0\n"
    "v_mov_b32 v137, 0\n"
    "v_mov_b32 v138, 0\n"
    "v_mov_b32 v139, 0\n"
    "v_mov_b32 v140, 0\n"
    "v_mov_b32 v141, 0\n"
    "v_mov_b32 v142, 0\n"
    "v_mov_b32 v143, 0\n"
    "v_mov_b32 v144, 0\n"
    "v_mov_b32 v145, 0\n"
    "v_mov_b32 v146, 0\n"
    "v_mov_b32 v147, 0\n"
    "v_mov_b32 v148, 0\n"
    "v_mov_b32 v149, 0\n"
    "v_mov_b32 v150, 0\n"
    "v_mov_b32 v151, 0\n"
    "v_mov_b32 v152, 0\n"
    "v_mov_b32 v153, 0\n"
    "v_mov_b32 v154, 0\n"
    "v_mov_b32 v155, 0\n"
    "v_mov_b32 v156, 0\n"
    "v_mov_b32 v157, 0\n"
    "v_mov_b32 v158, 0\n"
    "v_mov_b32 v159, 0\n"
    "s_mov_b32 s26, 0\n"
    "s_mov_b32 s30, 0\n"
    "v_mov_b32 v27, 0x7f000000\n"
    "v_mov_b32 v29, 0x7f000000\n"
    "v_mov_b32 v28, 0x7f000000\n"
    "v_mov_b32 v26, 0x100000\n"
    "s_mov_b32 s46, 1\n"
    "s_mov_b32 s43, s58\n"
    "s_mov_b32 s55, 32\n"
    "v_mov_b32 v5, 0xff7fffff\n"
    "v_mov_b32 v6, 0xff7fffff\n"
    "s_mov_b32 s54, 0x8000\n"
    ".Lk_fbl:\n"
    "buffer_load_dwordx4 v[56:59], v1, s[20:23], s43 offen sc1\n"
    "buffer_load_dwordx4 v[60:63], v1, s[20:23], s43 offen offset:1024 sc1\n"
    "s_waitcnt vmcnt(1)\n"
    "v_max3_u32 v12, v56, v57, v58\n"
    "s_waitcnt vmcnt(0)\n"
    "v_max3_u32 v12, v12, v59, v60\n"
    "v_max3_u32 v12, v12, v61, v62\n"
    "v_max_u32 v12, v12, v63\n"
    "v_cmp_eq_u32 vcc, -1, v12\n"
    "s_cbranch_vccz .Lk_fbk\n"
    "s_sub_u32 s54, s54, 1\n"
    "s_cbranch_scc0 .Lk_fbl\n"
    "s_mov_b32 s54, 0\n"
    ".Lk_fbk:\n"
    "v_mfma_f32_32x32x16_bf16 v[96:111], v[56:59], v[88:91], 0\n"
    "v_mfma_f32_32x32x16_bf16 v[112:127], v[56:59], v[92:95], 0\n"
    "s_nop 10\n"
    "v_max3_f32 v5, v96, v97, v5\n"
    "v_max3_f32 v5, v5, v98, v99\n"
    "v_max3_f32 v5, v5, v100, v101\n"
    "v_max3_f32 v5, v5, v102, v103\n"
    "v_max3_f32 v5, v5, v104, v105\n"
    "v_max3_f32 v5, v5, v106, v107\n"
    "v_max3_f32 v5, v5, v108, v109\n"
    "v_max3_f32 v5, v5, v110, v111\n"
    "v_max3_f32 v6, v112, v113, v6\n"
    "v_max3_f32 v6, v6, v114, v115\n"
    "v_max3_f32 v6, v6, v116, v117\n"
    "v_max3_f32 v6, v6, v118, v119\n"
    "v_max3_f32 v6, v6, v120, v121\n"
    "v_max3_f32 v6, v6, v122, v123\n"
    "v_max3_f32 v6, v6, v124, v125\n"
    "v_max3_f32 v6, v6, v126, v127\n"
    "s_add_u32 s43, s43, 0x800\n"
    "s_sub_u32 s55, s55, 1\n"
    "s_cmp_lg_u32 s55, 0\n"
    "s_cbranch_scc1 .Lk_fbl\n"
    "v_mov_b32 v12, v5\n"
    "s_nop 1\n"
    "v_permlane32_swap_b32 v5, v12\n"
    "v_max_f32 v5, v5, v12\n"
    "v_mov_b32 v13, v6\n"
    "s_nop 1\n"
    "v_permlane32_swap_b32 v6, v13\n"
    "v_max_f32 v6, v6, v13\n"
    "s_mov_b32 s43, s58\n"
    "s_waitcnt vmcnt(0)\n"
    "s_branch .Lk_b_try\n"
    ".Lk_end:\n"
    : "+{s[0:1]}"(karg), "+{v0}"(tid)
    : "{s2}"(blk)
    : "v1", "v2", "v3", "v4", "v5", "v6", "v7", "v8", "v9", "v10", "v11", "v12", "v13", "v14", "v15", "v16", "v17", "v18", "v19", "v20", "v21", "v22", "v23", "v24", "v25", "v26", "v27", "v28", "v29", "v30", "v31", "v32", "v33", "v34", "v35", "v36", "v37", "v38", "v39", "v40", "v41", "v42", "v43", "v44", "v45", "v46", "v47", "v48", "v49", "v50", "v51", "v52", "v53", "v54", "v55", "v56", "v57", "v58", "v59", "v60", "v61", "v62", "v63", "v64", "v65", "v66", "v67", "v68", "v69", "v70", "v71", "v72", "v73", "v74", "v75", "v76", "v77", "v78", "v79", "v80", "v81", "v82", "v83", "v84", "v85", "v86", "v87", "v88", "v89", "v90", "v91", "v92", "v93", "v94", "v95", "v96", "v97", "v98", "v99", "v100", "v101", "v102", "v103", "v104", "v105", "v106", "v107", "v108", "v109", "v110", "v111", "v112", "v113", "v114", "v115", "v116", "v117", "v118", "v119", "v120", "v121", "v122", "v123", "v124", "v125", "v126", "v127", "v128", "v129", "v130", "v131", "v132", "v133", "v134", "v135", "v136", "v137", "v138", "v139", "v140", "v141", "v142", "v143", "v144", "v145", "v146", "v147", "v148", "v149", "v150", "v151", "v152", "v153", "v154", "v155", "v156", "v157", "v158", "v159", "v160", "v161", "v162", "v163", "v164", "v165", "v166", "v167", "v168", "v169", "v170", "v171", "v172", "v173", "v174", "v175", "v176", "v177", "v178", "v179", "v180", "v181", "v182", "v183", "v184", "v185", "v186", "v187", "v188", "v189", "v190", "v191", "v192", "v193", "v194", "v195", "v196", "v197", "v198", "v199", "v200", "v201", "v202", "v203", "v204", "v205", "v206", "v207", "v208", "v209", "v210", "v211", "v212", "v213", "v214", "v215", "v216", "v217", "v218", "v219", "v220", "v221", "v222", "v223", "v224", "v225", "v226", "v227", "v228", "v229", "v230", "v231", "v232", "v233", "v234", "v235", "v236", "v237", "v238", "v239", "v240", "v241", "v242", "v243", "v244", "v245", "v246", "v247", "v248", "v249", "v250", "v251", "v252", "v253", "v254", "v255", "s3", "s4", "s5", "s6", "s7", "s8", "s9", "s10", "s11", "s12", "s13", "s14", "s15", "s16", "s17", "s18", "s19", "s20", "s21", "s22", "s23", "s24", "s25", "s26", "s27", "s28", "s29", "s30", "s31", "s33", "s34", "s35", "s36", "s37", "s38", "s39", "s40", "s41", "s42", "s43", "s44", "s45", "s46", "s47", "s48", "s49", "s50", "s51", "s52", "s53", "s54", "s55", "s56", "s57", "s58", "s59", "s60", "s61", "s62", "s63", "s64", "s65", "s66", "s67", "s68", "s69", "s70", "s71", "s72", "s73", "s74", "s75", "s76", "vcc", "scc", "memory");
}

extern "C" void kernel_launch(void* const* d_in, const int* in_sizes, int n_in, void* d_out, int out_size, void* d_ws, size_t ws_size, hipStream_t stream) {
  block_fwd<<<256, 512, 0, stream>>>(d_in[0], d_in[1], d_in[2], d_in[4], d_in[5], d_in[6], d_out, d_ws);
}
```
